```python
import torch

def _softmax(x, axis=-1):
    return torch.softmax(x, dim=axis)
def _gelu(x):
    return torch.nn.functional.gelu(x, approximate="tanh")
def _silu(x):
    return torch.nn.functional.silu(x)
def _sigmoid(x):
    return torch.sigmoid(x)
def _layernorm(x, axis=-1, eps=1e-5):
    m = x.mean(dim=axis, keepdim=True)
    v = x.var(dim=axis, keepdim=True, unbiased=False)
    return (x - m) / torch.sqrt(v + eps)
def _scatter_add(tgt, idx, src, axis=0):
    return tgt.clone().index_add_(axis, idx, src)
def _segment_sum(src, seg, n):
    out = torch.zeros((n,) + src.shape[1:], dtype=src.dtype)
    return out.index_add_(0, seg, src)
def _pad(x, pw):
    flat = tuple(v for pair in reversed(pw) for v in pair)
    return torch.nn.functional.pad(x, flat)

kernel_name = 'sequential_preLN__spectral_fnet__dense_activation_relu__prompt'

def setup_inputs():

    _w = (lambda a: (lambda t: t.to(torch.bfloat16) if t.is_floating_point() else t)(torch.as_tensor(a)))
    def _A_setup():
        B, S, D = 1, 2048, 512
        ns = torch.arange(S)[:, None] * torch.arange(S)[None, :]
        nd = torch.arange(D)[:, None] * torch.arange(D)[None, :]
        Ws_re = torch.cos(-2.0 * torch.pi * ns / S).to(float)
        Ws_im = torch.sin(-2.0 * torch.pi * ns / S).to(float)
        Wd_re = torch.cos(-2.0 * torch.pi * nd / D).to(float)
        Wd_im = torch.sin(-2.0 * torch.pi * nd / D).to(float)
        return {
            "x": _w((lambda *s: torch.randn(*s, dtype=torch.bfloat16) * 0.05)(B, S, D)),
            "dft_seq_re": _w(Ws_re),
            "dft_seq_im": _w(Ws_im),
            "dft_hid_re": _w(Wd_re),
            "dft_hid_im": _w(Wd_im),
        }
    _A_in = _A_setup()

    def _B_setup():
        B, S, D, Dff = 1, 2048, 512, 1792
        return {
            "x": _w((lambda *s: torch.randn(*s, dtype=torch.bfloat16) * 0.05)(B, S, D)),
            "w1": _w((lambda *s: torch.randn(*s, dtype=torch.bfloat16) * 0.4)(D, Dff)),
            "w2": _w((lambda *s: torch.randn(*s, dtype=torch.bfloat16) * 0.4)(Dff, D)),
        }
    _B_in = _B_setup()

    return {
        "A_x": _A_in["x"],
        "A_dft_seq_re": _A_in["dft_seq_re"],
        "A_dft_seq_im": _A_in["dft_seq_im"],
        "A_dft_hid_re": _A_in["dft_hid_re"],
        "A_dft_hid_im": _A_in["dft_hid_im"],
        "B_x": _B_in["x"],
        "B_w1": _B_in["w1"],
        "B_w2": _B_in["w2"],
    }

def reference(A_x, A_dft_seq_re, A_dft_seq_im, A_dft_hid_re, A_dft_hid_im, B_x, B_w1, B_w2):
    def _A_ref(x, dft_seq_re, dft_seq_im, dft_hid_re, dft_hid_im):
        a = torch.einsum("bsd,ed->bse", x, dft_hid_re)
        b = torch.einsum("bsd,ed->bse", x, dft_hid_im)
        y = torch.einsum("ts,bse->bte", dft_seq_re, a) - torch.einsum("ts,bse->bte", dft_seq_im, b)
        return y

    def _B_ref(x, w1, w2):
        h = torch.einsum("bsd,df->bsf", x, w1)
        zero = torch.zeros_like(h)
        h = torch.maximum(h, zero)
        return torch.einsum("bsf,fd->bsd", h, w2)

    x = A_x
    h = x + _A_ref(_layernorm(x, axis=-1), A_dft_seq_re, A_dft_seq_im, A_dft_hid_re, A_dft_hid_im)
    h2 = h + _B_ref(_layernorm(h, axis=-1), B_w1, B_w2)
    return h2
```

```cpp
#include <hip/hip_runtime.h>
#include <hip/hip_bf16.h>
#include <stdint.h>

typedef unsigned v4u __attribute__((ext_vector_type(4)));
typedef unsigned v2u __attribute__((ext_vector_type(2)));
typedef float f32x4 __attribute__((ext_vector_type(4)));
typedef float f32x2 __attribute__((ext_vector_type(2)));
typedef short bf16x8 __attribute__((ext_vector_type(8)));
typedef __bf16 bf2_t __attribute__((ext_vector_type(2)));
#define GAS __attribute__((address_space(1)))
#define LAS __attribute__((address_space(3)))

constexpr int S_ = 2048, D_ = 512, F_ = 1792;
constexpr unsigned CTRL_OFF = 0;
constexpr unsigned XNT_OFF = 65536;
constexpr unsigned PQ_OFF  = XNT_OFF + (2u << 20);
constexpr unsigned WD_OFF  = PQ_OFF + (4u << 20);
constexpr unsigned H_OFF   = WD_OFF + (1u << 20);
constexpr unsigned W1_OFF  = H_OFF + (2u << 20);
constexpr unsigned FF_OFF  = W1_OFF + 1835008u;
constexpr unsigned W2_OFF  = FF_OFF + 7340032u;
constexpr unsigned CS_OFF  = W2_OFF + 1835008u;
constexpr unsigned ST_OFF  = CS_OFF + 16 * F_ * 4;
constexpr unsigned WS_END  = ST_OFF + S_ * 8 * 8;

__device__ __forceinline__ unsigned pk2(float a, float b) {
  f32x2 v = {a, b};
  return __builtin_bit_cast(unsigned, __builtin_convertvector(v, bf2_t));
}
__device__ __forceinline__ float bflo(unsigned u) { return __builtin_bit_cast(float, u << 16); }
__device__ __forceinline__ float bfhi(unsigned u) { return __builtin_bit_cast(float, u & 0xffff0000u); }
__device__ __forceinline__ float rbf(float x) { return (float)(__bf16)x; }
__device__ __forceinline__ f32x4 mfma(v4u a, v4u b, f32x4 c) {
  return __builtin_amdgcn_mfma_f32_16x16x32_bf16(__builtin_bit_cast(bf16x8, a), __builtin_bit_cast(bf16x8, b), c, 0, 0, 0);
}
__device__ __forceinline__ float wave_sum(float v) {
#pragma unroll
  for (int o = 32; o >= 1; o >>= 1) v += __shfl_xor(v, o);
  return v;
}
#define SCHED() __builtin_amdgcn_sched_barrier(0)
#define WAITV(n) asm volatile("s_waitcnt vmcnt(%0)" ::"i"(n) : "memory")

__global__ __launch_bounds__(256) void k0_prep(const unsigned short* __restrict__ x, const unsigned short* __restrict__ wre,
                                               const unsigned short* __restrict__ wim, const unsigned short* __restrict__ w1,
                                               const unsigned short* __restrict__ w2, unsigned char* __restrict__ ws) {
  __shared__ __attribute__((aligned(16))) unsigned char sm[8192 + 4 * 112 * 4];
  const int blk = blockIdx.x, tid = threadIdx.x, lane = tid & 63, w = tid >> 6;
  v4u xr[2];
#pragma unroll
  for (int i = 0; i < 2; ++i) xr[i] = *(const v4u*)(x + (size_t)(8 * blk + 2 * w + i) * D_ + lane * 8);
  v4u wdp;
  {
    const int p = blk * 256 + tid, tile = (p >> 6) & 3, kb = (p >> 8) & 31, eg = p >> 13, q = lane >> 4, r = lane & 15;
    const int tau = eg * 4 + tile, e = 32 * (tau >> 1) + 8 * (r >> 2) + 4 * (tau & 1) + (r & 3);
    const unsigned short* src = (kb < 16 ? wre : wim) + (size_t)e * D_ + (kb & 15) * 32 + 8 * q;
    wdp = *(const v4u*)src;
    if (kb >= 16) wdp ^= 0x80008000u;
  }
  unsigned a1[8], a2[8];
  const int kb1 = blk >> 4, fc = blk & 15, q1 = tid / 56, pr = tid % 56, f0 = 112 * fc + 2 * pr;
  if (tid < 224) {
#pragma unroll
    for (int j = 0; j < 8; ++j) a1[j] = *(const unsigned*)(w1 + (size_t)(32 * kb1 + 8 * q1 + j) * F_ + f0);
  }
  if (blk < 224) {
#pragma unroll
    for (int j = 0; j < 8; ++j) a2[j] = *(const unsigned*)(w2 + (size_t)(8 * blk + j) * D_ + 2 * tid);
  }
#pragma unroll
  for (int i = 0; i < 2; ++i) {
    float v[8];
#pragma unroll
    for (int j = 0; j < 4; ++j) { v[2 * j] = bflo(xr[i][j]); v[2 * j + 1] = bfhi(xr[i][j]); }
    float s = 0.f;
#pragma unroll
    for (int j = 0; j < 8; ++j) s += v[j];
    const float mean = wave_sum(s) * (1.0f / D_);
    float ss = 0.f;
#pragma unroll
    for (int j = 0; j < 8; ++j) { const float c = v[j] - mean; ss += c * c; }
    const float var = wave_sum(ss) * (1.0f / D_);
    const float mb = rbf(mean), den = rbf(sqrtf(rbf(rbf(var) + 1e-5f))), rd = 1.0f / den;
    v4u o;
#pragma unroll
    for (int j = 0; j < 4; ++j) o[j] = pk2(rbf(v[2 * j] - mb) * rd, rbf(v[2 * j + 1] - mb) * rd);
    *(v4u*)(sm + (2 * w + i) * 1024 + lane * 16) = o;
  }
  *(v4u*)(ws + WD_OFF + (size_t)(blk * 256 + tid) * 16) = wdp;
  if (blk < 224) {
    const int d0 = 2 * tid, tau = 2 * (d0 >> 5) + ((d0 >> 2) & 1), r = 4 * ((d0 >> 3) & 3) + (d0 & 3), kb = blk >> 2, q = blk & 3;
    v4u lo, hi;
#pragma unroll
    for (int j = 0; j < 4; ++j) {
      lo[j] = __builtin_amdgcn_perm(a2[2 * j + 1], a2[2 * j], 0x05040100u);
      hi[j] = __builtin_amdgcn_perm(a2[2 * j + 1], a2[2 * j], 0x07060302u);
    }
    unsigned char* dst = ws + W2_OFF + (size_t)(((tau >> 2) * 56 + kb) * 4 + (tau & 3)) * 1024 + (q * 16 + r) * 16;
    *(v4u*)dst = lo;
    *(v4u*)(dst + 16) = hi;
  }
  float* csl = (float*)(sm + 8192);
  if (tid < 224) {
    const int tau = 2 * (f0 >> 5) + ((f0 >> 2) & 1), r = 4 * ((f0 >> 3) & 3) + (f0 & 3);
    v4u lo, hi;
    float s0 = 0.f, s1 = 0.f;
#pragma unroll
    for (int j = 0; j < 4; ++j) {
      lo[j] = __builtin_amdgcn_perm(a1[2 * j + 1], a1[2 * j], 0x05040100u);
      hi[j] = __builtin_amdgcn_perm(a1[2 * j + 1], a1[2 * j], 0x07060302u);
    }
#pragma unroll
    for (int j = 0; j < 8; ++j) { s0 += bflo(a1[j]); s1 += bfhi(a1[j]); }
    unsigned char* dst = ws + W1_OFF + (size_t)(((tau / 7) * 16 + kb1) * 7 + (tau % 7)) * 1024 + (q1 * 16 + r) * 16;
    *(v4u*)dst = lo;
    *(v4u*)(dst + 16) = hi;
    csl[q1 * 112 + 2 * pr] = s0;
    csl[q1 * 112 + 2 * pr + 1] = s1;
  }
  __syncthreads();
  if (tid < 112) {
    const float s = (csl[tid] + csl[112 + tid]) + (csl[224 + tid] + csl[336 + tid]);
    ((float*)(ws + CS_OFF))[kb1 * F_ + 112 * fc + tid] = s;
  }
  {
    unsigned c[8];
#pragma unroll
    for (int j = 0; j < 8; ++j) c[j] = *(const unsigned*)(sm + j * 1024 + tid * 4);
    v4u lo, hi;
#pragma unroll
    for (int j = 0; j < 4; ++j) {
      lo[j] = __builtin_amdgcn_perm(c[2 * j + 1], c[2 * j], 0x05040100u);
      hi[j] = __builtin_amdgcn_perm(c[2 * j + 1], c[2 * j], 0x07060302u);
    }
    const int d0 = 2 * tid, tau = 2 * (d0 >> 5) + ((d0 >> 2) & 1), r = 4 * ((d0 >> 3) & 3) + (d0 & 3), kb = blk >> 2, q = blk & 3;
    unsigned char* dst = ws + XNT_OFF + (size_t)(((tau >> 2) * 64 + kb) * 4 + (tau & 3)) * 1024 + (q * 16 + r) * 16;
    *(v4u*)dst = lo;
    *(v4u*)(dst + 16) = hi;
  }
}

constexpr int K1_NST = 6, K1_STAGE = 24576, K1_LDS = K1_NST * K1_STAGE;

__device__ __forceinline__ void glds16(const void* sbase, unsigned voff, unsigned ldsaddr) {
  asm volatile("s_mov_b32 m0, %2\n\ts_nop 0\n\tglobal_load_lds_dwordx4 %0, %1" ::"v"(voff), "s"(sbase), "s"(ldsaddr) : "memory");
}
__device__ __forceinline__ void glds16o1k(const void* sbase, unsigned voff, unsigned ldsaddr) {
  asm volatile("s_mov_b32 m0, %2\n\ts_nop 0\n\tglobal_load_lds_dwordx4 %0, %1\n\tglobal_load_lds_dwordx4 %0, %1 offset:1024" ::"v"(voff), "s"(sbase), "s"(ldsaddr) : "memory");
}

__global__ __launch_bounds__(256, 1) void k1_seq(const unsigned char* __restrict__ wsre, const unsigned char* __restrict__ wsim,
                                                 unsigned char* __restrict__ ws) {
  extern __shared__ __attribute__((aligned(16))) unsigned char smem[];
  const int bid = blockIdx.x, p = bid & 7, j = bid >> 3, ti = 4 * p + (j >> 3), dg = j & 7;
  const int tid = threadIdx.x, lane = tid & 63, w = __builtin_amdgcn_readfirstlane(tid >> 6);
  const unsigned lds0 = (unsigned)(size_t)(LAS unsigned char*)smem;
  const unsigned char* abase = ((w >> 1) ? wsim : wsre) + (size_t)(64 * ti + 32 * (w & 1)) * 4096;
  const unsigned char* bbase = ws + XNT_OFF + (size_t)dg * 262144 + w * 2048;
  const unsigned va0 = (lane >> 3) * 4096 + (((lane & 7) ^ (lane >> 4)) << 4);
  const unsigned va1 = va0 ^ 64;
  const unsigned vb = lane * 16;
  auto stage = [&](int ks, int slot) {
    const unsigned l = lds0 + slot * K1_STAGE;
    const unsigned char* a = abase + ks * 128;
    glds16(a, va0, l + w * 4096);
    glds16(a + 32768, va1, l + w * 4096 + 1024);
    glds16(a + 65536, va0, l + w * 4096 + 2048);
    glds16(a + 98304, va1, l + w * 4096 + 3072);
    glds16o1k(bbase + ks * 8192, vb, l + 16384 + w * 2048);
  };
#pragma unroll
  for (int s = 0; s < K1_NST - 1; ++s) stage(s, s);
  f32x4 acc[4][2];
#pragma unroll
  for (int i = 0; i < 4; ++i)
#pragma unroll
    for (int t = 0; t < 2; ++t) acc[i][t] = f32x4{0.f, 0.f, 0.f, 0.f};
  const int q = lane >> 4, r = lane & 15;
  const unsigned ra0 = w * 4096 + r * 128 + ((q ^ (r >> 1)) << 4), ra1 = ra0 ^ 64, rb = 16384 + lane * 16;
  int slot = 0, pslot = K1_NST - 1;
#pragma unroll 1
  for (int ks = 0; ks < 32; ++ks) {
    if (ks < 32 - (K1_NST - 2)) WAITV((K1_NST - 2) * 6);
    else WAITV(0);
    __builtin_amdgcn_s_barrier();
    if (ks + K1_NST - 1 < 32) stage(ks + K1_NST - 1, pslot);
    const unsigned char* sb = smem + slot * K1_STAGE;
    v4u wf[2][2], xf[4][2];
#pragma unroll
    for (int t = 0; t < 2; ++t) {
      wf[t][0] = *(const v4u*)(sb + ra0 + t * 2048);
      wf[t][1] = *(const v4u*)(sb + ra1 + t * 2048);
    }
#pragma unroll
    for (int k = 0; k < 2; ++k)
#pragma unroll
      for (int i = 0; i < 4; ++i) xf[i][k] = *(const v4u*)(sb + rb + k * 4096 + i * 1024);
#pragma unroll
    for (int k = 0; k < 2; ++k)
#pragma unroll
      for (int i = 0; i < 4; ++i)
#pragma unroll
        for (int t = 0; t < 2; ++t) acc[i][t] = mfma(xf[i][k], wf[t][k], acc[i][t]);
    pslot = slot;
    slot = (slot == K1_NST - 1) ? 0 : slot + 1;
  }
  unsigned char* pq = ws + PQ_OFF + (size_t)ti * 131072 + (size_t)(16 * (w >> 1) + 2 * dg) * 4096 + (2 * (w & 1)) * 1024 + lane * 16;
#pragma unroll
  for (int g = 0; g < 2; ++g)
#pragma unroll
    for (int t = 0; t < 2; ++t) {
      v4u o;
      o[0] = pk2(acc[2 * g][t][0], acc[2 * g][t][1]);
      o[1] = pk2(acc[2 * g][t][2], acc[2 * g][t][3]);
      o[2] = pk2(acc[2 * g + 1][t][0], acc[2 * g + 1][t][1]);
      o[3] = pk2(acc[2 * g + 1][t][2], acc[2 * g + 1][t][3]);
      *(v4u*)(pq + g * 4096 + t * 1024) = o;
    }
}

template <int NKB, int DEPTH>
__device__ __forceinline__ void gemm64_splitk(const unsigned char* __restrict__ ap, const unsigned char* __restrict__ bp,
                                              f32x4 (&acc)[4][4]) {
  v4u ra[DEPTH][4], rb[DEPTH][4];
#pragma unroll
  for (int s = 0; s < DEPTH; ++s) {
#pragma unroll
    for (int i = 0; i < 4; ++i) {
      ra[s][i] = *(const v4u*)(ap + s * 4096 + i * 1024);
      rb[s][i] = *(const v4u*)(bp + s * 4096 + i * 1024);
    }
    SCHED();
  }
#pragma unroll
  for (int i = 0; i < 4; ++i)
#pragma unroll
    for (int t = 0; t < 4; ++t) acc[i][t] = f32x4{0.f, 0.f, 0.f, 0.f};
#pragma unroll
  for (int s = 0; s < NKB; ++s) {
#pragma unroll
    for (int i = 0; i < 4; ++i)
#pragma unroll
      for (int t = 0; t < 4; ++t) acc[i][t] = mfma(ra[s % DEPTH][i], rb[s % DEPTH][t], acc[i][t]);
    SCHED();
    if (s + DEPTH < NKB) {
#pragma unroll
      for (int i = 0; i < 4; ++i) {
        ra[s % DEPTH][i] = *(const v4u*)(ap + (s + DEPTH) * 4096 + i * 1024);
        rb[s % DEPTH][i] = *(const v4u*)(bp + (s + DEPTH) * 4096 + i * 1024);
      }
      SCHED();
    }
  }
}
__device__ __forceinline__ void reduce4(unsigned char* sm, int w, int lane, f32x4 (&acc)[4][4], f32x4 (&y)[4]) {
#pragma unroll
  for (int t = 0; t < 4; ++t)
#pragma unroll
    for (int i = 0; i < 4; ++i) *(f32x4*)(sm + ((t * 4 + w) * 4 + i) * 1024 + lane * 16) = acc[i][t];
  __syncthreads();
#pragma unroll
  for (int i = 0; i < 4; ++i) {
    f32x4 a = *(const f32x4*)(sm + ((w * 4 + 0) * 4 + i) * 1024 + lane * 16);
    f32x4 b = *(const f32x4*)(sm + ((w * 4 + 1) * 4 + i) * 1024 + lane * 16);
    f32x4 c = *(const f32x4*)(sm + ((w * 4 + 2) * 4 + i) * 1024 + lane * 16);
    f32x4 d = *(const f32x4*)(sm + ((w * 4 + 3) * 4 + i) * 1024 + lane * 16);
    y[i] = (a + b) + (c + d);
  }
}

__global__ __launch_bounds__(256, 1) void k2_hid(const unsigned short* __restrict__ x, unsigned char* __restrict__ ws) {
  extern __shared__ __attribute__((aligned(16))) unsigned char smem[];
  const int bid = blockIdx.x, p = bid & 7, j = bid >> 3, ti = 4 * p + (j >> 3), eg = j & 7;
  const int tid = threadIdx.x, lane = tid & 63, w = __builtin_amdgcn_readfirstlane(tid >> 6), q = lane >> 4, r = lane & 15;
  const int t = 64 * ti + 16 * w + r;
  v4u xr[2];
#pragma unroll
  for (int g = 0; g < 2; ++g) xr[g] = *(const v4u*)(x + (size_t)t * D_ + 64 * eg + 32 * g + 8 * q);
  f32x4 acc[4][4], y[4];
  gemm64_splitk<8, 6>(ws + WD_OFF + (size_t)eg * 131072 + w * 32768 + lane * 16, ws + PQ_OFF + (size_t)ti * 131072 + w * 32768 + lane * 16, acc);
  reduce4(smem, w, lane, acc, y);
  float hv[16];
  float s = 0.f;
#pragma unroll
  for (int g = 0; g < 2; ++g) {
    v4u o;
#pragma unroll
    for (int h = 0; h < 2; ++h) {
      const f32x4 yy = y[2 * g + h];
      o[2 * h] = pk2(bflo(xr[g][2 * h]) + yy[0], bfhi(xr[g][2 * h]) + yy[1]);
      o[2 * h + 1] = pk2(bflo(xr[g][2 * h + 1]) + yy[2], bfhi(xr[g][2 * h + 1]) + yy[3]);
    }
#pragma unroll
    for (int k = 0; k < 4; ++k) { hv[8 * g + 2 * k] = bflo(o[k]); hv[8 * g + 2 * k + 1] = bfhi(o[k]); }
    *(v4u*)(ws + H_OFF + (size_t)((4 * ti + w) * 16 + 2 * eg + g) * 1024 + lane * 16) = o;
  }
#pragma unroll
  for (int k = 0; k < 16; ++k) s += hv[k];
  s += __shfl_xor(s, 16);
  s += __shfl_xor(s, 32);
  const float m = s * (1.0f / 64.0f);
  float m2 = 0.f;
#pragma unroll
  for (int k = 0; k < 16; ++k) { const float c = hv[k] - m; m2 += c * c; }
  m2 += __shfl_xor(m2, 16);
  m2 += __shfl_xor(m2, 32);
  if (q == 0) *(f32x2*)(ws + ST_OFF + (size_t)(t * 8 + eg) * 8) = f32x2{m, m2};
}

constexpr int K3_LDS = 114688 + 112 * 4 + 128 * 8;
__global__ __launch_bounds__(256, 1) void k3_ffn1(unsigned char* __restrict__ ws) {
  extern __shared__ __attribute__((aligned(16))) unsigned char smem[];
  const int bid = blockIdx.x, p = bid & 7, j = bid >> 3, t128 = 2 * p + (j >> 4), fs = j & 15;
  const int tid = threadIdx.x, lane = tid & 63, w = __builtin_amdgcn_readfirstlane(tid >> 6), q = lane >> 4, r = lane & 15;
  const unsigned lds0 = (unsigned)(size_t)(LAS unsigned char*)smem;
  float* csl = (float*)(smem + 114688);
  f32x2* stl = (f32x2*)(smem + 114688 + 448);
  if (tid < 112) {
    const int tau = 7 * fs + (tid >> 4), rr = tid & 15, f = 32 * (tau >> 1) + 8 * (rr >> 2) + 4 * (tau & 1) + (rr & 3);
    const float* cs = (const float*)(ws + CS_OFF) + f;
    float v[16];
#pragma unroll
    for (int k = 0; k < 16; ++k) v[k] = cs[k * F_];
    float s = 0.f;
#pragma unroll
    for (int k = 0; k < 16; ++k) s += v[k];
    csl[tid] = s;
  } else if (tid >= 128) {
    const f32x4* st = (const f32x4*)(ws + ST_OFF + (size_t)(128 * t128 + tid - 128) * 64);
    f32x4 v[4];
#pragma unroll
    for (int k = 0; k < 4; ++k) v[k] = st[k];
    float m = 0.f, m2 = 0.f;
#pragma unroll
    for (int k = 0; k < 4; ++k) { m += v[k][0] + v[k][2]; m2 += v[k][1] + v[k][3]; }
    m *= 0.125f;
#pragma unroll
    for (int k = 0; k < 4; ++k) { const float a = v[k][0] - m, b = v[k][2] - m; m2 += 64.0f * (a * a + b * b); }
    const float var = m2 * (1.0f / D_);
    const float den = rbf(sqrtf(rbf(rbf(var) + 1e-5f)));
    stl[tid - 128] = f32x2{rbf(m), 1.0f / den};
  }
  WAITV(0);
  const unsigned char* hb = ws + H_OFF + (size_t)(8 * t128 + 2 * w) * 16384;
  const unsigned char* wb = ws + W1_OFF + (size_t)fs * 114688 + w * 7168;
  const unsigned vo = lane * 16;
  v4u hf[16][2];
#pragma unroll
  for (int c = 0; c < 4; ++c) {
#pragma unroll
    for (int k = 0; k < 4; ++k)
#pragma unroll
      for (int t = 0; t < 2; ++t)
        asm volatile("global_load_dwordx4 %0, %1, %2 offset:%3" : "=v"(hf[4 * c + k][t]) : "v"(vo), "s"(hb + t * 16384 + c * 4096), "i"(k * 1024) : "memory");
#pragma unroll
    for (int i = 0; i < 7; ++i) glds16(wb + c * 28672 + i * 1024, vo, lds0 + c * 28672 + w * 7168 + i * 1024);
  }
  f32x4 acc[7][2];
#pragma unroll
  for (int i = 0; i < 7; ++i)
#pragma unroll
    for (int t = 0; t < 2; ++t) acc[i][t] = f32x4{0.f, 0.f, 0.f, 0.f};
#pragma unroll
  for (int c = 0; c < 4; ++c) {
    asm volatile("s_waitcnt vmcnt(%8)"
                 : "+v"(hf[4 * c][0]), "+v"(hf[4 * c][1]), "+v"(hf[4 * c + 1][0]), "+v"(hf[4 * c + 1][1]), "+v"(hf[4 * c + 2][0]),
                   "+v"(hf[4 * c + 2][1]), "+v"(hf[4 * c + 3][0]), "+v"(hf[4 * c + 3][1])
                 : "i"(45 - 15 * c)
                 : "memory");
    __builtin_amdgcn_s_barrier();
#pragma unroll
    for (int k = 0; k < 4; ++k) {
      v4u wf[7];
#pragma unroll
      for (int i = 0; i < 7; ++i) wf[i] = *(const v4u*)(smem + ((4 * c + k) * 7 + i) * 1024 + lane * 16);
#pragma unroll
      for (int i = 0; i < 7; ++i)
#pragma unroll
        for (int t = 0; t < 2; ++t) acc[i][t] = mfma(wf[i], hf[4 * c + k][t], acc[i][t]);
    }
  }
#pragma unroll
  for (int t = 0; t < 2; ++t) {
    const f32x2 st = stl[32 * w + 16 * t + r];
    const int t16 = 8 * t128 + 2 * w + t;
    unsigned char* fb = ws + FF_OFF + (size_t)(t16 >> 2) * 229376 + (t16 & 3) * 1024 + lane * 16;
#pragma unroll
    for (int i = 0; i < 7; ++i) {
      const f32x4 cs = *(const f32x4*)(csl + i * 16 + 4 * q);
      const int tau = 7 * fs + i;
      f32x4 v = (acc[i][t] - st[0] * cs) * st[1];
      v2u o;
      o[0] = pk2(fmaxf(v[0], 0.f), fmaxf(v[1], 0.f));
      o[1] = pk2(fmaxf(v[2], 0.f), fmaxf(v[3], 0.f));
      *(v2u*)(fb + (tau >> 1) * 4096 + (tau & 1) * 8) = o;
    }
  }
}

__global__ __launch_bounds__(256, 1) void k4_ffn2(const unsigned char* __restrict__ ws, unsigned short* __restrict__ out) {
  extern __shared__ __attribute__((aligned(16))) unsigned char smem[];
  const int bid = blockIdx.x, p = bid & 7, j = bid >> 3, ti = 4 * p + (j >> 3), dg = j & 7;
  const int tid = threadIdx.x, lane = tid & 63, w = __builtin_amdgcn_readfirstlane(tid >> 6), q = lane >> 4, r = lane & 15;
  const int t = 64 * ti + 16 * w + r;
  v4u hr[2];
#pragma unroll
  for (int g = 0; g < 2; ++g) hr[g] = *(const v4u*)(ws + H_OFF + (size_t)((4 * ti + w) * 16 + 2 * dg + g) * 1024 + lane * 16);
  f32x4 acc[4][4], y[4];
  gemm64_splitk<14, 6>(ws + W2_OFF + (size_t)dg * 229376 + w * 57344 + lane * 16, ws + FF_OFF + (size_t)ti * 229376 + w * 57344 + lane * 16, acc);
  reduce4(smem, w, lane, acc, y);
#pragma unroll
  for (int g = 0; g < 2; ++g) {
    v4u o;
#pragma unroll
    for (int h = 0; h < 2; ++h) {
      const f32x4 yy = y[2 * g + h];
      o[2 * h] = pk2(bflo(hr[g][2 * h]) + yy[0], bfhi(hr[g][2 * h]) + yy[1]);
      o[2 * h + 1] = pk2(bflo(hr[g][2 * h + 1]) + yy[2], bfhi(hr[g][2 * h + 1]) + yy[3]);
    }
    *(v4u*)(out + (size_t)t * D_ + 64 * dg + 32 * g + 8 * q) = o;
  }
}

extern "C" void kernel_launch(void* const* d_in, const int* in_sizes, int n_in, void* d_out, int out_size, void* d_ws,
                              size_t ws_size, hipStream_t stream) {
  const unsigned short* x = (const unsigned short*)d_in[0];
  const unsigned char* wsre = (const unsigned char*)d_in[1];
  const unsigned char* wsim = (const unsigned char*)d_in[2];
  const unsigned short* wre = (const unsigned short*)d_in[3];
  const unsigned short* wim = (const unsigned short*)d_in[4];
  const unsigned short* w1 = (const unsigned short*)d_in[6];
  const unsigned short* w2 = (const unsigned short*)d_in[7];
  unsigned char* ws = (unsigned char*)d_ws;
  (void)hipFuncSetAttribute((const void*)k1_seq, hipFuncAttributeMaxDynamicSharedMemorySize, K1_LDS);
  (void)hipFuncSetAttribute((const void*)k2_hid, hipFuncAttributeMaxDynamicSharedMemorySize, 65536);
  (void)hipFuncSetAttribute((const void*)k3_ffn1, hipFuncAttributeMaxDynamicSharedMemorySize, K3_LDS);
  (void)hipFuncSetAttribute((const void*)k4_ffn2, hipFuncAttributeMaxDynamicSharedMemorySize, 65536);
  k0_prep<<<256, 256, 0, stream>>>(x, wre, wim, w1, w2, ws);
  k1_seq<<<256, 256, K1_LDS, stream>>>(wsre, wsim, ws);
  k2_hid<<<256, 256, 65536, stream>>>(x, ws);
  k3_ffn1<<<256, 256, K3_LDS, stream>>>(ws);
  k4_ffn2<<<256, 256, 65536, stream>>>(ws, (unsigned short*)d_out);
}
```

```cpp
#include <hip/hip_runtime.h>
#include <hip/hip_bf16.h>
#include <stdint.h>
#include <type_traits>

typedef unsigned v4u __attribute__((ext_vector_type(4)));
typedef unsigned v2u __attribute__((ext_vector_type(2)));
typedef float f32x4 __attribute__((ext_vector_type(4)));
typedef float f32x2 __attribute__((ext_vector_type(2)));
typedef short bf16x8 __attribute__((ext_vector_type(8)));
typedef short v4s __attribute__((ext_vector_type(4)));
typedef __bf16 bf2_t __attribute__((ext_vector_type(2)));
typedef const unsigned char* cptr;
#define LAS __attribute__((address_space(3)))

constexpr int S_ = 2048, D_ = 512, F_ = 1792;
constexpr unsigned EPOCH_OFF = 0;
constexpr unsigned FLAG0_OFF = 8192;
constexpr unsigned FLAGW_OFF = 16384;
constexpr unsigned FLAG1_OFF = 32768;
constexpr unsigned FLAG2_OFF = 65536;
constexpr unsigned FLAG3_OFF = 98304;
constexpr unsigned XNT_OFF = 131072;
constexpr unsigned PQ_OFF  = XNT_OFF + (2u << 20);
constexpr unsigned WD_OFF  = PQ_OFF + (4u << 20);
constexpr unsigned H_OFF   = WD_OFF + (1u << 20);
constexpr unsigned W1_OFF  = H_OFF + (2u << 20);
constexpr unsigned FF_OFF  = W1_OFF + 1835008u;
constexpr unsigned W2_OFF  = FF_OFF + 7340032u;
constexpr unsigned CS_OFF  = W2_OFF + 1835008u;
constexpr unsigned ST_OFF  = CS_OFF + 131072;

constexpr int XS_ROW = 1088;
constexpr int XS_OFF = 131072, W2S_OFF = XS_OFF + 8 * XS_ROW, W1S_OFF = W2S_OFF + 8 * XS_ROW, WDS_OFF = W1S_OFF + 8192;
constexpr int RED_OFF = 114688;
constexpr int LDS_BYTES = 163840;
constexpr unsigned SPIN_CAP = 1u << 20;

__device__ __forceinline__ unsigned pk2(float a, float b) {
  f32x2 v = {a, b};
  return __builtin_bit_cast(unsigned, __builtin_convertvector(v, bf2_t));
}
__device__ __forceinline__ float asf(unsigned u) { return __builtin_bit_cast(float, u); }
__device__ __forceinline__ float bflo(unsigned u) { return __builtin_bit_cast(float, u << 16); }
__device__ __forceinline__ float bfhi(unsigned u) { return __builtin_bit_cast(float, u & 0xffff0000u); }
__device__ __forceinline__ float rbf(float x) { return (float)(__bf16)x; }
__device__ __forceinline__ float ln_rstd(float var) {
  return __builtin_amdgcn_rcpf(rbf(__builtin_amdgcn_sqrtf(rbf(rbf(var) + 1e-5f))));
}
__device__ __forceinline__ f32x4 mfma(v4u a, v4u b, f32x4 c) {
  return __builtin_amdgcn_mfma_f32_16x16x32_bf16(__builtin_bit_cast(bf16x8, a), __builtin_bit_cast(bf16x8, b), c, 0, 0, 0);
}
template <int CTRL> __device__ __forceinline__ float dpp_add(float x) {
  return x + __builtin_bit_cast(float, __builtin_amdgcn_update_dpp(0, __builtin_bit_cast(int, x), CTRL, 0xf, 0xf, true));
}
__device__ __forceinline__ float row16_sum(float x) {
  return dpp_add<0x121>(dpp_add<0x122>(dpp_add<0x124>(dpp_add<0x128>(x))));
}
__device__ __forceinline__ float rows_sum(float x) {
  unsigned a = __builtin_bit_cast(unsigned, x), b = a;
  asm volatile("" : "+v"(b));
  auto r = __builtin_amdgcn_permlane16_swap(a, b, false, false);
  x = asf(r[0]) + asf(r[1]);
  a = __builtin_bit_cast(unsigned, x); b = a;
  asm volatile("" : "+v"(b));
  auto s = __builtin_amdgcn_permlane32_swap(a, b, false, false);
  return asf(s[0]) + asf(s[1]);
}
#define SCHED() __builtin_amdgcn_sched_barrier(0)
#define WAITV(n) asm volatile("s_waitcnt vmcnt(%0)" ::"i"(n) : "memory")
#define WAITV_BAR(n) asm volatile("s_waitcnt vmcnt(%0) lgkmcnt(0)\n\ts_barrier" ::"i"(n) : "memory")
#define BAR() asm volatile("s_waitcnt lgkmcnt(0)\n\ts_barrier" ::: "memory")
#define PIN4(a, b, c, d) asm volatile("" : "+v"(a), "+v"(b), "+v"(c), "+v"(d))
#define PIN4A(a, b, c, d) asm volatile("" : "+a"(a), "+a"(b), "+a"(c), "+a"(d))

#define LD16(d, vo, sb, imm) asm volatile("global_load_dwordx4 %0, %1, %2 offset:%3" : "=v"(d) : "v"(vo), "s"(sb), "i"(imm) : "memory")
#define LD16C(d, vo, sb, imm) asm volatile("global_load_dwordx4 %0, %1, %2 offset:%3 sc1" : "=v"(d) : "v"(vo), "s"(sb), "i"(imm) : "memory")
#define LD16CA(d, vo, sb, imm) asm volatile("global_load_dwordx4 %0, %1, %2 offset:%3 sc1" : "=a"(d) : "v"(vo), "s"(sb), "i"(imm) : "memory")
#define LD8C(d, vo, sb, imm) asm volatile("global_load_dwordx2 %0, %1, %2 offset:%3 sc1" : "=v"(d) : "v"(vo), "s"(sb), "i"(imm) : "memory")
#define LD4(d, vo, sb, imm) asm volatile("global_load_dword %0, %1, %2 offset:%3" : "=v"(d) : "v"(vo), "s"(sb), "i"(imm) : "memory")
#define LD4C(d, vo, sb, imm) asm volatile("global_load_dword %0, %1, %2 offset:%3 sc1" : "=v"(d) : "v"(vo), "s"(sb), "i"(imm) : "memory")
#define ST16C(v, vo, sb, imm) asm volatile("global_store_dwordx4 %0, %1, %2 offset:%3 sc1\n\ts_nop 1" ::"v"(vo), "v"(v), "s"(sb), "i"(imm) : "memory")
#define ST16(v, vo, sb, imm) asm volatile("global_store_dwordx4 %0, %1, %2 offset:%3\n\ts_nop 1" ::"v"(vo), "v"(v), "s"(sb), "i"(imm) : "memory")
#define ST8C(v, vo, sb, imm) asm volatile("global_store_dwordx2 %0, %1, %2 offset:%3 sc1" ::"v"(vo), "v"(v), "s"(sb), "i"(imm) : "memory")
#define ST4C(v, vo, sb, imm) asm volatile("global_store_dword %0, %1, %2 offset:%3 sc1" ::"v"(vo), "v"(v), "s"(sb), "i"(imm) : "memory")
__device__ __forceinline__ void st_flag(unsigned char* p, unsigned tag) {
  const unsigned z = 0;
  asm volatile("s_mov_b64 exec, 1\n\tglobal_store_dword %0, %1, %2 sc1\n\ts_mov_b64 exec, -1" ::"v"(z), "v"(tag), "s"(p) : "memory");
}
#define GLDS(vo, sb, lds, imm) asm volatile("s_mov_b32 m0, %2\n\ts_nop 0\n\tglobal_load_lds_dwordx4 %0, %1 offset:%3" ::"v"(vo), "s"(sb), "s"(lds), "i"(imm) : "memory")
#define GLDSC(vo, sb, lds, imm) asm volatile("s_mov_b32 m0, %2\n\ts_nop 0\n\tglobal_load_lds_dwordx4 %0, %1 offset:%3 sc1" ::"v"(vo), "s"(sb), "s"(lds), "i"(imm) : "memory")

__device__ __forceinline__ void poll_flags(cptr base, int n, unsigned tag, int lane) {
  const unsigned vo = (lane < n ? lane : 0) * 128;
  for (unsigned spin = 0; spin < SPIN_CAP; ++spin) {
    unsigned v;
    asm volatile("global_load_dword %0, %1, %2 sc1\n\ts_waitcnt vmcnt(0)" : "=&v"(v) : "v"(vo), "s"(base) : "memory");
    if (__all(v == tag)) break;
  }
}
__device__ __forceinline__ void poll_flags256(cptr base, unsigned tag, int lane) {
  const unsigned vo = lane * 128;
  for (unsigned spin = 0; spin < SPIN_CAP; ++spin) {
    v4u v;
    asm volatile("global_load_dwordx4 %0, %1, %2 sc1\n\ts_waitcnt vmcnt(0)" : "=&v"(v) : "v"(vo), "s"(base) : "memory");
    if (__all(v[0] == tag && v[1] == tag && v[2] == tag && v[3] == tag)) break;
  }
}
__device__ __forceinline__ void reduce4(unsigned char* red, int w, int lane, f32x4 (&acc)[4][4]) {
#pragma unroll
  for (int c = 1; c < 4; ++c) {
    unsigned char* d = red + (((c + w) & 3) * 3 + c - 1) * 4096 + lane * 16;
#pragma unroll
    for (int i = 0; i < 4; ++i) *(f32x4*)(d + i * 1024) = acc[i][c];
  }
  BAR();
  const unsigned char* s = red + w * 12288 + lane * 16;
#pragma unroll
  for (int i = 0; i < 4; ++i)
    acc[i][0] = (acc[i][0] + *(const f32x4*)(s + i * 1024)) + (*(const f32x4*)(s + 4096 + i * 1024) + *(const f32x4*)(s + 8192 + i * 1024));
}

#define NP0 3
#define NP 12
#define PF2_WHEN 1
#define RT_WHEN 3
#define WD_PRE 2
constexpr int DA = 12, DB = 7, D2 = 4, D3 = 6, D4 = 5, FLW_IT = 20;

template <int I, int N, class F> __device__ __forceinline__ void static_for(F&& f) {
  if constexpr (I < N) { f(std::integral_constant<int, I>{}); static_for<I + 1, N>(f); }
}
constexpr int p1_wait(int it) {
  int posA[44] = {}, posB[40] = {}, n = 0;
  for (int s = 0; s < 44; ++s) posA[s] = -1;
  for (int s = 0; s < 8; ++s) {
    if (s < DB) { n += 4; posB[s] = n - 1; }
    for (int u = NP + 2 * s; u < NP + 2 * s + 2; ++u) if (u < DA) { n += 2; posA[u] = n - 1; }
  }
  if (it < 0) return n - 1 - posA[3];
  for (int s = 0; s < it; ++s) {
    if (s == FLW_IT) n += 1;
    if (s + DA < 32) { n += 2; posA[s + DA] = n - 1; }
    if (s + DB < 32) { n += 4; posB[s + DB] = n - 1; }
    if (s == 29) n += 4;
    if (s == 30) n += 5;
  }
  int req = posB[it];
  if ((it & 3) == 3 && it + 4 < 32 && posA[it + 4] > req) req = posA[it + 4];
  return n - 1 - req;
}
constexpr int p3_wait(int k) {
  int n = k < D3 ? 2 * (D3 - 1 - k) : 0;
  for (int s = (k < D3 ? 0 : k - D3 + 1); s < k; ++s) n += (s >= 2 ? 2 : 0) + (s + D3 < 16 ? 2 : 0);
  return n;
}

__global__ __launch_bounds__(256, 1) void fnet_fused(cptr x, cptr wsre, cptr wsim, cptr wre, cptr wim, cptr w1, cptr w2,
                                                     unsigned char* ws, unsigned char* out) {
  extern __shared__ __attribute__((aligned(16))) unsigned char smem[];
  const int bid = blockIdx.x, p = bid & 7, j = bid >> 3;
  const int ti = 4 * p + (j >> 3), cg = j & 7, t128 = 2 * p + (j >> 4), fs = j & 15;
  const int tid = threadIdx.x, lane = tid & 63, w = __builtin_amdgcn_readfirstlane(tid >> 6), q = lane >> 4, r = lane & 15;
  const unsigned lds0 = (unsigned)(size_t)(LAS unsigned char*)smem;
  const unsigned vl16 = lane * 16;

  const int mat = (j >> 2) & 1, nt = j & 3;
  v4u xr0, xr1;
  unsigned epoch;
  {
    cptr xb = x + (size_t)(8 * bid + 2 * w) * 1024;
    LD16(xr0, vl16, xb, 0);
    LD16(xr1, vl16, xb, 1024);
    const unsigned zero = 0;
    LD4C(epoch, zero, ws + EPOCH_OFF, 0);
  }
  const unsigned va0 = (lane >> 3) * 4096 + (((lane & 7) ^ (lane >> 4)) << 4), va1 = (va0 ^ 64) + 32768 - 1024;
  cptr abase = (mat ? wsim : wsre) + (size_t)(64 * ti + 16 * w) * 4096;
#define ROT 0
  const int ks0 = ROT == 2 ? 8 * nt : ROT == 1 ? 16 * (nt & 1) : 0;
  const unsigned lda = lds0 + w * 2048;
#define A_STAGE0(s) GLDS(va0, abase + ((ks0 + (s)) & 31) * 128, lda + ((s) & 15) * 8192, 0)
#define A_STAGE1(s) GLDS(va1, abase + ((ks0 + (s)) & 31) * 128, lda + ((s) & 15) * 8192, 1024)
  auto a_stage_all = [&](int s) {
    cptr sb = abase - w * 65536 + ((ks0 + s) & 31) * 128;
    const unsigned l = lds0 + s * 8192;
#pragma unroll
    for (int i = 0; i < 4; ++i) {
      GLDS(va0, sb + i * 65536, l + i * 2048, 0);
      GLDS(va1, sb + i * 65536, l + i * 2048, 1024);
    }
  };
  static_assert(NP0 % 3 == 0 && NP <= DA, "");
  const int kb1 = bid >> 4, fs1 = bid & 15, ob = bid < 224 ? bid : 223, kbd = bid & 31;
  if (w != 0) {
#pragma unroll 1
    for (int s = w - 1; s < NP0; s += 3) a_stage_all(s);
  }
  if (w == 1) {
#pragma unroll
    for (int k = 0; k < 8; ++k) GLDS(vl16, w2 + (size_t)(8 * ob + k) * 1024, lds0 + W2S_OFF + k * XS_ROW, 0);
    WAITV(8 * (NP0 / 3) + 8);
  } else if (w == 2) {
    const unsigned vo = q * (F_ * 2) + r * 16;
    cptr sb = w1 + (size_t)(32 * kb1) * (F_ * 2) + ((7 * fs1) >> 1) * 64;
#pragma unroll
    for (int k = 0; k < 8; ++k) GLDS(vo, sb + k * (4 * F_ * 2), lds0 + W1S_OFF + k * 1024, 0);
    WAITV(8 * (NP0 / 3) + 8);
  } else if (w == 3) {
    const unsigned vo = (8 * (r >> 2) + (r & 3)) * 1024 + q * 16;
    cptr sb = (kbd < 16 ? wre : wim) + (size_t)(64 * (bid >> 5)) * 1024 + (kbd & 15) * 64;
    GLDS(vo, sb, lds0 + WDS_OFF, 0);
    GLDS(vo, sb + 4096, lds0 + WDS_OFF + 1024, 0);
    GLDS(vo, sb + 32768, lds0 + WDS_OFF + 2048, 0);
    GLDS(vo, sb + 36864, lds0 + WDS_OFF + 3072, 0);
    WAITV(8 * (NP0 / 3) + 4);
  } else {
    WAITV(0);
  }
  asm volatile("" : "+v"(xr0), "+v"(xr1), "+v"(epoch));
  const unsigned tag = __builtin_amdgcn_readfirstlane(epoch) + 1u;
  {
    float v[2][8], s1[2], s2[2], pil[2];
#pragma unroll
    for (int k = 0; k < 4; ++k) {
      v[0][2 * k] = bflo(xr0[k]); v[0][2 * k + 1] = bfhi(xr0[k]);
      v[1][2 * k] = bflo(xr1[k]); v[1][2 * k + 1] = bfhi(xr1[k]);
    }
#pragma unroll
    for (int i = 0; i < 2; ++i) {
      pil[i] = __builtin_bit_cast(float, __builtin_amdgcn_readfirstlane(__builtin_bit_cast(int, v[i][0])));
      s1[i] = 0.f; s2[i] = 0.f;
#pragma unroll
      for (int k = 0; k < 8; ++k) { const float c = v[i][k] - pil[i]; s1[i] += c; s2[i] += c * c; }
    }
#pragma unroll
    for (int i = 0; i < 2; ++i) { s1[i] = rows_sum(row16_sum(s1[i])); s2[i] = rows_sum(row16_sum(s2[i])); }
#pragma unroll
    for (int i = 0; i < 2; ++i) {
      const float mc = s1[i] * (1.0f / D_);
      const float mb = rbf(pil[i] + mc), rd = ln_rstd(fmaxf(s2[i] * (1.0f / D_) - mc * mc, 0.f));
      v4u o;
#pragma unroll
      for (int k = 0; k < 4; ++k) o[k] = pk2(rbf(v[i][2 * k] - mb) * rd, rbf(v[i][2 * k + 1] - mb) * rd);
      *(v4u*)(smem + XS_OFF + (2 * w + i) * XS_ROW + lane * 16) = o;
    }
  }
  BAR();
  auto rows8_out = [&](int off, unsigned vo, unsigned char* sb, unsigned step) {
    const unsigned char* s = smem + off + ((lane >> 2) & 3) * XS_ROW + 64 * (q >> 1) + 16 * (lane & 3) + 8 * (q & 1);
#pragma unroll
    for (int i = 0; i < 8; ++i) {
      const v2u lo = __builtin_bit_cast(v2u, __builtin_amdgcn_ds_read_tr16_b64_v4i16((LAS v4s*)(s + 128 * i)));
      const v2u hi = __builtin_bit_cast(v2u, __builtin_amdgcn_ds_read_tr16_b64_v4i16((LAS v4s*)(s + 128 * i + 4 * XS_ROW)));
      const v4u o = {lo[0], lo[1], hi[0], hi[1]};
      ST16C(o, vo, sb + i * step, 0);
    }
  };
  auto retile = [&]() {
    if (w == 1) {
      rows8_out(W2S_OFF, q * 1024 + r * 16, ws + W2_OFF + (ob >> 2) * 4096 + (ob & 3) * 256, 229376);
    } else if (w == 2) {
      const unsigned char* s = smem + W1S_OFF + (8 * q + ((lane >> 2) & 3)) * 256 + 16 * (lane & 3);
      unsigned char* sb = ws + W1_OFF + fs1 * 114688 + kb1 * 7168;
      float csv = 0.f, csw = 0.f;
#pragma unroll
      for (int l = 0; l < 7; ++l) {
        const int tl = (l < 6) ? 2 * (fs1 & 1) + l : ((fs1 & 1) ? 1 : 6);
        const unsigned char* sl = s + (tl >> 1) * 64 + (tl & 1) * 8;
        const v2u lo = __builtin_bit_cast(v2u, __builtin_amdgcn_ds_read_tr16_b64_v4i16((LAS v4s*)sl));
        const v2u hi = __builtin_bit_cast(v2u, __builtin_amdgcn_ds_read_tr16_b64_v4i16((LAS v4s*)(sl + 1024)));
        const v4u o = {lo[0], lo[1], hi[0], hi[1]};
        ST16C(o, vl16, sb + l * 1024, 0);
        const float cs = rows_sum(((bflo(o[0]) + bfhi(o[0])) + (bflo(o[1]) + bfhi(o[1]))) + ((bflo(o[2]) + bfhi(o[2])) + (bflo(o[3]) + bfhi(o[3]))));
        if (l < 4) csv = (q == l) ? cs : csv; else csw = (q == l - 4 || (l == 6 && q == 3)) ? cs : csw;
      }
      unsigned char* cb = ws + CS_OFF + (kb1 * 16 + fs1) * 512;
      const unsigned vc = lane * 4;
      ST4C(csv, vc, cb, 0);
      ST4C(csw, vc, cb, 256);
    } else {
#pragma unroll
      for (int i = 0; i < 4; ++i) {
        v4u o = *(const v4u*)(smem + WDS_OFF + i * 1024 + vl16);
        if (kbd >= 16) o ^= 0x80008000u;
        ST16C(o, vl16, ws + WD_OFF + bid * 4096 + i * 1024, 0);
      }
    }
  };
  auto pf2 = [&]() {
#pragma unroll 1
    for (int s = NP0 + w - 1; s < NP; s += 3) a_stage_all(s);
  };
  if (w == 0) {
    rows8_out(XS_OFF, (q >> 1) * 131072 + (q & 1) * 1024 + r * 16, ws + XNT_OFF + (bid >> 2) * 2048 + (bid & 3) * 256, 262144);
    WAITV(0);
    st_flag(ws + FLAG0_OFF + (bid & 63) * 128 + (bid >> 6) * 4, tag);
  } else {
    WAITV(0);
    if (RT_WHEN == 0) retile();
    if (PF2_WHEN == 0) pf2();
  }
  if (RT_WHEN == 2 || PF2_WHEN == 1) BAR();
  if (w == 0) {
    poll_flags256(ws + FLAG0_OFF, tag, lane);
    if (bid == 0) st_flag(ws + EPOCH_OFF, tag);
  } else {
    if (RT_WHEN == 2) retile();
    if (PF2_WHEN == 1) pf2();
  }
  BAR();
  if (RT_WHEN == 1 && w != 0) retile();

  cptr bbase = ws + XNT_OFF + (4 * nt + w) * 131072;
  v4u bf[8][4];
#define B_LOAD(s, i) LD16CA(bf[(s) & 7][i], vl16, bbase + ((ks0 + (s)) & 31) * 4096, (i) * 1024)
  static_for<0, 8>([&](auto S) {
    constexpr int s = S;
    if constexpr (s < DB) { B_LOAD(s, 0); B_LOAD(s, 1); B_LOAD(s, 2); B_LOAD(s, 3); }
    if constexpr (NP + 2 * s < DA) { A_STAGE0(NP + 2 * s); A_STAGE1(NP + 2 * s); }
    if constexpr (NP + 2 * s + 1 < DA) { A_STAGE0(NP + 2 * s + 1); A_STAGE1(NP + 2 * s + 1); }
  });
  if (RT_WHEN == 3 && w != 0) retile();
  f32x4 acc1[2][4];
#pragma unroll
  for (int i = 0; i < 2; ++i)
#pragma unroll
    for (int t = 0; t < 4; ++t) acc1[i][t] = f32x4{0.f, 0.f, 0.f, 0.f};
  const unsigned ra0 = r * 128 + ((q ^ (r >> 1)) << 4), ra1 = ra0 ^ 64;
  v4u af[2][8];
  v4u flw, xres[2], csr[7];
  cptr xb2 = x + (size_t)(64 * ti + 16 * w) * 1024 + cg * 128;
  const unsigned vx = r * 1024 + q * 16;
  cptr cb2 = ws + CS_OFF + fs * 512;
  const unsigned vc2 = r * 8192 + q * 16;
  WAITV_BAR(p1_wait(-1));
#pragma unroll
  for (int t = 0; t < 4; ++t) { af[0][t] = *(const v4u*)(smem + ra0 + t * 2048); af[0][4 + t] = *(const v4u*)(smem + ra1 + t * 2048); }
  static_for<0, 32>([&](auto IT) {
    constexpr int it = IT, U = it & 7, C = it & 1, Nx = C ^ 1;
    constexpr bool RA = it + DA < 32, RB = it + DB < 32, RN = it + 1 < 32;
    if constexpr ((it & 3) == 3 && it + 4 < 32) { WAITV_BAR(p1_wait(it)); } else { WAITV(p1_wait(it)); }
    PIN4A(bf[U][0], bf[U][1], bf[U][2], bf[U][3]);
    if constexpr (it == 7) { if (w == 0) st_flag(ws + FLAGW_OFF + (bid & 63) * 128 + (bid >> 6) * 4, tag); }
    if constexpr (it == FLW_IT) { const unsigned vf = lane * 128; LD16C(flw, vf, ws + FLAGW_OFF, 0); };
    if constexpr (it == 28) {
      asm volatile("" : "+v"(flw));
      if (!__all(flw[0] == tag && flw[1] == tag && flw[2] == tag && flw[3] == tag)) poll_flags256(ws + FLAGW_OFF, tag, lane);
    }
    const unsigned char* sn = smem + ((it + 1) & 15) * 8192;
    SCHED();
    if constexpr (RA) A_STAGE0(it + DA);
    if constexpr (it == 29) LD16(xres[0], vx, xb2, 0);
    if constexpr (it == 30) LD16C(csr[2], vc2, cb2, 128);
    SCHED();
    if constexpr (RN) { af[Nx][0] = *(const v4u*)(sn + ra0); af[Nx][1] = *(const v4u*)(sn + ra0 + 2048); }
    acc1[0][0] = mfma(bf[U][0], af[C][0], acc1[0][0]); acc1[1][0] = mfma(bf[U][1], af[C][0], acc1[1][0]);
    acc1[0][1] = mfma(bf[U][0], af[C][1], acc1[0][1]);
    SCHED();
    if constexpr (RA) A_STAGE1(it + DA);
    if constexpr (it == 29) LD16(xres[1], vx, xb2, 64);
    if constexpr (it == 30) LD16C(csr[3], vc2, cb2, 192);
    SCHED();
    if constexpr (RN) { af[Nx][2] = *(const v4u*)(sn + ra0 + 4096); af[Nx][3] = *(const v4u*)(sn + ra0 + 6144); }
    acc1[1][1] = mfma(bf[U][1], af[C][1], acc1[1][1]); acc1[0][2] = mfma(bf[U][0], af[C][2], acc1[0][2]);
    acc1[1][2] = mfma(bf[U][1], af[C][2], acc1[1][2]);
    SCHED();
    if constexpr (RB) B_LOAD(it + DB, 0);
    if constexpr (it == 29) LD16C(csr[0], vc2, cb2, 0);
    if constexpr (it == 30) LD16C(csr[4], vc2, cb2, 256);
    SCHED();
    if constexpr (RN) { af[Nx][4] = *(const v4u*)(sn + ra1); af[Nx][5] = *(const v4u*)(sn + ra1 + 2048); }
    acc1[0][3] = mfma(bf[U][0], af[C][3], acc1[0][3]); acc1[1][3] = mfma(bf[U][1], af[C][3], acc1[1][3]);
    acc1[0][0] = mfma(bf[U][2], af[C][4], acc1[0][0]);
    SCHED();
    if constexpr (RB) B_LOAD(it + DB, 1);
    if constexpr (it == 29) LD16C(csr[1], vc2, cb2, 64);
    if constexpr (it == 30) LD16C(csr[5], vc2, cb2, 320);
    SCHED();
    if constexpr (RN) { af[Nx][6] = *(const v4u*)(sn + ra1 + 4096); af[Nx][7] = *(const v4u*)(sn + ra1 + 6144); }
    acc1[1][0] = mfma(bf[U][3], af[C][4], acc1[1][0]); acc1[0][1] = mfma(bf[U][2], af[C][5], acc1[0][1]);
    acc1[1][1] = mfma(bf[U][3], af[C][5], acc1[1][1]);
    SCHED();
    if constexpr (RB) B_LOAD(it + DB, 2);
    if constexpr (it == 30) LD16C(csr[6], vc2, cb2, 384);
    SCHED();
    acc1[0][2] = mfma(bf[U][2], af[C][6], acc1[0][2]); acc1[1][2] = mfma(bf[U][3], af[C][6], acc1[1][2]);
    SCHED();
    if constexpr (RB) B_LOAD(it + DB, 3);
    SCHED();
    acc1[0][3] = mfma(bf[U][2], af[C][7], acc1[0][3]); acc1[1][3] = mfma(bf[U][3], af[C][7], acc1[1][3]);
    SCHED();
  });
  {
    unsigned char* pq = ws + PQ_OFF + ti * 131072 + (16 * mat + 4 * nt + w) * 4096;
#pragma unroll
    for (int t = 0; t < 4; ++t) {
      const v4u o = {pk2(acc1[0][t][0], acc1[0][t][1]), pk2(acc1[0][t][2], acc1[0][t][3]),
                     pk2(acc1[1][t][0], acc1[1][t][1]), pk2(acc1[1][t][2], acc1[1][t][3])};
      ST16C(o, vl16, pq + t * 1024, 0);
    }
  }
  v4u wd[8][4];
  cptr wb = ws + WD_OFF + cg * 131072 + w * 32768;
#define WDL(k) { cptr sb_ = wb + (((k) + cg) & 7) * 4096; LD16CA(wd[k][0], vl16, sb_, 0); LD16CA(wd[k][1], vl16, sb_, 1024); LD16CA(wd[k][2], vl16, sb_, 2048); LD16CA(wd[k][3], vl16, sb_, 3072); }
#pragma unroll
  for (int k = 0; k < WD_PRE; ++k) WDL(k);
  WAITV_BAR(4 * WD_PRE);
  if (w == 0) st_flag(ws + FLAG1_OFF + (ti * 8 + cg) * 128, tag);
#pragma unroll
  for (int k = WD_PRE; k < 8; ++k) WDL(k);
  if (w == 0) poll_flags(ws + FLAG1_OFF + ti * 1024, 8, tag, lane);
  BAR();

  unsigned vrot[4];
#pragma unroll
  for (int c = 0; c < 4; ++c) vrot[c] = vl16 + ((c + w) & 3) * 1024;
  v4u hres[2];
  v4u w2a[7][4], w2b[7][4];
  cptr w2base = ws + W2_OFF + cg * 229376 + w * 57344;
  const int rot4 = (7 * cg) >> 2;
#define KB4(k) (((k) + rot4 >= 14 ? (k) + rot4 - 14 : (k) + rot4) * 4096)
  {
    f32x4 acc[4][4];
    v4u pq[8][4];
    cptr pb = ws + PQ_OFF + ti * 131072 + w * 32768;
#pragma unroll
    for (int k = 0; k < D2; ++k)
#pragma unroll
      for (int c = 0; c < 4; ++c) LD16C(pq[k][c], vrot[c], pb + ((k + cg) & 7) * 4096, 0);
#pragma unroll
    for (int i = 0; i < 4; ++i)
#pragma unroll
      for (int c = 0; c < 4; ++c) acc[i][c] = f32x4{0.f, 0.f, 0.f, 0.f};
#pragma unroll
    for (int k = 0; k < 8; ++k) {
      asm volatile("s_waitcnt vmcnt(%4)" : "+v"(pq[k][0]), "+v"(pq[k][1]), "+v"(pq[k][2]), "+v"(pq[k][3]) : "i"(k + D2 <= 8 ? 4 * D2 - 4 : 28 - 4 * k) : "memory");
      PIN4A(wd[k][0], wd[k][1], wd[k][2], wd[k][3]);
      SCHED();
#pragma unroll
      for (int i = 0; i < 4; ++i) {
#pragma unroll
        for (int c = 0; c < 4; ++c) acc[i][c] = mfma(wd[k][i], pq[k][c], acc[i][c]);
        SCHED();
        if (k + D2 < 8) LD16C(pq[k + D2][i], vrot[i], pb + ((k + D2 + cg) & 7) * 4096, 0);
        SCHED();
      }
    }
    reduce4(smem + RED_OFF, w, lane, acc);
    asm volatile("" : "+v"(xres[0]), "+v"(xres[1]));
    float hv[16];
#pragma unroll
    for (int g = 0; g < 2; ++g) {
#pragma unroll
      for (int h = 0; h < 2; ++h) {
        const f32x4 yy = acc[2 * g + h][0];
        hres[g][2 * h] = pk2(bflo(xres[g][2 * h]) + yy[0], bfhi(xres[g][2 * h]) + yy[1]);
        hres[g][2 * h + 1] = pk2(bflo(xres[g][2 * h + 1]) + yy[2], bfhi(xres[g][2 * h + 1]) + yy[3]);
      }
#pragma unroll
      for (int k = 0; k < 4; ++k) { hv[8 * g + 2 * k] = bflo(hres[g][k]); hv[8 * g + 2 * k + 1] = bfhi(hres[g][k]); }
    }
    unsigned char* hb = ws + H_OFF + ((4 * ti + w) * 16 + 2 * cg) * 1024;
    ST16C(hres[0], vl16, hb, 0);
    ST16C(hres[1], vl16, hb, 1024);
    float s = 0.f;
#pragma unroll
    for (int k = 0; k < 16; ++k) s += hv[k];
    const float m = rows_sum(s) * (1.0f / 64.0f);
    float m2 = 0.f;
#pragma unroll
    for (int k = 0; k < 16; ++k) { const float c = hv[k] - m; m2 += c * c; }
    m2 = rows_sum(m2);
    const f32x2 st = {m, m2};
    const unsigned vs = r * 8;
    asm volatile("s_mov_b64 exec, 0xffff\n\tglobal_store_dwordx2 %0, %1, %2 sc1\n\ts_mov_b64 exec, -1" ::"v"(vs), "v"(st), "s"(ws + ST_OFF + (4 * ti + w) * 1024 + cg * 128) : "memory");
  }
  WAITV_BAR(0);
  if (w == 0) {
    st_flag(ws + FLAG2_OFF + (t128 * 16 + fs) * 128, tag);
  } else {
    cptr sb = ws + W1_OFF + fs * 114688;
#pragma unroll 1
    for (int n = w - 1; n < 28; n += 3) {
      GLDSC(vl16, sb + n * 4096, lds0 + n * 4096, 0);
      GLDSC(vl16, sb + n * 4096, lds0 + n * 4096, 1024);
      GLDSC(vl16, sb + n * 4096, lds0 + n * 4096, 2048);
      GLDSC(vl16, sb + n * 4096, lds0 + n * 4096, 3072);
    }
  }
  f32x4 cs[7];
#pragma unroll
  for (int l = 0; l < 7; ++l) {
    asm volatile("" : "+v"(csr[l]));
#pragma unroll
    for (int k = 0; k < 4; ++k) cs[l][k] = row16_sum(asf(csr[l][k]));
  }
  if (w == 0) poll_flags(ws + FLAG2_OFF + t128 * 2048, 16, tag, lane);
  WAITV_BAR(0);

  {
    v4u hf[16][2];
    v2u str[2][2];
    cptr hb = ws + H_OFF + (8 * t128 + 2 * w) * 16384;
    const unsigned vh1 = vl16 + 16384;
    {
      const unsigned vs = q * 256 + r * 8;
      cptr sb = ws + ST_OFF + (8 * t128 + 2 * w) * 1024;
      LD8C(str[0][0], vs, sb, 0); LD8C(str[0][1], vs, sb, 128);
      LD8C(str[1][0], vs, sb, 1024); LD8C(str[1][1], vs, sb, 1152);
    }
#pragma unroll
    for (int k = 0; k < D3; ++k) { cptr sb = hb + ((k + fs) & 15) * 1024; LD16C(hf[k][0], vl16, sb, 0); LD16C(hf[k][1], vh1, sb, 0); }
    f32x4 acc[7][2];
#pragma unroll
    for (int l = 0; l < 7; ++l) { acc[l][0] = f32x4{0.f, 0.f, 0.f, 0.f}; acc[l][1] = f32x4{0.f, 0.f, 0.f, 0.f}; }
    v4u wf[2][7];
#pragma unroll
    for (int l = 0; l < 7; ++l) wf[0][l] = *(const v4u*)(smem + fs * 7168 + l * 1024 + vl16);
#pragma unroll
    for (int k = 0; k < 16; ++k) {
      asm volatile("s_waitcnt vmcnt(%2)" : "+v"(hf[k][0]), "+v"(hf[k][1]) : "i"(p3_wait(k)) : "memory");
      const unsigned char* wn = smem + ((k + 1 + fs) & 15) * 7168 + vl16;
      cptr hn = hb + ((k + D3 + fs) & 15) * 1024;
      SCHED();
#pragma unroll
      for (int l = 0; l < 7; ++l) {
        if (k + 1 < 16) wf[(k + 1) & 1][l] = *(const v4u*)(wn + l * 1024);
        acc[l][0] = mfma(wf[k & 1][l], hf[k][0], acc[l][0]);
        acc[l][1] = mfma(wf[k & 1][l], hf[k][1], acc[l][1]);
        SCHED();
        if (k + D3 < 16) {
          if (l == 3) LD16C(hf[k + D3][0], vl16, hn, 0);
          if (l == 5) LD16C(hf[k + D3][1], vh1, hn, 0);
        }
        if (k >= 2) {
          if (l == 0) LD16CA(w2a[(k - 2) >> 1][2 * (k & 1)], vl16, w2base + KB4((k - 2) >> 1), (2 * (k & 1)) * 1024);
          if (l == 1) LD16CA(w2a[(k - 2) >> 1][2 * (k & 1) + 1], vl16, w2base + KB4((k - 2) >> 1), (2 * (k & 1) + 1) * 1024);
        }
        SCHED();
      }
    }
    asm volatile("" : "+v"(str[0][0]), "+v"(str[0][1]), "+v"(str[1][0]), "+v"(str[1][1]));
    const int kbp = (7 * fs + (fs & 1)) >> 1, taus = 7 * fs + ((fs & 1) ? 0 : 6);
#pragma unroll
    for (int t = 0; t < 2; ++t) {
      const float m0 = asf(str[t][0][0]), m1 = asf(str[t][1][0]);
      const float m = rows_sum(m0 + m1) * 0.125f;
      const float da = m0 - m, db = m1 - m;
      const float m2 = rows_sum(asf(str[t][0][1]) + asf(str[t][1][1]) + 64.0f * (da * da + db * db));
      const float mb = rbf(m), rd = ln_rstd(m2 * (1.0f / D_));
      const int t16 = 8 * t128 + 2 * w + t;
      unsigned char* fb = ws + FF_OFF + (t16 >> 2) * 229376 + (t16 & 3) * 1024;
      v2u o[7];
#pragma unroll
      for (int l = 0; l < 7; ++l) {
        const f32x4 v = (acc[l][t] - mb * cs[l]) * rd;
        o[l][0] = pk2(fmaxf(v[0], 0.f), fmaxf(v[1], 0.f));
        o[l][1] = pk2(fmaxf(v[2], 0.f), fmaxf(v[3], 0.f));
      }
#pragma unroll
      for (int m_ = 0; m_ < 3; ++m_) {
        const v4u oo = {o[2 * m_][0], o[2 * m_][1], o[2 * m_ + 1][0], o[2 * m_ + 1][1]};
        ST16C(oo, vl16, fb + (kbp + m_) * 4096, 0);
      }
      ST8C(o[6], vl16, fb + (taus >> 1) * 4096 + (taus & 1) * 8, 0);
    }
  }
#define W2BL(k) { cptr sb_ = w2base + KB4(7 + (k)); LD16C(w2b[k][0], vl16, sb_, 0); LD16C(w2b[k][1], vl16, sb_, 1024); LD16C(w2b[k][2], vl16, sb_, 2048); LD16C(w2b[k][3], vl16, sb_, 3072); }
#pragma unroll
  for (int k = 0; k < WD_PRE; ++k) W2BL(k);
  WAITV_BAR(4 * WD_PRE);
  if (w == 0) st_flag(ws + FLAG3_OFF + (t128 * 16 + fs) * 128, tag);
#pragma unroll
  for (int k = WD_PRE; k < 7; ++k) W2BL(k);
  if (w == 0) poll_flags(ws + FLAG3_OFF + t128 * 2048, 16, tag, lane);
  BAR();

  {
    f32x4 acc[4][4];
    v4u ff[D4 + 1][4];
    cptr fb = ws + FF_OFF + ti * 229376 + w * 57344;
#pragma unroll
    for (int k = 0; k < D4; ++k)
#pragma unroll
      for (int c = 0; c < 4; ++c) LD16C(ff[k][c], vrot[c], fb + KB4(k), 0);
#pragma unroll
    for (int i = 0; i < 4; ++i)
#pragma unroll
      for (int c = 0; c < 4; ++c) acc[i][c] = f32x4{0.f, 0.f, 0.f, 0.f};
#pragma unroll
    for (int k = 0; k < 14; ++k) {
      constexpr int R = D4 + 1;
      asm volatile("s_waitcnt vmcnt(%4)" : "+v"(ff[k % R][0]), "+v"(ff[k % R][1]), "+v"(ff[k % R][2]), "+v"(ff[k % R][3]) : "i"(k + D4 <= 14 ? 4 * D4 - 4 : 52 - 4 * k) : "memory");
      if (k < 7) PIN4A(w2a[k][0], w2a[k][1], w2a[k][2], w2a[k][3]);
      else PIN4(w2b[k - 7][0], w2b[k - 7][1], w2b[k - 7][2], w2b[k - 7][3]);
      SCHED();
#pragma unroll
      for (int i = 0; i < 4; ++i) {
#pragma unroll
        for (int c = 0; c < 4; ++c) acc[i][c] = mfma(k < 7 ? w2a[k][i] : w2b[k - 7][i], ff[k % R][c], acc[i][c]);
        SCHED();
        if (k + D4 < 14) LD16C(ff[(k + D4) % R][i], vrot[i], fb + KB4(k + D4), 0);
        SCHED();
      }
    }
    reduce4(smem + RED_OFF, w, lane, acc);
    unsigned char* ob_ = out + (size_t)(64 * ti + 16 * w) * 1024 + cg * 128;
#pragma unroll
    for (int g = 0; g < 2; ++g) {
      v4u o;
#pragma unroll
      for (int h = 0; h < 2; ++h) {
        const f32x4 yy = acc[2 * g + h][0];
        o[2 * h] = pk2(bflo(hres[g][2 * h]) + yy[0], bfhi(hres[g][2 * h]) + yy[1]);
        o[2 * h + 1] = pk2(bflo(hres[g][2 * h + 1]) + yy[2], bfhi(hres[g][2 * h + 1]) + yy[3]);
      }
      if (g == 0) ST16(o, vx, ob_, 0); else ST16(o, vx, ob_, 64);
    }
  }
}

extern "C" void kernel_launch(void* const* d_in, const int* in_sizes, int n_in, void* d_out, int out_size, void* d_ws,
                              size_t ws_size, hipStream_t stream) {
  (void)hipFuncSetAttribute((const void*)fnet_fused, hipFuncAttributeMaxDynamicSharedMemorySize, LDS_BYTES);
  fnet_fused<<<256, 256, LDS_BYTES, stream>>>((cptr)d_in[0], (cptr)d_in[1], (cptr)d_in[2], (cptr)d_in[3], (cptr)d_in[4],
                                              (cptr)d_in[6], (cptr)d_in[7], (unsigned char*)d_ws, (unsigned char*)d_out);
}
```

```cpp
#define NTOUCH 3
#include <hip/hip_runtime.h>
#include <hip/hip_bf16.h>
#include <stdint.h>
#include <type_traits>

typedef unsigned v4u __attribute__((ext_vector_type(4)));
typedef unsigned v2u __attribute__((ext_vector_type(2)));
typedef float f32x4 __attribute__((ext_vector_type(4)));
typedef float f32x2 __attribute__((ext_vector_type(2)));
typedef short bf16x8 __attribute__((ext_vector_type(8)));
typedef short v4s __attribute__((ext_vector_type(4)));
typedef __bf16 bf2_t __attribute__((ext_vector_type(2)));
typedef const unsigned char* cptr;
#define LAS __attribute__((address_space(3)))

constexpr int S_ = 2048, D_ = 512, F_ = 1792;
constexpr unsigned EPOCH_OFF = 0;
constexpr unsigned FLAG0_OFF = 8192;
constexpr unsigned FLAGW_OFF = 16384;
constexpr unsigned XNT_OFF = 131072;
constexpr unsigned WD_OFF  = XNT_OFF + (2u << 20);
constexpr unsigned W1_OFF  = WD_OFF + (1u << 20);
constexpr unsigned W2_OFF  = W1_OFF + 1835008u;
constexpr unsigned CS_OFF  = W2_OFF + 1835008u;
constexpr unsigned PQ_OFF  = CS_OFF + 131072;
constexpr unsigned H_OFF   = PQ_OFF + (4u << 20);
constexpr unsigned FF_OFF  = H_OFF + (2u << 20);
constexpr unsigned ST_OFF  = FF_OFF + 7340032u;
constexpr unsigned CKP_OFF = ST_OFF + 262144;
constexpr unsigned CKH_OFF = CKP_OFF + 262144;
constexpr unsigned CKF_OFF = CKH_OFF + 262144;

constexpr int XS_ROW = 1088;
constexpr int XS_OFF = 131072;
constexpr int RED_OFF = 114688;
constexpr int LDS_BYTES = 163840;
constexpr unsigned SPIN_CAP = 1u << 20;

__device__ __forceinline__ unsigned pk2(float a, float b) {
  f32x2 v = {a, b};
  return __builtin_bit_cast(unsigned, __builtin_convertvector(v, bf2_t));
}
__device__ __forceinline__ float asf(unsigned u) { return __builtin_bit_cast(float, u); }
__device__ __forceinline__ float bflo(unsigned u) { return __builtin_bit_cast(float, u << 16); }
__device__ __forceinline__ float bfhi(unsigned u) { return __builtin_bit_cast(float, u & 0xffff0000u); }
__device__ __forceinline__ float rbf(float x) { return (float)(__bf16)x; }
__device__ __forceinline__ float relu(float v) { return v < 0.f ? 0.f : v; }
__device__ __forceinline__ float ln_rstd(float var) {
  return __builtin_amdgcn_rcpf(rbf(__builtin_amdgcn_sqrtf(rbf(rbf(var) + 1e-5f))));
}
__device__ __forceinline__ f32x4 mfma(v4u a, v4u b, f32x4 c) {
  return __builtin_amdgcn_mfma_f32_16x16x32_bf16(__builtin_bit_cast(bf16x8, a), __builtin_bit_cast(bf16x8, b), c, 0, 0, 0);
}
template <int CTRL> __device__ __forceinline__ float dpp_add(float x) {
  return x + __builtin_bit_cast(float, __builtin_amdgcn_update_dpp(0, __builtin_bit_cast(int, x), CTRL, 0xf, 0xf, true));
}
__device__ __forceinline__ float row16_sum(float x) {
  return dpp_add<0x121>(dpp_add<0x122>(dpp_add<0x124>(dpp_add<0x128>(x))));
}
__device__ __forceinline__ float rows_sum(float x) {
  unsigned a = __builtin_bit_cast(unsigned, x), b = a;
  asm volatile("" : "+v"(b));
  auto r = __builtin_amdgcn_permlane16_swap(a, b, false, false);
  x = asf(r[0]) + asf(r[1]);
  a = __builtin_bit_cast(unsigned, x); b = a;
  asm volatile("" : "+v"(b));
  auto s = __builtin_amdgcn_permlane32_swap(a, b, false, false);
  return asf(s[0]) + asf(s[1]);
}
#define SCHED() __builtin_amdgcn_sched_barrier(0)
#define WAITV(n) asm volatile("s_waitcnt vmcnt(%0)" ::"i"(n) : "memory")
#define WAITV_BAR(n) asm volatile("s_waitcnt vmcnt(%0) lgkmcnt(0)\n\ts_barrier" ::"i"(n) : "memory")
#define BAR() asm volatile("s_waitcnt lgkmcnt(0)\n\ts_barrier" ::: "memory")
#define PIN4(a, b, c, d) asm volatile("" : "+v"(a), "+v"(b), "+v"(c), "+v"(d))
#define PIN4A(a, b, c, d) asm volatile("" : "+a"(a), "+a"(b), "+a"(c), "+a"(d))

#define LD16(d, vo, sb, imm) asm volatile("global_load_dwordx4 %0, %1, %2 offset:%3" : "=v"(d) : "v"(vo), "s"(sb), "i"(imm) : "memory")
#define LD16C(d, vo, sb, imm) asm volatile("global_load_dwordx4 %0, %1, %2 offset:%3 sc1" : "=v"(d) : "v"(vo), "s"(sb), "i"(imm) : "memory")
#define LD16CA(d, vo, sb, imm) asm volatile("global_load_dwordx4 %0, %1, %2 offset:%3 sc1" : "=a"(d) : "v"(vo), "s"(sb), "i"(imm) : "memory")
#define LD8C(d, vo, sb, imm) asm volatile("global_load_dwordx2 %0, %1, %2 offset:%3 sc1" : "=v"(d) : "v"(vo), "s"(sb), "i"(imm) : "memory")
#define LD4(d, vo, sb, imm) asm volatile("global_load_dword %0, %1, %2 offset:%3" : "=v"(d) : "v"(vo), "s"(sb), "i"(imm) : "memory")
#define LD4C(d, vo, sb, imm) asm volatile("global_load_dword %0, %1, %2 offset:%3 sc1" : "=v"(d) : "v"(vo), "s"(sb), "i"(imm) : "memory")
#define ST16C(v, vo, sb, imm) asm volatile("global_store_dwordx4 %0, %1, %2 offset:%3 sc1\n\ts_nop 1" ::"v"(vo), "v"(v), "s"(sb), "i"(imm) : "memory")
#define ST16(v, vo, sb, imm) asm volatile("global_store_dwordx4 %0, %1, %2 offset:%3\n\ts_nop 1" ::"v"(vo), "v"(v), "s"(sb), "i"(imm) : "memory")
#define ST8C(v, vo, sb, imm) asm volatile("global_store_dwordx2 %0, %1, %2 offset:%3 sc1" ::"v"(vo), "v"(v), "s"(sb), "i"(imm) : "memory")
#define ST4C(v, vo, sb, imm) asm volatile("global_store_dword %0, %1, %2 offset:%3 sc1" ::"v"(vo), "v"(v), "s"(sb), "i"(imm) : "memory")
#define NT_T ""
#define NT_G ""
#define NT_W ""
#define TOUCHN(vo, sb) asm volatile("global_load_dword %0, %1, %2" NT_T : "+v"(sink) : "v"(vo), "s"(sb) : "memory")
#define TOUCH(vo, sb) asm volatile("global_load_dword %0, %1, %2" : "+v"(sink) : "v"(vo), "s"(sb) : "memory")
#define TOUCHC(vo, sb) asm volatile("global_load_dword %0, %1, %2 sc1" : "+v"(sink) : "v"(vo), "s"(sb) : "memory")
#define COUNT(i)
__device__ __forceinline__ void st_flag(unsigned char* p, unsigned tag) {
  const unsigned z = 0;
  asm volatile("s_mov_b64 exec, 1\n\tglobal_store_dword %0, %1, %2 sc1\n\ts_mov_b64 exec, -1" ::"v"(z), "v"(tag), "s"(p) : "memory");
}
#define GLDSW(vo, sb, lds, imm) asm volatile("s_mov_b32 m0, %2\n\ts_nop 0\n\tglobal_load_lds_dwordx4 %0, %1 offset:%3" NT_W ::"v"(vo), "s"(sb), "s"(lds), "i"(imm) : "memory")
#define GLDS(vo, sb, lds, imm) asm volatile("s_mov_b32 m0, %2\n\ts_nop 0\n\tglobal_load_lds_dwordx4 %0, %1 offset:%3" NT_G ::"v"(vo), "s"(sb), "s"(lds), "i"(imm) : "memory")
#define GLDSC(vo, sb, lds, imm) asm volatile("s_mov_b32 m0, %2\n\ts_nop 0\n\tglobal_load_lds_dwordx4 %0, %1 offset:%3 sc1" ::"v"(vo), "s"(sb), "s"(lds), "i"(imm) : "memory")

__device__ __forceinline__ unsigned sum4(unsigned m, v4u v) { return (m + v[0] + v[1]) + v[2] + v[3]; }
__device__ __forceinline__ void poll_flags(cptr base, unsigned tag, int lane) {
  const unsigned vo = lane * 128;
  for (unsigned spin = 0; spin < SPIN_CAP; ++spin) {
    v2u v;
    asm volatile("global_load_dwordx2 %0, %1, %2 sc1\n\ts_waitcnt vmcnt(0)" : "=&v"(v) : "v"(vo), "s"(base) : "memory");
    if (__all(v[0] == tag && v[1] == tag)) break;
  }
}
__device__ __forceinline__ void reduce4(unsigned char* red, int w, int lane, f32x4 (&acc)[4][4]) {
#pragma unroll
  for (int c = 1; c < 4; ++c) {
    unsigned char* d = red + (((c + w) & 3) * 3 + c - 1) * 4096 + lane * 16;
#pragma unroll
    for (int i = 0; i < 4; ++i) *(f32x4*)(d + i * 1024) = acc[i][c];
  }
  BAR();
  const unsigned char* s = red + w * 12288 + lane * 16;
#pragma unroll
  for (int i = 0; i < 4; ++i)
    acc[i][0] = (acc[i][0] + *(const f32x4*)(s + i * 1024)) + (*(const f32x4*)(s + 4096 + i * 1024) + *(const f32x4*)(s + 8192 + i * 1024));
}

#define CODE_KB 48
#define W1A 12
#define W2A 12
#define W2C 12
#define XT0 19
constexpr int DB = 7, D2 = 4, D4 = 5, FLW_IT = 12, FLW_CHK = 19;

template <int I, int N, class F> __device__ __forceinline__ void static_for(F&& f) {
  if constexpr (I < N) { f(std::integral_constant<int, I>{}); static_for<I + 1, N>(f); }
}
constexpr bool p1_ra(int s) { return s >= 3 && s <= 18; }
constexpr int p1_xbase(int s) { int n = 0; for (int u = XT0; u < s; ++u) n += (u + DB < 32 ? 2 : 6); return n > 41 ? 41 : n; }
constexpr int p1_extra(int s) { return s < XT0 ? 0 : p1_xbase(s + 1) - p1_xbase(s); }
static_assert(p1_xbase(32) == 41 && XT0 >= FLW_CHK && FLW_IT + DB <= FLW_CHK && (FLW_CHK & 3) == 3, "");
constexpr int p1_wait(int it) {
  int posA[48] = {}, posB[40] = {}, n = 0;
  for (int s = 0; s < 48; ++s) posA[s] = -1;
  for (int s = 0; s < DB; ++s) { n += 4; posB[s] = n - 1; }
  if (it < 0) return n;
  for (int s = 0; s < it; ++s) {
    if (p1_ra(s)) { n += 2; posA[s + 13] = n - 1; }
    n += p1_extra(s);
    if (s + DB < 32) { n += 4; posB[s + DB] = n - 1; }
  }
  int req = posB[it];
  if ((it & 3) == 3 && it + 4 < 32 && posA[it + 4] > req) req = posA[it + 4];
  return n - 1 - req;
}
constexpr int p2_w1(int s) { int n = 28 - W1A - 4 * (s - (8 - D2)); return s + D2 < 8 ? 0 : n > 4 ? 4 : n < 0 ? 0 : n; }
constexpr int p2_wait(int k) {
  int n = 0;
  for (int g = k + 1; g < 8 && g < k - 1 + D2; ++g) n += 5;
  for (int s = 0; s < k - 1; ++s) n += p2_w1(s);
  return n + ((k - 1 + D2 < 8) ? 3 : (p2_w1(k - 1) > 2 ? 2 : p2_w1(k - 1)));
}
constexpr int W2B = 56 - W2A - W2C;
constexpr int p3_w2(int s) { int n = W2B - 3 * (s - 5); return s < 5 ? 0 : n > 3 ? 3 : n < 0 ? 0 : n; }
constexpr int p3_ops(int s) { return s < 4 ? 6 : s == 4 ? 4 : p3_w2(s); }
constexpr int p3_wait(int m) {
  int n = m < 4 ? 6 * (3 - m) : 0;
  for (int s = (m < 4 ? 0 : m - 3); s < 2 * m; ++s) n += p3_ops(s);
  return n;
}
constexpr int p4_rec(int g) { return (g == 0 || g == 3 || g == 7 || g == 10) ? 2 : 0; }
constexpr int p4_wait(int k) {
  int n = 0;
  for (int g = k + 1; g < 14 && g < k - 1 + D4; ++g) n += 4 + p4_rec(g);
  return n + ((k - 1 + D4 < 14) ? 2 + p4_rec(k - 1 + D4) : 0);
}
static_assert(W2B >= 0 && W2B <= 33 && W1A >= 12 && W1A <= 28 && W1A % 4 == 0, "");
struct Tab { int v[16]; };
template <int (*F)(int)> constexpr Tab mk(int lo, int hi) { Tab t = {}; for (int k = lo; k < hi; ++k) t.v[k] = F(k); return t; }

__global__ __launch_bounds__(256, 1) void fnet_fused(cptr x, cptr wsre, cptr wsim, cptr wre, cptr wim, cptr w1, cptr w2,
                                                     unsigned char* ws, unsigned char* out) {
  extern __shared__ __attribute__((aligned(16))) unsigned char smem[];
  const int bid = blockIdx.x, p = bid & 7, j = bid >> 3;
  const int ti = 4 * p + (j >> 3), cg = j & 7, t128 = 2 * p + (j >> 4), fs = j & 15;
  const int tid = threadIdx.x, lane = tid & 63, w = __builtin_amdgcn_readfirstlane(tid >> 6), q = lane >> 4, r = lane & 15;
  const unsigned lds0 = (unsigned)(size_t)(LAS unsigned char*)smem;
  const unsigned vl16 = lane * 16, vl4 = lane * 4;
  unsigned long long ta_ = 0, tb_ = 0;asm volatile("s_memrealtime %0\n\ts_waitcnt lgkmcnt(0)" : "=s"(ta_) :: "memory");asm volatile("s_memrealtime %0\n\ts_waitcnt lgkmcnt(0)" : "=s"(tb_) :: "memory");

  const int hb_ = p + 8 * (j >> 1);
  unsigned epoch, sink = 0;
  v4u xr[4];
  {
    const unsigned zero = 0;
    LD4C(epoch, zero, ws + EPOCH_OFF, 0);
  }
  const int mat = (j >> 2) & 1, nt = j & 3;
  auto rows8_out = [&](int off, unsigned vo, unsigned char* sb, unsigned step, int i0, auto NI) {
    const unsigned char* s = smem + off + ((lane >> 2) & 3) * XS_ROW + 64 * (q >> 1) + 16 * (lane & 3) + 8 * (q & 1) + 128 * i0;
#pragma unroll
    for (int i = 0; i < NI.value; ++i) {
      const v2u lo = __builtin_bit_cast(v2u, __builtin_amdgcn_ds_read_tr16_b64_v4i16((LAS v4s*)(s + 128 * i)));
      const v2u hi = __builtin_bit_cast(v2u, __builtin_amdgcn_ds_read_tr16_b64_v4i16((LAS v4s*)(s + 128 * i + 4 * XS_ROW)));
      const v4u o = {lo[0], lo[1], hi[0], hi[1]};
      ST16C(o, vo, sb + (i0 + i) * step, 0);
    }
  };
  if ((j & 1) == 0) {
    const unsigned vo = q * 1024 + r * 16;
    cptr xb = x + (size_t)(16 * hb_ + 4 * w) * 1024;
    asm volatile("global_load_dwordx4 %0, %4, %5\n\tglobal_load_dwordx4 %1, %4, %5 offset:256\n\t"
                 "global_load_dwordx4 %2, %4, %5 offset:512\n\tglobal_load_dwordx4 %3, %4, %5 offset:768\n\ts_waitcnt vmcnt(0)"
                 : "=&v"(xr[0]), "=&v"(xr[1]), "=&v"(xr[2]), "=&v"(xr[3]) : "v"(vo), "s"(xb) : "memory");

    float v[32];
#pragma unroll
    for (int i = 0; i < 4; ++i)
#pragma unroll
      for (int k = 0; k < 4; ++k) { v[8 * i + 2 * k] = bflo(xr[i][k]); v[8 * i + 2 * k + 1] = bfhi(xr[i][k]); }
    float s1[4] = {0.f, 0.f, 0.f, 0.f};
#pragma unroll
    for (int k = 0; k < 32; ++k) s1[k & 3] += v[k];
    const float mean = row16_sum((s1[0] + s1[1]) + (s1[2] + s1[3])) * (1.0f / D_);
    float s2[4] = {0.f, 0.f, 0.f, 0.f};
#pragma unroll
    for (int k = 0; k < 32; ++k) { const float c = v[k] - mean; s2[k & 3] += c * c; }
    const float mb = rbf(mean), rd = ln_rstd(row16_sum((s2[0] + s2[1]) + (s2[2] + s2[3])) * (1.0f / D_));
#pragma unroll
    for (int i = 0; i < 4; ++i) {
      v4u o;
#pragma unroll
      for (int k = 0; k < 4; ++k) {
        const unsigned c2 = pk2(v[8 * i + 2 * k] - mb, v[8 * i + 2 * k + 1] - mb);
        o[k] = pk2(bflo(c2) * rd, bfhi(c2) * rd);
      }
      *(v4u*)(smem + XS_OFF + (4 * w + q) * XS_ROW + r * 16 + i * 256) = o;
    }
    BAR();

    const int oc = 2 * hb_ + (w >> 1);
    rows8_out(XS_OFF + (w >> 1) * 8 * XS_ROW, (q >> 1) * 131072 + (q & 1) * 1024 + r * 16, ws + XNT_OFF + (oc >> 2) * 2048 + (oc & 3) * 256, 262144,
              4 * (w & 1), std::integral_constant<int, 4>{});
  } else {
    const int u = 2 * hb_ + (w & 1), kb1 = u >> 4, fs1 = u & 15, ob = u < 224 ? u : 223, kbd = u & 31;
    const int so = w * 16384;
    if (w == 3) {
      cptr pc;
      asm volatile("s_getpc_b64 %0" : "=s"(pc));
      const unsigned vo = ((j >> 1) * (CODE_KB / 2) + (lane < CODE_KB / 2 ? lane : 0)) * 128;
      TOUCH(vo, pc);
    }
    if (w < 2) {
#pragma unroll
      for (int k = 0; k < 8; ++k) GLDSW(vl16, w2 + (size_t)(8 * ob + k) * 1024, lds0 + so + k * XS_ROW, 0);
      const unsigned vo = (8 * (r >> 2) + (r & 3)) * 1024 + q * 16;
      cptr sb = (kbd < 16 ? wre : wim) + (size_t)(64 * (u >> 5)) * 1024 + (kbd & 15) * 64;
      GLDSW(vo, sb, lds0 + so + 12288, 0);
      GLDSW(vo, sb + 4096, lds0 + so + 13312, 0);
      GLDSW(vo, sb + 32768, lds0 + so + 14336, 0);
      GLDSW(vo, sb + 36864, lds0 + so + 15360, 0);
    } else {
      const unsigned vo = q * (F_ * 2) + r * 16;
      cptr sb = w1 + (size_t)(32 * kb1) * (F_ * 2) + ((7 * fs1) >> 1) * 64;
#pragma unroll
      for (int k = 0; k < 8; ++k) GLDSW(vo, sb + k * (4 * F_ * 2), lds0 + so + k * 1024, 0);
    }
    {
      const unsigned vo = (lane >> 3) * 4096 + (lane & 7) * 128;
      cptr sb = (mat ? wsim : wsre) + (size_t)(64 * ti + 32 * (nt >> 1) + 8 * w) * 4096;
#pragma unroll
      for (int k = 0; k < NTOUCH; ++k) TOUCHN(vo, sb + k * 1024);
    }
    WAITV(NTOUCH);
    if (w < 2) {
      rows8_out(so, q * 1024 + r * 16, ws + W2_OFF + (ob >> 2) * 4096 + (ob & 3) * 256, 229376, 0, std::integral_constant<int, 8>{});
#pragma unroll
      for (int i = 0; i < 4; ++i) {
        v4u o = *(const v4u*)(smem + so + 12288 + i * 1024 + vl16);
        if (kbd >= 16) o ^= 0x80008000u;
        ST16C(o, vl16, ws + WD_OFF + u * 4096 + i * 1024, 0);
      }
    } else {
      const unsigned char* s = smem + so + (8 * q + ((lane >> 2) & 3)) * 256 + 16 * (lane & 3);
      unsigned char* sb = ws + W1_OFF + fs1 * 114688 + kb1 * 7168;
      float csv = 0.f, csw = 0.f;
#pragma unroll
      for (int l = 0; l < 7; ++l) {
        const int tl = (l < 6) ? 2 * (fs1 & 1) + l : ((fs1 & 1) ? 1 : 6);
        const unsigned char* sl = s + (tl >> 1) * 64 + (tl & 1) * 8;
        const v2u lo = __builtin_bit_cast(v2u, __builtin_amdgcn_ds_read_tr16_b64_v4i16((LAS v4s*)sl));
        const v2u hi = __builtin_bit_cast(v2u, __builtin_amdgcn_ds_read_tr16_b64_v4i16((LAS v4s*)(sl + 1024)));
        const v4u o = {lo[0], lo[1], hi[0], hi[1]};
        ST16C(o, vl16, sb + l * 1024, 0);
        const float cs = rows_sum(((bflo(o[0]) + bfhi(o[0])) + (bflo(o[1]) + bfhi(o[1]))) + ((bflo(o[2]) + bfhi(o[2])) + (bflo(o[3]) + bfhi(o[3]))));
        if (l < 4) csv = (q == l) ? cs : csv; else csw = (q == l - 4 || (l == 6 && q == 3)) ? cs : csw;
      }
      unsigned char* cb = ws + CS_OFF + (kb1 * 16 + fs1) * 512;
      ST4C(csv, vl4, cb, 0);
      ST4C(csw, vl4, cb, 256);
    }
  }
  WAITV_BAR(0);
  asm volatile("" : "+v"(epoch), "+v"(sink));
  const unsigned tag = __builtin_amdgcn_readfirstlane(epoch) + 1u;

  const unsigned va0 = (lane >> 3) * 4096 + (((lane & 7) ^ (lane >> 4)) << 4), va1 = (va0 ^ 64) + 32768 - 1024;
  cptr abase = (mat ? wsim : wsre) + (size_t)(64 * ti + 16 * w) * 4096;
  const unsigned lda = lds0 + w * 2048;
#define A_STAGE0(s) GLDS(va0, abase + (s) * 128, lda + ((s) & 15) * 8192, 0)
#define A_STAGE1(s) GLDS(va1, abase + (s) * 128, lda + ((s) & 15) * 8192, 1024)
  if (w == 0) {
    st_flag(ws + ((j & 1) ? FLAGW_OFF : FLAG0_OFF) + (hb_ & 63) * 128 + (hb_ >> 6) * 4, tag);
    poll_flags(ws + FLAG0_OFF, tag, lane);
  } else {
#pragma unroll 1
    for (int s = w - 1; s < 16; s += 3) {
      cptr sb = abase - w * 65536 + s * 128;
      const unsigned l = lds0 + s * 8192;
#pragma unroll
      for (int i = 0; i < 4; ++i) {
        GLDS(va0, sb + i * 65536, l + i * 2048, 0);
        GLDS(va1, sb + i * 65536, l + i * 2048, 1024);
      }
    }
  }
  BAR();

  cptr bbase = ws + XNT_OFF + (4 * nt + w) * 131072;
  v4u bf[8][4];
#define B_LOAD(s, i) LD16CA(bf[(s) & 7][i], vl16, bbase + (s) * 4096, (i) * 1024)
#pragma unroll
  for (int s = 0; s < DB; ++s) { B_LOAD(s, 0); B_LOAD(s, 1); B_LOAD(s, 2); B_LOAD(s, 3); }
  f32x4 acc1[2][4];
#pragma unroll
  for (int i = 0; i < 2; ++i)
#pragma unroll
    for (int t = 0; t < 4; ++t) acc1[i][t] = f32x4{0.f, 0.f, 0.f, 0.f};
  const unsigned ra0 = r * 128 + ((q ^ (r >> 1)) << 4), ra1 = ra0 ^ 64;
  v4u af[2][8];
  v4u xres[2], csr[7], wd[8][4];
  v2u flw = {tag, tag};
  const unsigned vl128 = lane * 128;
  cptr xb2 = x + (size_t)(64 * ti + 16 * w) * 1024 + cg * 128;
  const unsigned vx = r * 1024 + q * 16;
  cptr cb2 = ws + CS_OFF + fs * 512;
  const unsigned vc2 = r * 8192 + q * 16;
  cptr wb = ws + WD_OFF + cg * 131072 + w * 32768;
  WAITV_BAR(p1_wait(-1));
#pragma unroll
  for (int t = 0; t < 4; ++t) { af[0][t] = *(const v4u*)(smem + ra0 + t * 2048); af[0][4 + t] = *(const v4u*)(smem + ra1 + t * 2048); }

#define P1_EXTRA(e)                                                                                                     \
  if constexpr ((e) < p1_extra(it)) {                                                                                   \
    constexpr int n_ = p1_xbase(it) + (e);                                                                              \
    if constexpr (n_ < 32) LD16C(wd[n_ >> 2][n_ & 3], vl16, wb + (n_ >> 2) * 4096, (n_ & 3) * 1024);                    \
    else if constexpr (n_ < 34) LD16(xres[n_ - 32], vx, xb2, (n_ - 32) * 64);                                           \
    else LD16C(csr[n_ - 34], vc2, cb2, (n_ - 34) * 64);                                                                 \
  }
#define MM1(i, t, kb) acc1[i][t] = mfma(bf[U][2 * (kb) + (i)], af[C][4 * (kb) + (t)], acc1[i][t]); SCHED()
#define RD1(n) if constexpr (RN) { af[Nx][n] = *(const v4u*)(sn + ((n) < 4 ? ra0 : ra1) + ((n) & 3) * 2048); SCHED(); }
  static_for<0, 32>([&](auto IT) {
    constexpr int it = IT, U = it & 7, C = it & 1, Nx = C ^ 1;
    constexpr bool RA = p1_ra(it), RB = it + DB < 32, RN = it + 1 < 32;
    if constexpr (it == FLW_CHK) {
      asm volatile("s_waitcnt vmcnt(%1)" : "+v"(flw) : "i"(p1_wait(it)) : "memory");
      if (!__all(flw[0] == tag && flw[1] == tag)) { COUNT(0); poll_flags(ws + FLAGW_OFF, tag, lane); }
      if (bid == 0 && w == 0) st_flag(ws + EPOCH_OFF, tag);
    }
    if constexpr ((it & 3) == 3) { WAITV_BAR(p1_wait(it)); } else { WAITV(p1_wait(it)); }
    PIN4A(bf[U][0], bf[U][1], bf[U][2], bf[U][3]);
    if constexpr (it == FLW_IT)
      asm volatile("s_cmp_lg_u32 %3, 0\n\ts_cbranch_scc1 1f\n\tglobal_load_dwordx2 %0, %1, %2 sc1\n1:"
                   : "+v"(flw) : "v"(vl128), "s"(ws + FLAGW_OFF), "s"(w) : "memory", "scc");
    const unsigned char* sn = smem + ((it + 1) & 15) * 8192;
    SCHED();
    MM1(0, 0, 0); RD1(0);
    MM1(1, 0, 0); if constexpr (RA) A_STAGE0(it + 13); P1_EXTRA(0) SCHED();
    MM1(0, 1, 0); RD1(1);
    MM1(1, 1, 0); RD1(2);
    MM1(0, 2, 0); if constexpr (RA) A_STAGE1(it + 13); P1_EXTRA(1) SCHED();
    MM1(1, 2, 0); RD1(3);
    MM1(0, 3, 0); if constexpr (RB) B_LOAD(it + DB, 0); P1_EXTRA(2) SCHED();
    MM1(1, 3, 0); RD1(4);
    MM1(0, 0, 1); RD1(5);
    MM1(1, 0, 1); if constexpr (RB) B_LOAD(it + DB, 1); P1_EXTRA(3) SCHED();
    MM1(0, 1, 1); RD1(6);
    MM1(1, 1, 1); if constexpr (RB) B_LOAD(it + DB, 2); P1_EXTRA(4) SCHED();
    MM1(0, 2, 1); RD1(7);
    MM1(1, 2, 1); if constexpr (RB) B_LOAD(it + DB, 3); P1_EXTRA(5) SCHED();
    MM1(0, 3, 1);
    MM1(1, 3, 1);
  });

  {
    const int kbq = ti * 32 + 16 * mat + 4 * nt + w;
    unsigned char* pq = ws + PQ_OFF + kbq * 4096;
    unsigned ck = tag;
#pragma unroll
    for (int t = 0; t < 4; ++t) {
      const v4u o = {pk2(acc1[0][t][0], acc1[0][t][1]), pk2(acc1[0][t][2], acc1[0][t][3]),
                     pk2(acc1[1][t][0], acc1[1][t][1]), pk2(acc1[1][t][2], acc1[1][t][3])};
      ST16C(o, vl16, pq + t * 1024, 0);
      ck = sum4(ck, o);
    }
    ST4C(ck, vl4, ws + CKP_OFF + kbq * 256, 0);
  }
  cptr w1src = ws + W1_OFF + fs * 114688 + w * 28672;
  const unsigned w1dst = lds0 + w * 28672;
#define W1L(n) GLDSC(vl16, w1src + ((n) >> 2) * 4096, w1dst + ((n) >> 2) * 4096, ((n) & 3) * 1024)
#pragma unroll 1
  for (int n = 0; n < W1A / 4; ++n) {
    GLDSC(vl16, w1src + n * 4096, w1dst + n * 4096, 0);
    GLDSC(vl16, w1src + n * 4096, w1dst + n * 4096, 1024);
    GLDSC(vl16, w1src + n * 4096, w1dst + n * 4096, 2048);
    GLDSC(vl16, w1src + n * 4096, w1dst + n * 4096, 3072);
  }

#define BAD(M) (__builtin_amdgcn_ballot_w64((M) != 0u) != 0)
#define SUM16(M0, F) sum4(sum4(sum4(sum4(M0, F[0]), F[1]), F[2]), F[3])
#define LOAD4_WAIT(F, SB)                                                                                               \
  asm volatile("global_load_dwordx4 %0, %4, %8 sc1\n\tglobal_load_dwordx4 %1, %5, %8 sc1\n\t"                           \
               "global_load_dwordx4 %2, %6, %8 sc1\n\tglobal_load_dwordx4 %3, %7, %8 sc1\n\ts_waitcnt vmcnt(0)"         \
               : "=&v"(F[0]), "=&v"(F[1]), "=&v"(F[2]), "=&v"(F[3])                                                     \
               : "v"(vrot[0]), "v"(vrot[1]), "v"(vrot[2]), "v"(vrot[3]), "s"(SB) : "memory")
#define STEP64(WT, FC, FN, HASN, WAITN, M0, OP0, OP1, OP2, OP3)                                                         \
  {                                                                                                                     \
    SCHED();                                                                                                            \
    acc[0][0] = mfma(WT[0], FC[0], acc[0][0]); SCHED(); OP0; SCHED();                                                   \
    acc[0][1] = mfma(WT[0], FC[1], acc[0][1]); acc[0][2] = mfma(WT[0], FC[2], acc[0][2]);                               \
    acc[0][3] = mfma(WT[0], FC[3], acc[0][3]); acc[1][0] = mfma(WT[1], FC[0], acc[1][0]); SCHED(); OP1; SCHED();        \
    acc[1][1] = mfma(WT[1], FC[1], acc[1][1]); SCHED();                                                                 \
    if (HASN) { WAITN; }                                                                                                \
    SCHED();                                                                                                            \
    acc[1][2] = mfma(WT[1], FC[2], acc[1][2]); SCHED(); if (HASN) mx = (M0); SCHED();                                   \
    acc[1][3] = mfma(WT[1], FC[3], acc[1][3]); SCHED(); if (HASN) mx = sum4(mx, FN[0]); SCHED();                        \
    acc[2][0] = mfma(WT[2], FC[0], acc[2][0]); SCHED(); OP2; SCHED();                                                   \
    acc[2][1] = mfma(WT[2], FC[1], acc[2][1]); SCHED(); if (HASN) mx = sum4(mx, FN[1]); SCHED();                        \
    acc[2][2] = mfma(WT[2], FC[2], acc[2][2]); SCHED(); if (HASN) mx = sum4(mx, FN[2]); SCHED();                        \
    acc[2][3] = mfma(WT[2], FC[3], acc[2][3]); SCHED(); if (HASN) mx = sum4(mx, FN[3]); SCHED();                        \
    acc[3][0] = mfma(WT[3], FC[0], acc[3][0]); SCHED(); OP3; SCHED();                                                   \
    acc[3][1] = mfma(WT[3], FC[1], acc[3][1]); acc[3][2] = mfma(WT[3], FC[2], acc[3][2]);                               \
    acc[3][3] = mfma(WT[3], FC[3], acc[3][3]); SCHED();                                                                 \
  }

  unsigned vrot[4];
#pragma unroll
  for (int c = 0; c < 4; ++c) vrot[c] = vl16 + ((c + w) & 3) * 1024;
  v4u hres[2];
  v4u w2f[14][4];
  cptr w2base = ws + W2_OFF + cg * 229376 + w * 57344;
#define W2L(n) do { if ((n) < 44) LD16CA(w2f[(n) >> 2][(n) & 3], vl16, w2base + ((n) >> 2) * 4096, ((n) & 3) * 1024);   \
                    else LD16C(w2f[(n) >> 2][(n) & 3], vl16, w2base + ((n) >> 2) * 4096, ((n) & 3) * 1024); } while (0)
  {
    constexpr Tab p2t = mk<p2_wait>(1, 8), p2w = mk<p2_w1>(0, 8);
    f32x4 acc[4][4];
    v4u pq[8][4];
    unsigned ck[8];
    cptr pb = ws + PQ_OFF + ti * 131072 + w * 32768;
    cptr cb = ws + CKP_OFF + (ti * 32 + 8 * w) * 256;
#pragma unroll
    for (int k = 0; k < D2; ++k) {
      LD4C(ck[k], vl4, cb, k * 256);
#pragma unroll
      for (int c = 0; c < 4; ++c) LD16C(pq[k][c], vrot[c], pb + k * 4096, 0);
    }
#pragma unroll
    for (int i = 0; i < 4; ++i)
#pragma unroll
      for (int c = 0; c < 4; ++c) acc[i][c] = f32x4{0.f, 0.f, 0.f, 0.f};
#define P2WAIT(g, N) asm volatile("s_waitcnt vmcnt(%5)" : "+v"(pq[g][0]), "+v"(pq[g][1]), "+v"(pq[g][2]), "+v"(pq[g][3]), "+v"(ck[g]) : "i"(N) : "memory")
    P2WAIT(0, 5 * D2 - 5);
    unsigned mx = SUM16(tag - ck[0], pq[0]);

#define P2OP(i) do { if (k + D2 < 8) { if ((i) == 0) LD4C(ck[(k + D2) & 7], vl4, cb, ((k + D2) & 7) * 256);              \
                                       LD16C(pq[(k + D2) & 7][i], vrot[i], pb + (k + D2) * 4096, 0); }                  \
                     else if ((i) < p2w.v[k]) W1L(W1A + 4 * (k + D2 - 8) + (i)); } while (0)
#pragma unroll
    for (int k = 0; k < 8; ++k) {
      if (__builtin_expect(BAD(mx), 0)) {
        unsigned spin = SPIN_CAP;
        WAITV(0);
        do {
          COUNT(1);
          unsigned c_;
          LD4C(c_, vl4, cb, k * 256);
          LOAD4_WAIT(pq[k], pb + k * 4096);
          asm volatile("" : "+v"(c_));
          if (!BAD(SUM16(tag - c_, pq[k]))) spin = 1;
        } while (--spin);
      }
      PIN4(wd[k][0], wd[k][1], wd[k][2], wd[k][3]);
      STEP64(wd[k], pq[k], pq[(k + 1) & 7], k + 1 < 8, P2WAIT((k + 1) & 7, p2t.v[(k + 1) & 7]), tag - ck[(k + 1) & 7], P2OP(0), P2OP(1), P2OP(2), P2OP(3));
    }

    reduce4(smem + RED_OFF, w, lane, acc);
    asm volatile("" : "+v"(xres[0]), "+v"(xres[1]));
    float hv[16];
#pragma unroll
    for (int g = 0; g < 2; ++g) {
#pragma unroll
      for (int h = 0; h < 2; ++h) {
        const f32x4 yy = acc[2 * g + h][0];
        hres[g][2 * h] = pk2(bflo(xres[g][2 * h]) + yy[0], bfhi(xres[g][2 * h]) + yy[1]);
        hres[g][2 * h + 1] = pk2(bflo(xres[g][2 * h + 1]) + yy[2], bfhi(xres[g][2 * h + 1]) + yy[3]);
      }
#pragma unroll
      for (int k = 0; k < 4; ++k) { hv[8 * g + 2 * k] = bflo(hres[g][k]); hv[8 * g + 2 * k + 1] = bfhi(hres[g][k]); }
    }
    const int hp = (4 * ti + w) * 8 + cg;
    ST16C(hres[0], vl16, ws + H_OFF + hp * 2048, 0);
    ST16C(hres[1], vl16, ws + H_OFF + hp * 2048, 1024);
    const unsigned hck = sum4(sum4(tag, hres[0]), hres[1]);
    ST4C(hck, vl4, ws + CKH_OFF + hp * 256, 0);
    float s = 0.f;
#pragma unroll
    for (int k = 0; k < 16; ++k) s += hv[k];
    const float m = rows_sum(s) * (1.0f / 64.0f);
    float m2 = 0.f;
#pragma unroll
    for (int k = 0; k < 16; ++k) { const float c = hv[k] - m; m2 += c * c; }
    m2 = rows_sum(m2);
    const unsigned mu = __builtin_bit_cast(unsigned, m), m2u = __builtin_bit_cast(unsigned, m2);
    const v4u st = {mu, m2u, tag + mu + m2u, 0u};
    asm volatile("s_mov_b64 exec, 0xffff\n\tglobal_store_dwordx4 %0, %1, %2 sc1\n\ts_mov_b64 exec, -1\n\ts_nop 0" ::"v"(vl16), "v"(st), "s"(ws + ST_OFF + hp * 256) : "memory");
  }

#pragma unroll
  for (int n = 0; n < W2A; ++n) W2L(n);
  f32x4 cs[7];
#pragma unroll
  for (int l = 0; l < 7; ++l) {
    asm volatile("" : "+v"(csr[l]));
#pragma unroll
    for (int k = 0; k < 4; ++k) cs[l][k] = row16_sum(asf(csr[l][k]));
  }

  {
    constexpr Tab p3t = mk<p3_wait>(0, 8), p3w = mk<p3_w2>(0, 16);
    v4u hf[8][4];
    unsigned ck[8][2];
    v4u str[2][2];
    cptr hb = ws + H_OFF + (8 * t128 + 2 * w) * 16384;
    cptr cb = ws + CKH_OFF + (8 * t128 + 2 * w) * 2048;
    const unsigned vh1 = vl16 + 16384;
    const unsigned vs = q * 512 + r * 16;
    cptr stb = ws + ST_OFF + (8 * t128 + 2 * w) * 2048;
    const unsigned tag2 = 2u * tag;
#define HL(m, i) LD16C(hf[m][i], ((i) & 1) ? vh1 : vl16, hb + ((m) >> 1) * 4096, ((m) & 1) * 2048 + ((i) >> 1) * 1024)
#define HC(m, t) LD4C(ck[m][t], vl4, cb, (t) * 2048 + (m) * 256)
#pragma unroll
    for (int m = 0; m < 4; ++m) { HC(m, 0); HC(m, 1); HL(m, 0); HL(m, 1); HL(m, 2); HL(m, 3); }
    f32x4 acc[7][2];
#pragma unroll
    for (int l = 0; l < 7; ++l) { acc[l][0] = f32x4{0.f, 0.f, 0.f, 0.f}; acc[l][1] = f32x4{0.f, 0.f, 0.f, 0.f}; }
#define P3WAIT(m, N, TAIL) asm volatile("s_waitcnt vmcnt(%6)" TAIL : "+v"(hf[m][0]), "+v"(hf[m][1]), "+v"(hf[m][2]), "+v"(hf[m][3]), "+v"(ck[m][0]), "+v"(ck[m][1]) : "i"(N) : "memory")
    P3WAIT(0, p3t.v[0], " lgkmcnt(0)\n\ts_barrier");

    unsigned whi_ = lds0 + vl16 + 57344;
    asm volatile("" : "+v"(whi_));
    const LAS unsigned char* wlo = (const LAS unsigned char*)smem + vl16;
    const LAS unsigned char* whi = (const LAS unsigned char*)(size_t)whi_;
    v4u wf[2][7];
#pragma unroll
    for (int l = 0; l < 7; ++l) wf[0][l] = *(const LAS v4u*)(wlo + l * 1024);
    unsigned mx = SUM16(tag2 - ck[0][0] - ck[0][1], hf[0]);
#define P3OP(i) do { if (k < 4) { if ((i) < 2) HC((4 + k) & 7, (i) & 1); else HL((4 + k) & 7, ((i) - 2) & 3); }          \
                     else if (k == 4) { if ((i) < 4) LD16C(str[((i) >> 1) & 1][(i) & 1], vs, stb, (((i) >> 1) & 1) * 2048 + ((i) & 1) * 256); } \
                     else if ((i) < p3w.v[k]) W2L(W2A + 3 * (k - 5) + (i)); } while (0)
#pragma unroll
    for (int k = 0; k < 16; ++k) {
      const int m = k >> 1, mn = ((k + 1) >> 1) & 7;
      if ((k & 1) == 0 && __builtin_expect(BAD(mx), 0)) {
        unsigned spin = SPIN_CAP;
        WAITV(0);
        do {
          COUNT(2);
          unsigned c0_, c1_;
          asm volatile("global_load_dword %4, %8, %10 sc1\n\tglobal_load_dword %5, %8, %10 offset:2048 sc1\n\t"
                       "global_load_dwordx4 %0, %6, %9 sc1\n\tglobal_load_dwordx4 %1, %7, %9 sc1\n\t"
                       "global_load_dwordx4 %2, %6, %9 offset:1024 sc1\n\tglobal_load_dwordx4 %3, %7, %9 offset:1024 sc1\n\ts_waitcnt vmcnt(0)"
                       : "=&v"(hf[m][0]), "=&v"(hf[m][1]), "=&v"(hf[m][2]), "=&v"(hf[m][3]), "=&v"(c0_), "=&v"(c1_)
                       : "v"(vl16), "v"(vh1), "v"(vl4), "s"(hb + m * 2048), "s"(cb + m * 256) : "memory");
          if (!BAD(SUM16(tag2 - c0_ - c1_, hf[m]))) spin = 1;
        } while (--spin);
      }
      const LAS unsigned char* wn = (k + 1 < 8 ? wlo + (k + 1) * 7168 : whi + (k + 1 - 8) * 7168);
      SCHED();
#pragma unroll
      for (int l = 0; l < 7; ++l) {
        acc[l][0] = mfma(wf[k & 1][l], hf[m][2 * (k & 1)], acc[l][0]);
        SCHED();
        if (k + 1 < 16) wf[(k + 1) & 1][l] = *(const LAS v4u*)(wn + l * 1024);
        if ((k & 1) && k + 1 < 16 && k >= 4) {
          if (l == 3) P3WAIT(mn, p3t.v[mn], "");
          if (l == 4) mx = sum4(tag2 - ck[mn][0] - ck[mn][1], hf[mn][0]);
          if (l == 5) mx = sum4(mx, hf[mn][2]);
        }
        SCHED();
        acc[l][1] = mfma(wf[k & 1][l], hf[m][2 * (k & 1) + 1], acc[l][1]);
        SCHED();
        if (l < 6) P3OP(l);
        if ((k & 1) && k + 1 < 16) {
          if (k >= 4) {
            if (l == 4) mx = sum4(mx, hf[mn][1]);
            if (l == 5) mx = sum4(mx, hf[mn][3]);
          } else if (l == 5) {
            P3WAIT(mn, p3t.v[mn], "");
            mx = SUM16(tag2 - ck[mn][0] - ck[mn][1], hf[mn]);
          }
        }
        SCHED();
      }
    }

    asm volatile("s_waitcnt vmcnt(%4)" : "+v"(str[0][0]), "+v"(str[0][1]), "+v"(str[1][0]), "+v"(str[1][1]) : "i"(W2B) : "memory");
    for (unsigned spin = 0; spin < SPIN_CAP; ++spin) {
#define STCK(a) (tag + (a)[0] + (a)[1] - (a)[2])
      if (!BAD(STCK(str[0][0]) | STCK(str[0][1]) | STCK(str[1][0]) | STCK(str[1][1]))) break;
      asm volatile("global_load_dwordx4 %0, %4, %5 sc1\n\tglobal_load_dwordx4 %1, %4, %5 offset:256 sc1\n\t"
                   "global_load_dwordx4 %2, %4, %5 offset:2048 sc1\n\tglobal_load_dwordx4 %3, %4, %5 offset:2304 sc1\n\ts_waitcnt vmcnt(0)"
                   : "+v"(str[0][0]), "+v"(str[0][1]), "+v"(str[1][0]), "+v"(str[1][1]) : "v"(vs), "s"(stb) : "memory");
    }
    const int kbp = (7 * fs + (fs & 1)) >> 1, taus = 7 * fs + ((fs & 1) ? 0 : 6);
    const int t64 = 2 * t128 + (w >> 1);
    v4u fck = {tag, tag, tag, tag};
#pragma unroll
    for (int t = 0; t < 2; ++t) {
      const float m0 = asf(str[t][0][0]), m1 = asf(str[t][1][0]);
      const float m = rows_sum(m0 + m1) * 0.125f;
      const float da = m0 - m, db = m1 - m;
      const float m2 = rows_sum(asf(str[t][0][1]) + asf(str[t][1][1]) + 64.0f * (da * da + db * db));
      const float mb = rbf(m), rd = ln_rstd(m2 * (1.0f / D_));
      unsigned char* fb = ws + FF_OFF + t64 * 229376 + (2 * (w & 1) + t) * 1024;
      v2u o[7];
#pragma unroll
      for (int l = 0; l < 7; ++l) {
        const f32x4 v = (acc[l][t] - mb * cs[l]) * rd;
        o[l][0] = pk2(relu(v[0]), relu(v[1]));
        o[l][1] = pk2(relu(v[2]), relu(v[3]));
      }
#pragma unroll
      for (int m_ = 0; m_ < 3; ++m_) {
        const v4u oo = {o[2 * m_][0], o[2 * m_][1], o[2 * m_ + 1][0], o[2 * m_ + 1][1]};
        ST16C(oo, vl16, fb + (kbp + m_) * 4096, 0);
        fck[m_] = sum4(fck[m_], oo);
      }
      ST8C(o[6], vl16, fb + (taus >> 1) * 4096 + (taus & 1) * 8, 0);
      fck[3] += o[6][0] + o[6][1];
    }
    ST16C(fck, vl16, ws + CKF_OFF + ((t64 * 16 + fs) * 2 + (w & 1)) * 1024, 0);
  }

#pragma unroll
  for (int n = 56 - W2C; n < 56; ++n) W2L(n);

  {
    constexpr Tab p4t = mk<p4_wait>(1, 14);
    f32x4 acc[4][4];
    constexpr int R = D4 + 1;
    v4u ff[R][4];
    v4u rec[4][2];
    cptr fb = ws + FF_OFF + ti * 229376 + w * 57344;
    cptr cb = ws + CKF_OFF + (ti * 16 + 4 * w) * 2048;
    const unsigned tag2 = 2u * tag;
#define RECB(g) ((g) == 0 ? 0 : (g) == 3 ? 1 : (g) == 7 ? 2 : 3)
#define RECL(b) { LD16C(rec[b][0], vl16, cb + ((b) >> 1) * 4096, ((b) & 1) * 2048); LD16C(rec[b][1], vl16, cb + ((b) >> 1) * 4096, ((b) & 1) * 2048 + 1024); }
#define RB0(g) (2 * ((g) / 7) + ((g) % 7 > 3 ? 1 : 0))
#define RC0(g) ((g) % 7 > 3 ? (g) % 7 - 4 : (g) % 7)
#define M4(RE, g) ((g) % 7 == 3 ? 2u * tag2 - RE[RB0(g)][0][3] - RE[RB0(g)][1][3] - RE[RB0(g) + 1][0][3] - RE[RB0(g) + 1][1][3]   \
                                : tag2 - RE[RB0(g)][0][RC0(g)] - RE[RB0(g)][1][RC0(g)])
#pragma unroll
    for (int k = 0; k < D4; ++k) {
      if (p4_rec(k)) RECL(RECB(k));
#pragma unroll
      for (int c = 0; c < 4; ++c) LD16C(ff[k][c], vrot[c], fb + k * 4096, 0);
    }
#pragma unroll
    for (int i = 0; i < 4; ++i)
#pragma unroll
      for (int c = 0; c < 4; ++c) acc[i][c] = f32x4{0.f, 0.f, 0.f, 0.f};
#define P4WAIT(g, N) do { asm volatile("s_waitcnt vmcnt(%4)" : "+v"(ff[(g) % R][0]), "+v"(ff[(g) % R][1]), "+v"(ff[(g) % R][2]), "+v"(ff[(g) % R][3]) : "i"(N) : "memory"); \
                          if (p4_rec(g)) asm volatile("" : "+v"(rec[RECB(g)][0]), "+v"(rec[RECB(g)][1])); } while (0)
    P4WAIT(0, 4 * D4 - 4 + 2);
    unsigned mx = SUM16(M4(rec, 0), ff[0]);

#define P4OP(i) do { if (k + D4 < 14) { if ((i) == 0 && p4_rec(k + D4)) RECL(RECB(k + D4));                              \
                                        LD16C(ff[(k + D4) % R][i], vrot[i], fb + (k + D4) * 4096, 0); } } while (0)
#pragma unroll
    for (int k = 0; k < 14; ++k) {
      if (__builtin_expect(BAD(mx), 0)) {
        unsigned spin = SPIN_CAP;
        WAITV(0);
        do {
          COUNT(3);
          RECL(RB0(k));
          if (k % 7 == 3) RECL(RB0(k) + 1);
          LOAD4_WAIT(ff[k % R], fb + k * 4096);
          asm volatile("" : "+v"(rec[RB0(k)][0]), "+v"(rec[RB0(k)][1]));
          if (k % 7 == 3) asm volatile("" : "+v"(rec[(RB0(k) + 1) & 3][0]), "+v"(rec[(RB0(k) + 1) & 3][1]));
          if (!BAD(SUM16(M4(rec, k), ff[k % R]))) spin = 1;
        } while (--spin);
      }
      if (k < 11) PIN4A(w2f[k][0], w2f[k][1], w2f[k][2], w2f[k][3]);
      else PIN4(w2f[k][0], w2f[k][1], w2f[k][2], w2f[k][3]);
      STEP64(w2f[k], ff[k % R], ff[(k + 1) % R], k + 1 < 14, P4WAIT((k + 1) % 14, p4t.v[(k + 1) % 14]), M4(rec, (k + 1) % 14), P4OP(0), P4OP(1), P4OP(2), P4OP(3));
    }
    reduce4(smem + RED_OFF, w, lane, acc);
    unsigned char* ob_ = out + (size_t)(64 * ti + 16 * w) * 1024 + cg * 128;
#pragma unroll
    for (int g = 0; g < 2; ++g) {
      v4u o;
#pragma unroll
      for (int h = 0; h < 2; ++h) {
        const f32x4 yy = acc[2 * g + h][0];
        o[2 * h] = pk2(bflo(hres[g][2 * h]) + yy[0], bfhi(hres[g][2 * h]) + yy[1]);
        o[2 * h + 1] = pk2(bflo(hres[g][2 * h + 1]) + yy[2], bfhi(hres[g][2 * h + 1]) + yy[3]);
      }
      if (g == 0) ST16(o, vx, ob_, 0); else ST16(o, vx, ob_, 64);
    }
  }
  { unsigned long long t0_, t1_; asm volatile("s_memrealtime %0\n\ts_waitcnt lgkmcnt(0)" : "=s"(t0_) :: "memory"); do { asm volatile("s_memrealtime %0\n\ts_waitcnt lgkmcnt(0)" : "=s"(t1_) :: "memory"); } while (t1_ - t0_ < tb_ - ta_); }
}

extern "C" void kernel_launch(void* const* d_in, const int* in_sizes, int n_in, void* d_out, int out_size, void* d_ws,
                              size_t ws_size, hipStream_t stream) {
  (void)hipFuncSetAttribute((const void*)fnet_fused, hipFuncAttributeMaxDynamicSharedMemorySize, LDS_BYTES);
  fnet_fused<<<256, 256, LDS_BYTES, stream>>>((cptr)d_in[0], (cptr)d_in[1], (cptr)d_in[2], (cptr)d_in[3], (cptr)d_in[4],
                                              (cptr)d_in[6], (cptr)d_in[7], (unsigned char*)d_ws, (unsigned char*)d_out);
}
```

```cpp
#define TLBW 0
#define NP0 8
#include <hip/hip_runtime.h>
#include <hip/hip_bf16.h>
#include <stdint.h>
#include <type_traits>

typedef unsigned v4u __attribute__((ext_vector_type(4)));
typedef unsigned v2u __attribute__((ext_vector_type(2)));
typedef float f32x4 __attribute__((ext_vector_type(4)));
typedef float f32x2 __attribute__((ext_vector_type(2)));
typedef short bf16x8 __attribute__((ext_vector_type(8)));
typedef short v4s __attribute__((ext_vector_type(4)));
typedef __bf16 bf2_t __attribute__((ext_vector_type(2)));
typedef const unsigned char* cptr;
#define LAS __attribute__((address_space(3)))

constexpr int S_ = 2048, D_ = 512, F_ = 1792;
constexpr unsigned EPOCH_OFF = 0;
constexpr unsigned FLAG0_OFF = 8192;
constexpr unsigned XNT_OFF = 131072;
constexpr unsigned WD_OFF  = XNT_OFF + (2u << 20);
constexpr unsigned W1_OFF  = WD_OFF + (1u << 20);
constexpr unsigned W2_OFF  = W1_OFF + 1835008u;
constexpr unsigned CS_OFF  = W2_OFF + 1835008u;
constexpr unsigned PQ_OFF  = CS_OFF + 131072;
constexpr unsigned H_OFF   = PQ_OFF + (4u << 20);
constexpr unsigned FF_OFF  = H_OFF + (2u << 20);
constexpr unsigned ST_OFF  = FF_OFF + 7340032u;
constexpr unsigned CKP_OFF = ST_OFF + 262144;
constexpr unsigned CKH_OFF = CKP_OFF + 262144;
constexpr unsigned CKF_OFF = CKH_OFF + 262144;
constexpr unsigned CKX_OFF = CKF_OFF + (1u << 20);

constexpr int XS_ROW = 1088;
constexpr int XS_OFF = 131072, WST_OFF = XS_OFF + 8 * XS_ROW;
constexpr int RED_OFF = 114688;
constexpr int LDS_BYTES = 163840;
constexpr unsigned SPIN_CAP = 1u << 20;

__device__ __forceinline__ unsigned pk2(float a, float b) {
  f32x2 v = {a, b};
  return __builtin_bit_cast(unsigned, __builtin_convertvector(v, bf2_t));
}
__device__ __forceinline__ float asf(unsigned u) { return __builtin_bit_cast(float, u); }
__device__ __forceinline__ float bflo(unsigned u) { return __builtin_bit_cast(float, u << 16); }
__device__ __forceinline__ float bfhi(unsigned u) { return __builtin_bit_cast(float, u & 0xffff0000u); }
__device__ __forceinline__ float rbf(float x) { return (float)(__bf16)x; }
typedef short v2s __attribute__((ext_vector_type(2)));
__device__ __forceinline__ unsigned relu2(unsigned u) {
  return __builtin_bit_cast(unsigned, __builtin_elementwise_max(__builtin_bit_cast(v2s, u), v2s{0, 0}));
}
__device__ __forceinline__ f32x4 mfma16(v2u a, v2u b, f32x4 c) {
  return __builtin_amdgcn_mfma_f32_16x16x16bf16_1k(__builtin_bit_cast(v4s, a), __builtin_bit_cast(v4s, b), c, 0, 0, 0);
}
__device__ __forceinline__ f32x4 unpk(unsigned a, unsigned b) { return f32x4{bflo(a), bfhi(a), bflo(b), bfhi(b)}; }
__device__ __forceinline__ float ln_rstd(float var) {
  return __builtin_amdgcn_rcpf(rbf(__builtin_amdgcn_sqrtf(rbf(rbf(var) + 1e-5f))));
}
__device__ __forceinline__ f32x4 mfma(v4u a, v4u b, f32x4 c) {
  return __builtin_amdgcn_mfma_f32_16x16x32_bf16(__builtin_bit_cast(bf16x8, a), __builtin_bit_cast(bf16x8, b), c, 0, 0, 0);
}
template <int CTRL> __device__ __forceinline__ float dpp_add(float x) {
  return x + __builtin_bit_cast(float, __builtin_amdgcn_update_dpp(0, __builtin_bit_cast(int, x), CTRL, 0xf, 0xf, true));
}
__device__ __forceinline__ float row16_sum(float x) {
  return dpp_add<0x121>(dpp_add<0x122>(dpp_add<0x124>(dpp_add<0x128>(x))));
}
__device__ __forceinline__ float rows_sum(float x) {
  unsigned a = __builtin_bit_cast(unsigned, x), b = a;
  asm volatile("" : "+v"(b));
  auto r = __builtin_amdgcn_permlane16_swap(a, b, false, false);
  x = asf(r[0]) + asf(r[1]);
  a = __builtin_bit_cast(unsigned, x); b = a;
  asm volatile("" : "+v"(b));
  auto s = __builtin_amdgcn_permlane32_swap(a, b, false, false);
  return asf(s[0]) + asf(s[1]);
}
__device__ __forceinline__ float half_sum(float x) {
  x = row16_sum(x);
  unsigned a = __builtin_bit_cast(unsigned, x), b = a;
  asm volatile("" : "+v"(b));
  auto r = __builtin_amdgcn_permlane16_swap(a, b, false, false);
  return asf(r[0]) + asf(r[1]);
}
#define SCHED() __builtin_amdgcn_sched_barrier(0)
#define WAITV(n) asm volatile("s_waitcnt vmcnt(%0)" ::"i"(n) : "memory")
#define WAITV_BAR(n) asm volatile("s_waitcnt vmcnt(%0) lgkmcnt(0)\n\ts_barrier" ::"i"(n) : "memory")
#define BAR() asm volatile("s_waitcnt lgkmcnt(0)\n\ts_barrier" ::: "memory")
#define PIN4(a, b, c, d) asm volatile("" : "+v"(a), "+v"(b), "+v"(c), "+v"(d))
#define PIN4A(a, b, c, d) asm volatile("" : "+a"(a), "+a"(b), "+a"(c), "+a"(d))

#define LD16(d, vo, sb, imm) asm volatile("global_load_dwordx4 %0, %1, %2 offset:%3" : "=v"(d) : "v"(vo), "s"(sb), "i"(imm) : "memory")
#define LD16C(d, vo, sb, imm) asm volatile("global_load_dwordx4 %0, %1, %2 offset:%3 sc1" : "=v"(d) : "v"(vo), "s"(sb), "i"(imm) : "memory")
#define LD16CA(d, vo, sb, imm) asm volatile("global_load_dwordx4 %0, %1, %2 offset:%3 sc1" : "=a"(d) : "v"(vo), "s"(sb), "i"(imm) : "memory")
#define LD8C(d, vo, sb, imm) asm volatile("global_load_dwordx2 %0, %1, %2 offset:%3 sc1" : "=v"(d) : "v"(vo), "s"(sb), "i"(imm) : "memory")
#define LD4(d, vo, sb, imm) asm volatile("global_load_dword %0, %1, %2 offset:%3" : "=v"(d) : "v"(vo), "s"(sb), "i"(imm) : "memory")
#define LD4C(d, vo, sb, imm) asm volatile("global_load_dword %0, %1, %2 offset:%3 sc1" : "=v"(d) : "v"(vo), "s"(sb), "i"(imm) : "memory")
#define ST16C(v, vo, sb, imm) asm volatile("global_store_dwordx4 %0, %1, %2 offset:%3 sc1\n\ts_nop 1" ::"v"(vo), "v"(v), "s"(sb), "i"(imm) : "memory")
#define ST16(v, vo, sb, imm) asm volatile("global_store_dwordx4 %0, %1, %2 offset:%3\n\ts_nop 1" ::"v"(vo), "v"(v), "s"(sb), "i"(imm) : "memory")
#define ST8C(v, vo, sb, imm) asm volatile("global_store_dwordx2 %0, %1, %2 offset:%3 sc1" ::"v"(vo), "v"(v), "s"(sb), "i"(imm) : "memory")
#define ST4C(v, vo, sb, imm) asm volatile("global_store_dword %0, %1, %2 offset:%3 sc1" ::"v"(vo), "v"(v), "s"(sb), "i"(imm) : "memory")
#define TOUCH(vo, sb) asm volatile("global_load_dword %0, %1, %2" : "+v"(sink) : "v"(vo), "s"(sb) : "memory")
#define TOUCHC(vo, sb) asm volatile("global_load_dword %0, %1, %2 sc1" : "+v"(sink) : "v"(vo), "s"(sb) : "memory")
#define COUNT(i)
__device__ __forceinline__ void st_flag(unsigned char* p, unsigned tag) {
  const unsigned z = 0;
  asm volatile("s_mov_b64 exec, 1\n\tglobal_store_dword %0, %1, %2 sc1\n\ts_mov_b64 exec, -1" ::"v"(z), "v"(tag), "s"(p) : "memory");
}
#define GLDS(vo, sb, lds, imm) asm volatile("s_mov_b32 m0, %2\n\ts_nop 0\n\tglobal_load_lds_dwordx4 %0, %1 offset:%3" ::"v"(vo), "s"(sb), "s"(lds), "i"(imm) : "memory")
#define GLDSC(vo, sb, lds, imm) asm volatile("s_mov_b32 m0, %2\n\ts_nop 0\n\tglobal_load_lds_dwordx4 %0, %1 offset:%3 sc1" ::"v"(vo), "s"(sb), "s"(lds), "i"(imm) : "memory")

__device__ __forceinline__ unsigned sum4(unsigned m, v4u v) { return (m + v[0] + v[1]) + v[2] + v[3]; }
__device__ __forceinline__ void poll_flags(cptr base, unsigned tag, int lane) {
  const unsigned vo = lane * 128;
  for (unsigned spin = 0; spin < SPIN_CAP; ++spin) {
    v4u v;
    asm volatile("global_load_dwordx4 %0, %1, %2 sc1\n\ts_waitcnt vmcnt(0)" : "=&v"(v) : "v"(vo), "s"(base) : "memory");
    if (__all(v[0] == tag && v[1] == tag && v[2] == tag && v[3] == tag)) break;
  }
}
__device__ __forceinline__ void last_step_reduce(unsigned char* red, int w, int lane, const v4u (&wt)[4], const v4u (&fc)[4],
                                                 f32x4 (&acc)[4][4], v4u (&o)[2]) {
#pragma unroll
  for (int c = 1; c <= 4; ++c) {
#pragma unroll
    for (int i = 0; i < 4; ++i) {
      acc[i][c & 3] = mfma(wt[i], fc[c & 3], acc[i][c & 3]);
      SCHED();
      if (c >= 2) *(f32x4*)(red + (((c - 1) ^ w) * 3 + c - 2) * 4096 + lane * 16 + i * 1024) = acc[i][c - 1];
      SCHED();
    }
  }
  BAR();
  const unsigned char* s = red + w * 12288 + lane * 16;
  f32x4 pt[3][4];
#pragma unroll
  for (int i = 0; i < 4; ++i)
#pragma unroll
    for (int c = 0; c < 3; ++c) pt[c][i] = *(const f32x4*)(s + c * 4096 + i * 1024);
  SCHED();
#pragma unroll
  for (int i = 0; i < 4; ++i) {
    const f32x4 y = (acc[i][0] + pt[0][i]) + (pt[1][i] + pt[2][i]);
    o[i >> 1][2 * (i & 1)] = pk2(y[0], y[1]);
    o[i >> 1][2 * (i & 1) + 1] = pk2(y[2], y[3]);
  }
}

#define FL0_CHK 11
#define CODE_KB 48
#define W1A 0
#define W2A 0
#define W2C 12
#define WDS 12
#define XT0 19
#define DB 5
#define NPA 16
constexpr int D2 = 4, D4 = 5, FL0_PUB = 3, FL0_IT = FL0_CHK - 3, NV = FL0_CHK + DB, NX = 41 - WDS;
constexpr int RA0 = NPA == 16 ? 3 : 0, RAD = NPA - RA0;

template <int I, int N, class F> __device__ __forceinline__ void static_for(F&& f) {
  if constexpr (I < N) { f(std::integral_constant<int, I>{}); static_for<I + 1, N>(f); }
}
constexpr bool p1_ra(int s) { return s >= RA0 && s + RAD < 32; }
constexpr int p1_xbase(int s) { int n = 0; for (int u = XT0; u < s; ++u) n += (u + DB < 32 ? 2 : 6); return n > NX ? NX : n; }
constexpr int p1_extra(int s) { return s < XT0 ? 0 : p1_xbase(s + 1) - p1_xbase(s); }
static_assert(p1_xbase(32) == NX && !p1_ra(XT0) && XT0 > FL0_CHK && (FL0_CHK & 3) == 3 && DB <= 7 && (NPA == 16 || NPA <= 12), "");
constexpr int p1_wait(int it, bool mid) {
  int posA[48] = {}, posB[40] = {}, posF = -1, n = 0, pos0 = 5 * DB;
  for (int s = 0; s < 48; ++s) posA[s] = -1;
  for (int s = 0; s < DB; ++s) { n += 5; posB[s] = n - 1; }
  if (it < 0) return n - 1 - posB[0];
  for (int s = 0; s <= it; ++s) {
    if (s == it && !mid) break;
    if (s == FL0_IT) posF = n - 1;
    if (p1_ra(s)) { n += 2; posA[s + RAD] = n - 1; }
    if (s == it) { n += p1_extra(s) > 2 ? 2 : p1_extra(s); break; }
    n += p1_extra(s);
    if (s + DB < 32) { n += (s + DB < NV ? 5 : 4); posB[s + DB] = n - 1; }
  }
  int req = -1;
  if (mid) req = posB[it + 1];
  else {
    if (it + 4 < 32) req = posA[it + 4];
    if (it == FL0_CHK && posF > req) req = posF;
    if (it == FL0_PUB && pos0 > req) req = pos0;
  }
  return n - 1 - req > 63 ? 63 : n - 1 - req;
}
constexpr int p2_w1(int s) { int n = 28 - W1A - 4 * (s - (8 - D2)); return (s + D2 < 8 || s == 7) ? 0 : n > 4 ? 4 : n < 0 ? 0 : n; }
constexpr int p2_wait(int k) {
  int n = 0;
  for (int g = k + 1; g < 8 && g < k - 1 + D2; ++g) n += 5;
  for (int s = 0; s < k - 1; ++s) n += p2_w1(s);
  return n + ((k - 1 + D2 < 8) ? 3 : (p2_w1(k - 1) > 2 ? 2 : p2_w1(k - 1)));
}
constexpr int W2B = 56 - W2A - W2C;
constexpr int p3_w2(int s) { int n = W2B - 4 * (s - 5); return s < 5 ? 0 : n > 4 ? 4 : n < 0 ? 0 : n; }
constexpr int p3_ops(int s) { return s < 4 ? 6 : s == 4 ? 4 : p3_w2(s); }
constexpr int p3_wait(int m) {
  int n = m < 4 ? 6 * (3 - m) : 0;
  for (int s = (m < 4 ? 0 : m - 3); s < 2 * m; ++s) n += p3_ops(s);
  return n;
}
constexpr int P3_ST = 12;
constexpr int p3_stwait() { int n = 0; for (int s = 5; s < P3_ST; ++s) n += p3_ops(s); return n; }
constexpr int p4_rec(int g) { return (g == 0 || g == 3 || g == 7 || g == 10) ? 2 : 0; }
constexpr int p4_wait(int k) {
  int n = 0;
  for (int g = k + 1; g < 14 && g < k - 1 + D4; ++g) n += 4 + p4_rec(g);
  return n + ((k - 1 + D4 < 14) ? 2 + p4_rec(k - 1 + D4) : 0);
}
static_assert(W2B >= 0 && W2B <= 44 && W1A >= 0 && W1A <= 28 && W1A % 4 == 0, "");
struct Tab { int v[16]; };
template <int (*F)(int)> constexpr Tab mk(int lo, int hi) { Tab t = {}; for (int k = lo; k < hi; ++k) t.v[k] = F(k); return t; }

__global__ __launch_bounds__(256, 1) void fnet_fused(cptr x, cptr wsre, cptr wsim, cptr wre, cptr wim, cptr w1, cptr w2,
                                                     unsigned char* ws, unsigned char* out) {
  extern __shared__ __attribute__((aligned(16))) unsigned char smem[];
  const int bid = blockIdx.x, p = bid & 7, j = bid >> 3;
  const int ti = 4 * p + (j >> 3), cg = j & 7;
#define MAP3 1
  const int t128 = MAP3 ? 4 * (p >> 1) + (j >> 3) : 2 * p + (j >> 4), fs = MAP3 ? 8 * (p & 1) + (j & 7) : j & 15;
  const int tid = threadIdx.x, lane = tid & 63, w = __builtin_amdgcn_readfirstlane(tid >> 6), q = lane >> 4, r = lane & 15;
  const unsigned lds0 = (unsigned)(size_t)(LAS unsigned char*)smem;
  const unsigned vl16 = lane * 16, vl4 = lane * 4;
  unsigned long long ta_ = 0, tb_ = 0;asm volatile("s_memrealtime %0\n\ts_waitcnt lgkmcnt(0)" : "=s"(ta_) :: "memory");

  const int mat = (j >> 2) & 1, nt = j & 3;
  unsigned epoch, sink = 0;
  v4u xr[2];
  {
    const unsigned vo = (lane >> 5) * 1024 + (lane & 31) * 16, zero = 0;
    cptr xb = x + (size_t)(8 * bid + 2 * w) * 1024;
    LD16(xr[0], vo, xb, 0);
    LD16(xr[1], vo, xb, 512);
    LD4C(epoch, zero, ws + EPOCH_OFF, 0);
  }
  const unsigned va0 = (lane >> 3) * 4096 + (((lane & 7) ^ (lane >> 4)) << 4), va1 = (va0 ^ 64) + 32768 - 1024;
  cptr abase = (mat ? wsim : wsre) + (size_t)(64 * ti + 16 * w) * 4096;
  const unsigned lda = lds0 + w * 2048;
#define A_STAGE0(s) GLDS(va0, abase + (s) * 128, lda + ((s) & 15) * 8192, 0)
#define A_STAGE1(s) GLDS(va1, abase + (s) * 128, lda + ((s) & 15) * 8192, 1024)
  const int kb1 = bid >> 4, fs1 = bid & 15, ob = bid < 224 ? bid : 223, kbd = bid & 31;
  {
    cptr pc;
    asm volatile("s_getpc_b64 %0" : "=s"(pc));
    const unsigned vo = ((4 * j + w) * (CODE_KB / 16) + (lane < CODE_KB / 16 ? lane : 0)) * 128;
    TOUCH(vo, pc);
  }
  if (TLBW) {
#define TLB_STEP (512 * 1024)
    cptr sb = w == 0 ? ws : w == 1 ? w1 : w == 2 ? w2 : (cptr)out;
    const unsigned lim = w == 0 ? (CKX_OFF + 262144) / TLB_STEP : 1835008 / TLB_STEP;
    const unsigned vo = (lane <= lim ? lane : 0) * TLB_STEP;
    TOUCHC(vo, sb);
    const unsigned z = 0;
    TOUCH(z, w & 1 ? wim : wre);
  }
#pragma unroll 1
  for (int s = 0; s < NP0; ++s) { A_STAGE0(s); A_STAGE1(s); }
  WAITV(2 * NP0 + 1 + (TLBW ? 2 : 0));
  asm volatile("" : "+v"(xr[0]), "+v"(xr[1]), "+v"(epoch));
  const unsigned tag = __builtin_amdgcn_readfirstlane(epoch) + 1u;
  asm volatile("s_memrealtime %0\n\ts_waitcnt lgkmcnt(0)" : "=s"(tb_) :: "memory");
  {
    float v[16];
#pragma unroll
    for (int i = 0; i < 2; ++i)
#pragma unroll
      for (int k = 0; k < 4; ++k) { v[8 * i + 2 * k] = bflo(xr[i][k]); v[8 * i + 2 * k + 1] = bfhi(xr[i][k]); }
    float s1[4] = {0.f, 0.f, 0.f, 0.f};
#pragma unroll
    for (int k = 0; k < 16; ++k) s1[k & 3] += v[k];
    const float mean = half_sum((s1[0] + s1[1]) + (s1[2] + s1[3])) * (1.0f / D_);
    float s2[4] = {0.f, 0.f, 0.f, 0.f};
#pragma unroll
    for (int k = 0; k < 16; ++k) { const float c = v[k] - mean; s2[k & 3] += c * c; }
    const float mb = rbf(mean), rd = ln_rstd(half_sum((s2[0] + s2[1]) + (s2[2] + s2[3])) * (1.0f / D_));
#pragma unroll
    for (int i = 0; i < 2; ++i) {
      v4u o;
#pragma unroll
      for (int k = 0; k < 4; ++k) {
        const unsigned c2 = pk2(v[8 * i + 2 * k] - mb, v[8 * i + 2 * k + 1] - mb);
        o[k] = pk2(bflo(c2) * rd, bfhi(c2) * rd);
      }
      *(v4u*)(smem + XS_OFF + (2 * w + (lane >> 5)) * XS_ROW + (lane & 31) * 16 + i * 512) = o;
    }
  }
  BAR();

  auto tr8 = [&](int off, int i) {
    const unsigned char* s = smem + off + ((lane >> 2) & 3) * XS_ROW + 64 * (q >> 1) + 16 * (lane & 3) + 8 * (q & 1) + 128 * i;
    const v2u lo = __builtin_bit_cast(v2u, __builtin_amdgcn_ds_read_tr16_b64_v4i16((LAS v4s*)s));
    const v2u hi = __builtin_bit_cast(v2u, __builtin_amdgcn_ds_read_tr16_b64_v4i16((LAS v4s*)(s + 4 * XS_ROW)));
    return v4u{lo[0], lo[1], hi[0], hi[1]};
  };
  {
    const v4u o0 = tr8(XS_OFF, 2 * w), o1 = tr8(XS_OFF, 2 * w + 1);
    const unsigned vo = (q >> 1) * 131072 + (q & 1) * 1024 + r * 16;
    unsigned char* sb = ws + XNT_OFF + (bid >> 2) * 2048 + (bid & 3) * 256 + w * 524288;
    ST16C(o0, vo, sb, 0);
    ST16C(o1, vo, sb + 262144, 0);
    auto sw = __builtin_amdgcn_permlane16_swap(sum4(0u, o0), sum4(0u, o1), false, false);
    const unsigned ck = tag + sw[0] + sw[1];
    const unsigned vc = ((q >> 1) + 2 * (q & 1)) * 16384 + r * 8;
    ST4C(ck, vc, ws + CKX_OFF + w * 65536 + (bid >> 3) * 512 + (bid & 3) * 128 + ((bid >> 2) & 1) * 4, 0);
  }
  const int wst = WST_OFF + w * 5952;
  if (w < 2) {
    const unsigned vo = (lane >> 5) * 1024 + (lane & 31) * 16;
    cptr sb = w2 + (size_t)(8 * ob) * 1024 + w * 512;
#pragma unroll
    for (int k = 0; k < 4; ++k) GLDS(vo, sb + k * 2048, lds0 + wst + k * 1088, 0);
  } else {
    const unsigned vo = (lane >> 3) * (F_ * 2) + (lane & 7) * 16;
    cptr sb = w1 + (size_t)(32 * kb1) * (F_ * 2) + ((7 * fs1) >> 1) * 64 + (w - 2) * 128;
#pragma unroll
    for (int k = 0; k < 4; ++k) GLDS(vo, sb + k * (8 * F_ * 2), lds0 + wst + k * 1088, 0);
  }
  v4u wdp;
  {
    const unsigned vo = (8 * (r >> 2) + (r & 3)) * 1024 + q * 16;
    LD16(wdp, vo, (kbd < 16 ? wre : wim) + (size_t)(64 * (bid >> 5) + 32 * (w >> 1) + 4 * (w & 1)) * 1024 + (kbd & 15) * 64, 0);
  }
#pragma unroll 1
  for (int s = NP0; s < NPA; ++s) { A_STAGE0(s); A_STAGE1(s); }
  asm volatile("" : "+v"(sink));

  cptr bbase = ws + XNT_OFF + (4 * nt + w) * 131072;
  cptr xcb = ws + CKX_OFF + (4 * nt + w) * 16384;
  const unsigned vl8 = lane * 8, tag2 = 2u * tag;
  v4u bf[8][4];
  v2u bc[8];
#define B_LOAD(s, i) LD16C(bf[(s) & 7][i], vl16, bbase + (s) * 4096, (i) * 1024)
#define B_CHK(s) LD8C(bc[(s) & 7], vl8, xcb + ((s) >> 3) * 4096, ((s) & 7) * 512)
#pragma unroll
  for (int s = 0; s < DB; ++s) { B_CHK(s); B_LOAD(s, 0); B_LOAD(s, 1); B_LOAD(s, 2); B_LOAD(s, 3); }

  asm volatile("s_waitcnt vmcnt(%1)" : "+v"(wdp) : "i"(5 * DB) : "memory");
  if (kbd >= 16) wdp ^= 0x80008000u;
  ST16C(wdp, vl16, ws + WD_OFF + bid * 4096 + w * 1024, 0);
  if (w < 2) {
    const int rr = (lane >> 2) & 3;
    const unsigned char* s = smem + wst + (rr >> 1) * 1088 + (rr & 1) * 512 + 64 * (q >> 1) + 16 * (lane & 3) + 8 * (q & 1);
    const unsigned vo = q * 1024 + r * 16;
    unsigned char* sb = ws + W2_OFF + (ob >> 2) * 4096 + (ob & 3) * 256 + w * (4 * 229376);
#pragma unroll
    for (int i = 0; i < 4; ++i) {
      const v2u lo = __builtin_bit_cast(v2u, __builtin_amdgcn_ds_read_tr16_b64_v4i16((LAS v4s*)(s + 128 * i)));
      const v2u hi = __builtin_bit_cast(v2u, __builtin_amdgcn_ds_read_tr16_b64_v4i16((LAS v4s*)(s + 128 * i + 2176)));
      const v4u o = {lo[0], lo[1], hi[0], hi[1]};
      ST16C(o, vo, sb + i * 229376, 0);
    }
  } else {
    const unsigned char* s = smem + wst + q * 1088 + ((lane >> 2) & 3) * 128 + 16 * (lane & 3);
    float csv = 0.f;
    auto pos = [&](int tl) { return (fs1 & 1) ? (tl == 0 ? 7 : tl == 1 ? 6 : tl - 2) : tl; };
#pragma unroll
    for (int i = 0; i < 4; ++i) {
      const int l = pos(4 * (w - 2) + i);
      const unsigned char* sl = s + (i >> 1) * 64 + (i & 1) * 8;
      const v2u lo = __builtin_bit_cast(v2u, __builtin_amdgcn_ds_read_tr16_b64_v4i16((LAS v4s*)sl));
      const v2u hi = __builtin_bit_cast(v2u, __builtin_amdgcn_ds_read_tr16_b64_v4i16((LAS v4s*)(sl + 512)));
      const v4u o = {lo[0], lo[1], hi[0], hi[1]};
      if (l < 7) ST16C(o, vl16, ws + W1_OFF + fs1 * 114688 + kb1 * 7168 + l * 1024, 0);
      const float cs = rows_sum(((bflo(o[0]) + bfhi(o[0])) + (bflo(o[1]) + bfhi(o[1]))) + ((bflo(o[2]) + bfhi(o[2])) + (bflo(o[3]) + bfhi(o[3]))));
      csv = (q == i) ? cs : csv;
    }
    const unsigned vc = pos(4 * (w - 2) + q) * 1024 + r * 4;
    ST4C(csv, vc, ws + CS_OFF + fs1 * 8192 + kb1 * 64, 0);
  }
  f32x4 acc1[2][4];
#pragma unroll
  for (int i = 0; i < 2; ++i)
#pragma unroll
    for (int t = 0; t < 4; ++t) acc1[i][t] = f32x4{0.f, 0.f, 0.f, 0.f};
  const unsigned ra0 = r * 128 + ((q ^ (r >> 1)) << 4), ra1 = ra0 ^ 64;
  v4u af[2][8];
  v4u xres[2], csr[7], wd[8][4];
  v4u flw = {tag, tag, tag, tag};
  const unsigned vl128 = lane * 128;
  cptr xb2 = x + (size_t)(64 * ti + 16 * w) * 1024 + cg * 128;
  const unsigned vx = r * 1024 + q * 16;
  cptr cb2 = ws + CS_OFF + fs * 8192;
  const unsigned vc2 = r * 64 + q * 16;
  cptr wb = ws + WD_OFF + cg * 131072 + w * 32768;
#define BAD(M) (__builtin_amdgcn_ballot_w64((M) != 0u) != 0)
#define SUM16(M0, F) sum4(sum4(sum4(sum4(M0, F[0]), F[1]), F[2]), F[3])
#define B_WAIT(s, N, TAIL) asm volatile("s_waitcnt vmcnt(%5)" TAIL : "+v"(bf[(s) & 7][0]), "+v"(bf[(s) & 7][1]), "+v"(bf[(s) & 7][2]), "+v"(bf[(s) & 7][3]), "+v"(bc[(s) & 7]) : "i"(N) : "memory")
  B_WAIT(0, p1_wait(-1, false), " lgkmcnt(0)\n\ts_barrier");
#pragma unroll
  for (int t = 0; t < 4; ++t) { af[0][t] = *(const v4u*)(smem + ra0 + t * 2048); af[0][4 + t] = *(const v4u*)(smem + ra1 + t * 2048); }
  unsigned bx = SUM16(tag2 - bc[0][0] - bc[0][1], bf[0]);

#define P1_EXTRA(e)                                                                                                     \
  if constexpr ((e) < p1_extra(it)) {                                                                                   \
    constexpr int n_ = p1_xbase(it) + (e);                                                                              \
    if constexpr (n_ < 2) LD16(xres[n_], vx, xb2, n_ * 64);                                                             \
    else if constexpr (n_ < 9) LD16C(csr[n_ - 2], vc2, cb2 + ((n_ - 2) >> 2) * 4096, ((n_ - 2) & 3) * 1024);            \
    else LD16CA(wd[(n_ - 9) >> 2][(n_ - 9) & 3], vl16, wb + ((n_ - 9) >> 2) * 4096, ((n_ - 9) & 3) * 1024);             \
  }
#define MM1(i, t, kb) acc1[i][t] = mfma(bf[U][2 * (kb) + (i)], af[C][4 * (kb) + (t)], acc1[i][t]); SCHED()
#define RD1(n) if constexpr (RN) { af[Nx][n] = *(const v4u*)(sn + ((n) < 4 ? ra0 : ra1) + ((n) & 3) * 2048); SCHED(); }
#define CK1(i) if constexpr (it + 1 < NV) { bx = sum4((i) == 0 ? tag2 - bc[Un][0] - bc[Un][1] : bx, bf[Un][i]); SCHED(); }
  static_for<0, 32>([&](auto IT) {
    constexpr int it = IT, U = it & 7, Un = (it + 1) & 7, C = it & 1, Nx = C ^ 1;
    constexpr bool RA = p1_ra(it), RB = it + DB < 32, RN = it + 1 < 32;
    if constexpr (it < NV) {
      if (__builtin_expect(BAD(bx), 0)) {
        unsigned spin = SPIN_CAP;
        WAITV(0);
        do {
          COUNT(0);
          v2u c_;
          asm volatile("global_load_dwordx2 %4, %6, %8 sc1\n\tglobal_load_dwordx4 %0, %5, %7 sc1\n\tglobal_load_dwordx4 %1, %5, %7 offset:1024 sc1\n\t"
                       "global_load_dwordx4 %2, %5, %7 offset:2048 sc1\n\tglobal_load_dwordx4 %3, %5, %7 offset:3072 sc1\n\ts_waitcnt vmcnt(0)"
                       : "=&v"(bf[U][0]), "=&v"(bf[U][1]), "=&v"(bf[U][2]), "=&v"(bf[U][3]), "=&v"(c_)
                       : "v"(vl16), "v"(vl8), "s"(bbase + it * 4096), "s"(xcb + it * 512) : "memory");
          if (!BAD(SUM16(tag2 - c_[0] - c_[1], bf[U]))) spin = 1;
        } while (--spin);
      }
    }
    if constexpr (it == FL0_CHK) {
      asm volatile("s_waitcnt vmcnt(%1)" : "+v"(flw) : "i"(p1_wait(it, false)) : "memory");
      if (!__all(flw[0] == tag && flw[1] == tag && flw[2] == tag && flw[3] == tag)) { COUNT(1); poll_flags(ws + FLAG0_OFF, tag, lane); }
    }
    if constexpr ((it & 3) == 3) WAITV_BAR(p1_wait(it, false));
    if constexpr (it == FL0_PUB) { if (w == 0) st_flag(ws + FLAG0_OFF + (bid & 63) * 128 + (bid >> 6) * 4, tag); }
    if constexpr (it == FL0_IT)
      asm volatile("s_cmp_lg_u32 %3, 0\n\ts_cbranch_scc1 1f\n\tglobal_load_dwordx4 %0, %1, %2 sc1\n1:"
                   : "+v"(flw) : "v"(vl128), "s"(ws + FLAG0_OFF), "s"(w) : "memory", "scc");
    const unsigned char* sn = smem + ((it + 1) & 15) * 8192;
    SCHED();
    MM1(0, 0, 0); RD1(0);
    MM1(1, 0, 0); if constexpr (RA) A_STAGE0(it + RAD); P1_EXTRA(0) SCHED();
    MM1(0, 1, 0); RD1(1);
    MM1(1, 1, 0); RD1(2);
    MM1(0, 2, 0); if constexpr (RA) A_STAGE1(it + RAD); P1_EXTRA(1) SCHED();
    MM1(1, 2, 0); RD1(3); if constexpr (RN) { B_WAIT(it + 1, p1_wait(it, true), ""); SCHED(); }
    MM1(0, 3, 0); if constexpr (RB && it + DB < NV) B_CHK(it + DB); if constexpr (RB) B_LOAD(it + DB, 0); P1_EXTRA(2) SCHED();
    MM1(1, 3, 0); RD1(4); CK1(0);
    MM1(0, 0, 1); RD1(5); CK1(1);
    MM1(1, 0, 1); if constexpr (RB) B_LOAD(it + DB, 1); P1_EXTRA(3) SCHED();
    MM1(0, 1, 1); RD1(6); CK1(2);
    MM1(1, 1, 1); if constexpr (RB) B_LOAD(it + DB, 2); P1_EXTRA(4) SCHED();
    MM1(0, 2, 1); RD1(7); CK1(3);
    MM1(1, 2, 1); if constexpr (RB) B_LOAD(it + DB, 3); P1_EXTRA(5) SCHED();
    MM1(0, 3, 1);
    MM1(1, 3, 1);
  });

  {
    const int kbq = ti * 32 + 16 * mat + 4 * nt + w;
    unsigned char* pq = ws + PQ_OFF + kbq * 4096;
    unsigned ck = tag;
#pragma unroll
    for (int t = 0; t < 4; ++t) {
      const v4u o = {pk2(acc1[0][t][0], acc1[0][t][1]), pk2(acc1[0][t][2], acc1[0][t][3]),
                     pk2(acc1[1][t][0], acc1[1][t][1]), pk2(acc1[1][t][2], acc1[1][t][3])};
      ST16C(o, vl16, pq + t * 1024, 0);
      ck = sum4(ck, o);
    }
    ST4C(ck, vl4, ws + CKP_OFF + kbq * 256, 0);
    if (bid == 0 && w == 0) st_flag(ws + EPOCH_OFF, tag);
  }
#pragma unroll
  for (int n = 32 - WDS; n < 32; ++n) LD16CA(wd[n >> 2][n & 3], vl16, wb + (n >> 2) * 4096, (n & 3) * 1024);
  cptr w1src = ws + W1_OFF + fs * 114688 + w * 28672;
  const unsigned w1dst = lds0 + w * 28672;
#define W1L(n) GLDSC(vl16, w1src + ((n) >> 2) * 4096, w1dst + ((n) >> 2) * 4096, ((n) & 3) * 1024)
#pragma unroll 1
  for (int n = 0; n < W1A / 4; ++n) {
    GLDSC(vl16, w1src + n * 4096, w1dst + n * 4096, 0);
    GLDSC(vl16, w1src + n * 4096, w1dst + n * 4096, 1024);
    GLDSC(vl16, w1src + n * 4096, w1dst + n * 4096, 2048);
    GLDSC(vl16, w1src + n * 4096, w1dst + n * 4096, 3072);
  }

#define LOAD4_WAIT(F, SB, VR)                                                                                               \
  asm volatile("global_load_dwordx4 %0, %4, %8 sc1\n\tglobal_load_dwordx4 %1, %5, %8 sc1\n\t"                           \
               "global_load_dwordx4 %2, %6, %8 sc1\n\tglobal_load_dwordx4 %3, %7, %8 sc1\n\ts_waitcnt vmcnt(0)"         \
               : "=&v"(F[0]), "=&v"(F[1]), "=&v"(F[2]), "=&v"(F[3])                                                     \
               : "v"(VR(0)), "v"(VR(1)), "v"(VR(2)), "v"(VR(3)), "s"(SB) : "memory")
#define STEP64(WT, FC, FN, HASN, WAITN, M0, OP0, OP1, OP2, OP3)                                                         \
  {                                                                                                                     \
    SCHED();                                                                                                            \
    acc[0][0] = mfma(WT[0], FC[0], acc[0][0]); SCHED(); OP0; SCHED();                                                   \
    acc[0][1] = mfma(WT[0], FC[1], acc[0][1]); acc[0][2] = mfma(WT[0], FC[2], acc[0][2]);                               \
    acc[0][3] = mfma(WT[0], FC[3], acc[0][3]); acc[1][0] = mfma(WT[1], FC[0], acc[1][0]); SCHED(); OP1; SCHED();        \
    acc[1][1] = mfma(WT[1], FC[1], acc[1][1]); SCHED();                                                                 \
    if (HASN) { WAITN; }                                                                                                \
    SCHED();                                                                                                            \
    acc[1][2] = mfma(WT[1], FC[2], acc[1][2]); SCHED(); if (HASN) mx = (M0); SCHED();                                   \
    acc[1][3] = mfma(WT[1], FC[3], acc[1][3]); SCHED(); if (HASN) mx = sum4(mx, FN[0]); SCHED();                        \
    acc[2][0] = mfma(WT[2], FC[0], acc[2][0]); SCHED(); OP2; SCHED();                                                   \
    acc[2][1] = mfma(WT[2], FC[1], acc[2][1]); SCHED(); if (HASN) mx = sum4(mx, FN[1]); SCHED();                        \
    acc[2][2] = mfma(WT[2], FC[2], acc[2][2]); SCHED(); if (HASN) mx = sum4(mx, FN[2]); SCHED();                        \
    acc[2][3] = mfma(WT[2], FC[3], acc[2][3]); SCHED(); if (HASN) mx = sum4(mx, FN[3]); SCHED();                        \
    acc[3][0] = mfma(WT[3], FC[0], acc[3][0]); SCHED(); OP3; SCHED();                                                   \
    acc[3][1] = mfma(WT[3], FC[1], acc[3][1]); acc[3][2] = mfma(WT[3], FC[2], acc[3][2]);                               \
    acc[3][3] = mfma(WT[3], FC[3], acc[3][3]); SCHED();                                                                 \
  }

  unsigned vrot[4];
#pragma unroll
  for (int c = 0; c < 4; ++c) vrot[c] = vl16 + (c ^ w) * 1024;
#define VROT(c) vrot[c]
  v4u hres[2];
  v4u w2f[14][4];
  cptr w2base = ws + W2_OFF + cg * 229376 + w * 57344;
#define W2L(n) do { if ((n) < 44) LD16CA(w2f[(n) >> 2][(n) & 3], vl16, w2base + ((n) >> 2) * 4096, ((n) & 3) * 1024);   \
                    else LD16C(w2f[(n) >> 2][(n) & 3], vl16, w2base + ((n) >> 2) * 4096, ((n) & 3) * 1024); } while (0)
  {
    constexpr Tab p2t = mk<p2_wait>(1, 8), p2w = mk<p2_w1>(0, 8);
    f32x4 acc[4][4];
    v4u pq[8][4];
    unsigned ck[8];
    cptr pb = ws + PQ_OFF + ti * 131072 + w * 32768;
    cptr cb = ws + CKP_OFF + (ti * 32 + 8 * w) * 256;
#pragma unroll
    for (int k = 0; k < D2; ++k) {
      LD4C(ck[k], vl4, cb, k * 256);
#pragma unroll
      for (int c = 0; c < 4; ++c) LD16C(pq[k][c], vrot[c], pb + k * 4096, 0);
    }
    asm volatile("s_waitcnt vmcnt(%2)" : "+v"(xres[0]), "+v"(xres[1]) : "i"(5 * D2) : "memory");
#pragma unroll
    for (int i = 0; i < 4; ++i) {
      acc[i][0] = unpk(xres[i >> 1][2 * (i & 1)], xres[i >> 1][2 * (i & 1) + 1]);
#pragma unroll
      for (int c = 1; c < 4; ++c) acc[i][c] = f32x4{0.f, 0.f, 0.f, 0.f};
    }
#define P2WAIT(g, N) asm volatile("s_waitcnt vmcnt(%5)" : "+v"(pq[g][0]), "+v"(pq[g][1]), "+v"(pq[g][2]), "+v"(pq[g][3]), "+v"(ck[g]) : "i"(N) : "memory")
    P2WAIT(0, 5 * D2 - 5);
    unsigned mx = SUM16(tag - ck[0], pq[0]);

#define P2OP(i) do { if (k + D2 < 8) { if ((i) == 0) LD4C(ck[(k + D2) & 7], vl4, cb, ((k + D2) & 7) * 256);              \
                                       LD16C(pq[(k + D2) & 7][i], vrot[i], pb + (k + D2) * 4096, 0); }                  \
                     else if ((i) < p2w.v[k]) W1L(W1A + 4 * (k + D2 - 8) + (i)); } while (0)
#pragma unroll
    for (int k = 0; k < 8; ++k) {
      if (__builtin_expect(BAD(mx), 0)) {
        unsigned spin = SPIN_CAP;
        WAITV(0);
        do {
          COUNT(1);
          unsigned c_;
          LD4C(c_, vl4, cb, k * 256);
          LOAD4_WAIT(pq[k], pb + k * 4096, VROT);
          asm volatile("" : "+v"(c_));
          if (!BAD(SUM16(tag - c_, pq[k]))) spin = 1;
        } while (--spin);
      }
      PIN4A(wd[k][0], wd[k][1], wd[k][2], wd[k][3]);
      if (k < 7) STEP64(wd[k], pq[k], pq[(k + 1) & 7], true, P2WAIT((k + 1) & 7, p2t.v[(k + 1) & 7]), tag - ck[(k + 1) & 7], P2OP(0), P2OP(1), P2OP(2), P2OP(3));
    }

    last_step_reduce(smem + RED_OFF, w, lane, wd[7], pq[7], acc, hres);
    const int hp = (4 * ti + w) * 8 + cg;
    ST16C(hres[0], vl16, ws + H_OFF + hp * 2048, 0);
    ST16C(hres[1], vl16, ws + H_OFF + hp * 2048, 1024);
    const unsigned hck = sum4(sum4(tag, hres[0]), hres[1]);
    ST4C(hck, vl4, ws + CKH_OFF + hp * 256, 0);
    f32x4 hv[4];
#pragma unroll
    for (int i = 0; i < 4; ++i) hv[i] = unpk(hres[i >> 1][2 * (i & 1)], hres[i >> 1][2 * (i & 1) + 1]);
    const f32x4 s4 = (hv[0] + hv[1]) + (hv[2] + hv[3]);
    const float m = rows_sum((s4[0] + s4[1]) + (s4[2] + s4[3])) * (1.0f / 64.0f);
    f32x4 q4 = {0.f, 0.f, 0.f, 0.f};
#pragma unroll
    for (int i = 0; i < 4; ++i) { const f32x4 c = hv[i] - m; q4 += c * c; }
    const float m2 = rows_sum((q4[0] + q4[1]) + (q4[2] + q4[3]));
    const unsigned mu = __builtin_bit_cast(unsigned, m), m2u = __builtin_bit_cast(unsigned, m2);
    const v4u st = {mu, m2u, tag + mu + m2u, 0u};
    asm volatile("s_mov_b64 exec, 0xffff\n\tglobal_store_dwordx4 %0, %1, %2 sc1\n\ts_mov_b64 exec, -1\n\ts_nop 0" ::"v"(vl16), "v"(st), "s"(ws + ST_OFF + hp * 256) : "memory");
  }

#pragma unroll
  for (int n = W1A + 4 * (D2 - 1); n < 28; ++n) W1L(n);
#pragma unroll
  for (int n = 0; n < W2A; ++n) W2L(n);
  v2u csf[7];
  {
    unsigned char* csl = smem + RED_OFF + w * 12288;
#pragma unroll
    for (int l = 0; l < 7; ++l) {
      asm volatile("" : "+v"(csr[l]));
      f32x4 c;
#pragma unroll
      for (int k = 0; k < 4; ++k) c[k] = row16_sum(asf(csr[l][k]));
      *(f32x4*)(csl + q * 16 + l * 64) = c;
    }
#pragma unroll
    for (int l = 0; l < 7; ++l) {
      const float c = *(const float*)(csl + r * 4 + l * 64);
      const float c0 = rbf(c), c1 = rbf(c - c0);
      csf[l] = v2u{pk2(c0, c1), pk2(c - c0 - c1, 0.f)};
    }
  }

  {
    constexpr Tab p3t = mk<p3_wait>(0, 8), p3w = mk<p3_w2>(0, 16);
    v4u hf[8][4];
    unsigned ck[8][2];
    v4u str[2][2];
    cptr hb = ws + H_OFF + (8 * t128 + 2 * w) * 16384;
    cptr cb = ws + CKH_OFF + (8 * t128 + 2 * w) * 2048;
    const unsigned vh1 = vl16 + 16384;
    const unsigned vs = q * 512 + r * 16;
    cptr stb = ws + ST_OFF + (8 * t128 + 2 * w) * 2048;
#define HL(m, i) LD16C(hf[m][i], ((i) & 1) ? vh1 : vl16, hb + ((m) >> 1) * 4096, ((m) & 1) * 2048 + ((i) >> 1) * 1024)
#define HC(m, t) LD4C(ck[m][t], vl4, cb, (t) * 2048 + (m) * 256)
#pragma unroll
    for (int m = 0; m < 4; ++m) { HC(m, 0); HC(m, 1); HL(m, 0); HL(m, 1); HL(m, 2); HL(m, 3); }
    f32x4 acc[7][2];
#pragma unroll
    for (int l = 0; l < 7; ++l) { acc[l][0] = f32x4{0.f, 0.f, 0.f, 0.f}; acc[l][1] = f32x4{0.f, 0.f, 0.f, 0.f}; }
#define P3WAIT(m, N, TAIL) asm volatile("s_waitcnt vmcnt(%6)" TAIL : "+v"(hf[m][0]), "+v"(hf[m][1]), "+v"(hf[m][2]), "+v"(hf[m][3]), "+v"(ck[m][0]), "+v"(ck[m][1]) : "i"(N) : "memory")
    P3WAIT(0, p3t.v[0], " lgkmcnt(0)\n\ts_barrier");

    unsigned whi_ = lds0 + vl16 + 57344;
    asm volatile("" : "+v"(whi_));
    const LAS unsigned char* wlo = (const LAS unsigned char*)smem + vl16;
    const LAS unsigned char* whi = (const LAS unsigned char*)(size_t)whi_;
#define W1F(k, l) (*(const LAS v4u*)(((k) < 8 ? wlo + (k) * 7168 : whi + ((k) - 8) * 7168) + (l) * 1024))
    v4u wf[2][7];
#pragma unroll
    for (int l = 0; l < 7; ++l) wf[0][l] = W1F(0, l);
    unsigned mx = SUM16(tag2 - ck[0][0] - ck[0][1], hf[0]);
    float rd[2];
    v2u nmf[2];
    auto stats = [&](int t) {
      const float m0 = asf(str[t][0][0]), m1 = asf(str[t][1][0]);
      const float m = rows_sum(m0 + m1) * 0.125f;
      const float da = m0 - m, db = m1 - m;
      const float m2 = rows_sum(asf(str[t][0][1]) + asf(str[t][1][1]) + 64.0f * (da * da + db * db));
      const float nm = q == 0 ? -m : 0.f;
      nmf[t] = v2u{pk2(nm, nm), pk2(nm, 0.f)};
      rd[t] = ln_rstd(m2 * (1.0f / D_));
    };
#define P3OP(i) do { if (k < 4) { if ((i) < 2) HC((4 + k) & 7, (i) & 1); else HL((4 + k) & 7, ((i) - 2) & 3); }          \
                     else if (k == 4) { if ((i) < 4) LD16C(str[((i) >> 1) & 1][(i) & 1], vs, stb, (((i) >> 1) & 1) * 2048 + ((i) & 1) * 256); } \
                     else if ((i) < p3w.v[k]) W2L(W2A + 4 * (k - 5) + (i)); } while (0)
#pragma unroll
    for (int k = 0; k < 16; ++k) {
      const int m = k >> 1, mn = ((k + 1) >> 1) & 7;
      if ((k & 1) == 0 && __builtin_expect(BAD(mx), 0)) {
        unsigned spin = SPIN_CAP;
        WAITV(0);
        do {
          COUNT(2);
          unsigned c0_, c1_;
          asm volatile("global_load_dword %4, %8, %10 sc1\n\tglobal_load_dword %5, %8, %10 offset:2048 sc1\n\t"
                       "global_load_dwordx4 %0, %6, %9 sc1\n\tglobal_load_dwordx4 %1, %7, %9 sc1\n\t"
                       "global_load_dwordx4 %2, %6, %9 offset:1024 sc1\n\tglobal_load_dwordx4 %3, %7, %9 offset:1024 sc1\n\ts_waitcnt vmcnt(0)"
                       : "=&v"(hf[m][0]), "=&v"(hf[m][1]), "=&v"(hf[m][2]), "=&v"(hf[m][3]), "=&v"(c0_), "=&v"(c1_)
                       : "v"(vl16), "v"(vh1), "v"(vl4), "s"(hb + m * 2048), "s"(cb + m * 256) : "memory");
          if (!BAD(SUM16(tag2 - c0_ - c1_, hf[m]))) spin = 1;
        } while (--spin);
      }
      if (k == P3_ST) {
        asm volatile("s_waitcnt vmcnt(%4)" : "+v"(str[0][0]), "+v"(str[0][1]), "+v"(str[1][0]), "+v"(str[1][1]) : "i"(p3_stwait()) : "memory");
#define STCK(a) (tag + (a)[0] + (a)[1] - (a)[2])
        if (__builtin_expect(BAD(STCK(str[0][0]) | STCK(str[0][1]) | STCK(str[1][0]) | STCK(str[1][1])), 0)) {
          unsigned spin = SPIN_CAP;
          WAITV(0);
          do {
            asm volatile("global_load_dwordx4 %0, %4, %5 sc1\n\tglobal_load_dwordx4 %1, %4, %5 offset:256 sc1\n\t"
                         "global_load_dwordx4 %2, %4, %5 offset:2048 sc1\n\tglobal_load_dwordx4 %3, %4, %5 offset:2304 sc1\n\ts_waitcnt vmcnt(0)"
                         : "=&v"(str[0][0]), "=&v"(str[0][1]), "=&v"(str[1][0]), "=&v"(str[1][1]) : "v"(vs), "s"(stb) : "memory");
            if (!BAD(STCK(str[0][0]) | STCK(str[0][1]) | STCK(str[1][0]) | STCK(str[1][1]))) spin = 1;
          } while (--spin);
        }
      }
      SCHED();
      if (k == P3_ST || k == P3_ST + 2) {
        stats((k - P3_ST) >> 1);
#pragma unroll
        for (int l = 0; l < 7; ++l) {
          acc[l][0] = mfma(wf[k & 1][l], hf[m][0], acc[l][0]);
          wf[(k + 1) & 1][l] = W1F(k + 1, l);
          acc[l][1] = mfma(wf[k & 1][l], hf[m][1], acc[l][1]);
        }
#pragma unroll
        for (int i = 0; i < 14; ++i) {
          __builtin_amdgcn_sched_group_barrier(0x008, 1, 0);
          if (i & 1) __builtin_amdgcn_sched_group_barrier(0x100, 1, 0);
          __builtin_amdgcn_sched_group_barrier(0x002, 2, 0);
        }
        SCHED();
        P3OP(0); P3OP(1); P3OP(2); P3OP(3);
        SCHED();
      } else {
#pragma unroll
        for (int l = 0; l < 7; ++l) {
          acc[l][0] = mfma(wf[k & 1][l], hf[m][2 * (k & 1)], acc[l][0]);
          SCHED();
          if (k + 1 < 16) wf[(k + 1) & 1][l] = W1F(k + 1, l);
          if ((k & 1) && k + 1 < 16 && k >= 4) {
            if (l == 4) P3WAIT(mn, p3t.v[mn], "");
            if (l == 5) mx = sum4(mx, hf[mn][1]);
            if (l == 6) mx = sum4(mx, hf[mn][3]);
          }
          SCHED();
          acc[l][1] = mfma(wf[k & 1][l], hf[m][2 * (k & 1) + 1], acc[l][1]);
          SCHED();
          if (l < 6) P3OP(l);
          if ((k & 1) && k + 1 < 16) {
            if (k >= 4) {
              if (l == 4) mx = sum4(tag2 - ck[mn][0] - ck[mn][1], hf[mn][0]);
              if (l == 5) mx = sum4(mx, hf[mn][2]);
            } else if (l == 5) {
              P3WAIT(mn, p3t.v[mn], "");
              mx = SUM16(tag2 - ck[mn][0] - ck[mn][1], hf[mn]);
            }
          }
          SCHED();
        }
      }
    }

    const int kbp = (7 * fs + (fs & 1)) >> 1, taus = 7 * fs + ((fs & 1) ? 0 : 6);
    const int t64 = 2 * t128 + (w >> 1);
    unsigned char* fb = ws + FF_OFF + t64 * 229376 + kbp * 4096 + 2 * (w & 1) * 1024;
    v4u fck = {tag, tag, tag, tag};
#pragma unroll
    for (int l = 0; l < 7; ++l) { acc[l][0] = mfma16(csf[l], nmf[0], acc[l][0]); acc[l][1] = mfma16(csf[l], nmf[1], acc[l][1]); }
    auto act = [&](int l, int t) {
      const f32x4 v = acc[l][t] * rd[t];
      return v2u{relu2(pk2(v[0], v[1])), relu2(pk2(v[2], v[3]))};
    };
#pragma unroll
    for (int m_ = 0; m_ < 3; ++m_)
#pragma unroll
      for (int t = 0; t < 2; ++t) {
        const v2u a = act(2 * m_, t), b = act(2 * m_ + 1, t);
        const v4u oo = {a[0], a[1], b[0], b[1]};
        if (t == 0) ST16C(oo, vl16, fb + m_ * 4096, 0); else ST16C(oo, vl16, fb + m_ * 4096, 1024);
        fck[m_] = sum4(fck[m_], oo);
      }
    const v2u a6 = act(6, 0), b6 = act(6, 1);
    const v4u o6 = {a6[0], a6[1], b6[0], b6[1]};
    ST16C(o6, vl16, ws + FF_OFF + t64 * 229376 + (taus >> 1) * 4096 + (2 * (taus & 1) + (w & 1)) * 1024, 0);
    fck[3] = sum4(fck[3], o6);
    ST16C(fck, vl16, ws + CKF_OFF + ((t64 * 16 + fs) * 2 + (w & 1)) * 1024, 0);
  }

#pragma unroll
  for (int n = 56 - W2C; n < 56; ++n) W2L(n);

  {
    constexpr Tab p4t = mk<p4_wait>(1, 14);
    f32x4 acc[4][4];
    constexpr int R = D4 + 1;
    v4u ff[R][4];
    v4u rec[4][2];
    cptr fb = ws + FF_OFF + ti * 229376 + w * 57344;
    cptr cb = ws + CKF_OFF + (ti * 16 + 4 * w) * 2048;
    unsigned vsh[4];
#pragma unroll
    for (int c = 0; c < 4; ++c) vsh[c] = vl16 + (c ^ (w >> 1)) * 1024;
#define VR4(g, c) ((g) % 7 == 3 ? vsh[c] : vrot[c])
#define RECB(g) ((g) == 0 ? 0 : (g) == 3 ? 1 : (g) == 7 ? 2 : 3)
#define RECL(b) { LD16C(rec[b][0], vl16, cb + ((b) >> 1) * 4096, ((b) & 1) * 2048); LD16C(rec[b][1], vl16, cb + ((b) >> 1) * 4096, ((b) & 1) * 2048 + 1024); }
#define RB0(g) (2 * ((g) / 7) + ((g) % 7 > 3 ? 1 : 0))
#define RC0(g) ((g) % 7 > 3 ? (g) % 7 - 4 : (g) % 7)
#define M4(RE, g) ((g) % 7 == 3 ? 2u * tag2 - RE[RB0(g)][0][3] - RE[RB0(g)][1][3] - RE[RB0(g) + 1][0][3] - RE[RB0(g) + 1][1][3]   \
                                : tag2 - RE[RB0(g)][0][RC0(g)] - RE[RB0(g)][1][RC0(g)])
#pragma unroll
    for (int k = 0; k < D4; ++k) {
      if (p4_rec(k)) RECL(RECB(k));
#pragma unroll
      for (int c = 0; c < 4; ++c) LD16C(ff[k][c], VR4(k, c), fb + k * 4096, 0);
    }
#pragma unroll
    for (int i = 0; i < 4; ++i) {
      acc[i][0] = unpk(hres[i >> 1][2 * (i & 1)], hres[i >> 1][2 * (i & 1) + 1]);
#pragma unroll
      for (int c = 1; c < 4; ++c) acc[i][c] = f32x4{0.f, 0.f, 0.f, 0.f};
    }
#define P4WAIT(g, N) do { asm volatile("s_waitcnt vmcnt(%4)" : "+v"(ff[(g) % R][0]), "+v"(ff[(g) % R][1]), "+v"(ff[(g) % R][2]), "+v"(ff[(g) % R][3]) : "i"(N) : "memory"); \
                          if (p4_rec(g)) asm volatile("" : "+v"(rec[RECB(g)][0]), "+v"(rec[RECB(g)][1])); } while (0)
    P4WAIT(0, 4 * D4 - 4 + 2);
    unsigned mx = SUM16(M4(rec, 0), ff[0]);

#define P4OP(i) do { if (k + D4 < 14) { if ((i) == 0 && p4_rec(k + D4)) RECL(RECB(k + D4));                              \
                                        LD16C(ff[(k + D4) % R][i], VR4(k + D4, i), fb + (k + D4) * 4096, 0); } } while (0)
#pragma unroll
    for (int k = 0; k < 14; ++k) {
      if (__builtin_expect(BAD(mx), 0)) {
        unsigned spin = SPIN_CAP;
        WAITV(0);
        do {
          COUNT(3);
          RECL(RB0(k));
          if (k % 7 == 3) RECL(RB0(k) + 1);
          #define VRK(c) VR4(k, c)
          LOAD4_WAIT(ff[k % R], fb + k * 4096, VRK);
          asm volatile("" : "+v"(rec[RB0(k)][0]), "+v"(rec[RB0(k)][1]));
          if (k % 7 == 3) asm volatile("" : "+v"(rec[(RB0(k) + 1) & 3][0]), "+v"(rec[(RB0(k) + 1) & 3][1]));
          if (!BAD(SUM16(M4(rec, k), ff[k % R]))) spin = 1;
        } while (--spin);
      }
      if (k % 7 == 3) {
        v4u* F = ff[k % R];
        const bool w0 = w & 1;
        const v4u e0 = {F[0][0], F[0][1], F[2][0], F[2][1]}, o0 = {F[0][2], F[0][3], F[2][2], F[2][3]};
        const v4u e1 = {F[1][0], F[1][1], F[3][0], F[3][1]}, o1 = {F[1][2], F[1][3], F[3][2], F[3][3]};
        F[0] = w0 ? o0 : e0; F[1] = w0 ? e0 : o0; F[2] = w0 ? o1 : e1; F[3] = w0 ? e1 : o1;
      }
      if (k < 11) PIN4A(w2f[k][0], w2f[k][1], w2f[k][2], w2f[k][3]);
      else PIN4(w2f[k][0], w2f[k][1], w2f[k][2], w2f[k][3]);
      if (k < 13) STEP64(w2f[k], ff[k % R], ff[(k + 1) % R], true, P4WAIT((k + 1) % 14, p4t.v[(k + 1) % 14]), M4(rec, (k + 1) % 14), P4OP(0), P4OP(1), P4OP(2), P4OP(3));
    }
    v4u o[2];
    last_step_reduce(smem + RED_OFF, w, lane, w2f[13], ff[13 % R], acc, o);
    unsigned char* ob_ = out + (size_t)(64 * ti + 16 * w) * 1024 + cg * 128;
    ST16(o[0], vx, ob_, 0);
    ST16(o[1], vx, ob_, 64);
  }
  { unsigned long long t0_, t1_; asm volatile("s_memrealtime %0\n\ts_waitcnt lgkmcnt(0)" : "=s"(t0_) :: "memory"); do { asm volatile("s_memrealtime %0\n\ts_waitcnt lgkmcnt(0)" : "=s"(t1_) :: "memory"); } while (t1_ - t0_ < tb_ - ta_); }
}

extern "C" void kernel_launch(void* const* d_in, const int* in_sizes, int n_in, void* d_out, int out_size, void* d_ws,
                              size_t ws_size, hipStream_t stream) {
  (void)hipFuncSetAttribute((const void*)fnet_fused, hipFuncAttributeMaxDynamicSharedMemorySize, LDS_BYTES);
  fnet_fused<<<256, 256, LDS_BYTES, stream>>>((cptr)d_in[0], (cptr)d_in[1], (cptr)d_in[2], (cptr)d_in[3], (cptr)d_in[4],
                                              (cptr)d_in[6], (cptr)d_in[7], (unsigned char*)d_ws, (unsigned char*)d_out);
}
```

```cpp
#include <hip/hip_runtime.h>
#include <hip/hip_bf16.h>
#include <stdint.h>
#include <type_traits>

typedef unsigned v4u __attribute__((ext_vector_type(4)));
typedef unsigned v2u __attribute__((ext_vector_type(2)));
typedef float f32x4 __attribute__((ext_vector_type(4)));
typedef float f32x2 __attribute__((ext_vector_type(2)));
typedef short bf16x8 __attribute__((ext_vector_type(8)));
typedef short v4s __attribute__((ext_vector_type(4)));
typedef __bf16 bf2_t __attribute__((ext_vector_type(2)));
typedef const unsigned char* cptr;
#define LAS __attribute__((address_space(3)))

constexpr int S_ = 2048, D_ = 512, F_ = 1792;
constexpr unsigned EPOCH_OFF = 0;
constexpr unsigned FLAG0_OFF = 8192;
constexpr unsigned XNT_OFF = 131072;
constexpr unsigned WD_OFF  = XNT_OFF + (2u << 20);
constexpr unsigned W1_OFF  = WD_OFF + (1u << 20);
constexpr unsigned W2_OFF  = W1_OFF + 1835008u;
constexpr unsigned CS_OFF  = W2_OFF + 1835008u;
constexpr unsigned PQ_OFF  = CS_OFF + 131072;
constexpr unsigned H_OFF   = PQ_OFF + (4u << 20);
constexpr unsigned FF_OFF  = H_OFF + (2u << 20);
constexpr unsigned ST_OFF  = FF_OFF + 7340032u;
constexpr unsigned CKP_OFF = ST_OFF + 262144;
constexpr unsigned CKH_OFF = CKP_OFF + 262144;
constexpr unsigned CKF_OFF = CKH_OFF + 262144;
constexpr unsigned CKX_OFF = CKF_OFF + (1u << 20);

constexpr int XS_ROW = 1088;
constexpr int XS_OFF = 131072, WST_OFF = XS_OFF + 8 * XS_ROW;
constexpr int RED_OFF = 114688;
constexpr int LDS_BYTES = 163840;
constexpr unsigned SPIN_CAP = 1u << 20;

__device__ __forceinline__ unsigned pk2(float a, float b) {
  f32x2 v = {a, b};
  return __builtin_bit_cast(unsigned, __builtin_convertvector(v, bf2_t));
}
__device__ __forceinline__ float asf(unsigned u) { return __builtin_bit_cast(float, u); }
__device__ __forceinline__ float bflo(unsigned u) { return __builtin_bit_cast(float, u << 16); }
__device__ __forceinline__ float bfhi(unsigned u) { return __builtin_bit_cast(float, u & 0xffff0000u); }
__device__ __forceinline__ float rbf(float x) { return (float)(__bf16)x; }
typedef short v2s __attribute__((ext_vector_type(2)));
__device__ __forceinline__ unsigned relu2(unsigned u) {
  return __builtin_bit_cast(unsigned, __builtin_elementwise_max(__builtin_bit_cast(v2s, u), v2s{0, 0}));
}
__device__ __forceinline__ f32x4 mfma16(v2u a, v2u b, f32x4 c) {
  return __builtin_amdgcn_mfma_f32_16x16x16bf16_1k(__builtin_bit_cast(v4s, a), __builtin_bit_cast(v4s, b), c, 0, 0, 0);
}
__device__ __forceinline__ f32x4 unpk(unsigned a, unsigned b) { return f32x4{bflo(a), bfhi(a), bflo(b), bfhi(b)}; }
__device__ __forceinline__ float ln_rstd(float var) {
  return __builtin_amdgcn_rcpf(rbf(__builtin_amdgcn_sqrtf(rbf(rbf(var) + 1e-5f))));
}
__device__ __forceinline__ f32x4 mfma(v4u a, v4u b, f32x4 c) {
  return __builtin_amdgcn_mfma_f32_16x16x32_bf16(__builtin_bit_cast(bf16x8, a), __builtin_bit_cast(bf16x8, b), c, 0, 0, 0);
}
template <int CTRL> __device__ __forceinline__ float dpp_add(float x) {
  return x + __builtin_bit_cast(float, __builtin_amdgcn_update_dpp(0, __builtin_bit_cast(int, x), CTRL, 0xf, 0xf, true));
}
__device__ __forceinline__ float row16_sum(float x) {
  return dpp_add<0x121>(dpp_add<0x122>(dpp_add<0x124>(dpp_add<0x128>(x))));
}
__device__ __forceinline__ float rows_sum(float x) {
  unsigned a = __builtin_bit_cast(unsigned, x), b = a;
  asm volatile("" : "+v"(b));
  auto r = __builtin_amdgcn_permlane16_swap(a, b, false, false);
  x = asf(r[0]) + asf(r[1]);
  a = __builtin_bit_cast(unsigned, x); b = a;
  asm volatile("" : "+v"(b));
  auto s = __builtin_amdgcn_permlane32_swap(a, b, false, false);
  return asf(s[0]) + asf(s[1]);
}
__device__ __forceinline__ float half_sum(float x) {
  x = row16_sum(x);
  unsigned a = __builtin_bit_cast(unsigned, x), b = a;
  asm volatile("" : "+v"(b));
  auto r = __builtin_amdgcn_permlane16_swap(a, b, false, false);
  return asf(r[0]) + asf(r[1]);
}
#define SCHED() __builtin_amdgcn_sched_barrier(0)
#define WAITV(n) asm volatile("s_waitcnt vmcnt(%0)" ::"i"(n) : "memory")
#define WAITV_BAR(n) asm volatile("s_waitcnt vmcnt(%0) lgkmcnt(0)\n\ts_barrier" ::"i"(n) : "memory")
#define BAR() asm volatile("s_waitcnt lgkmcnt(0)\n\ts_barrier" ::: "memory")
#define PIN4(a, b, c, d) asm volatile("" : "+v"(a), "+v"(b), "+v"(c), "+v"(d))
#define PIN4A(a, b, c, d) asm volatile("" : "+a"(a), "+a"(b), "+a"(c), "+a"(d))

#define LD16(d, vo, sb, imm) asm volatile("global_load_dwordx4 %0, %1, %2 offset:%3" : "=v"(d) : "v"(vo), "s"(sb), "i"(imm) : "memory")
#define LD16C(d, vo, sb, imm) asm volatile("global_load_dwordx4 %0, %1, %2 offset:%3 sc1" : "=v"(d) : "v"(vo), "s"(sb), "i"(imm) : "memory")
#define LD16CA(d, vo, sb, imm) asm volatile("global_load_dwordx4 %0, %1, %2 offset:%3 sc1" : "=a"(d) : "v"(vo), "s"(sb), "i"(imm) : "memory")
#define LD8C(d, vo, sb, imm) asm volatile("global_load_dwordx2 %0, %1, %2 offset:%3 sc1" : "=v"(d) : "v"(vo), "s"(sb), "i"(imm) : "memory")
#define LD4(d, vo, sb, imm) asm volatile("global_load_dword %0, %1, %2 offset:%3" : "=v"(d) : "v"(vo), "s"(sb), "i"(imm) : "memory")
#define LD4C(d, vo, sb, imm) asm volatile("global_load_dword %0, %1, %2 offset:%3 sc1" : "=v"(d) : "v"(vo), "s"(sb), "i"(imm) : "memory")
#define ST16C(v, vo, sb, imm) asm volatile("global_store_dwordx4 %0, %1, %2 offset:%3 sc1\n\ts_nop 1" ::"v"(vo), "v"(v), "s"(sb), "i"(imm) : "memory")
#define ST16(v, vo, sb, imm) asm volatile("global_store_dwordx4 %0, %1, %2 offset:%3\n\ts_nop 1" ::"v"(vo), "v"(v), "s"(sb), "i"(imm) : "memory")
#define ST8C(v, vo, sb, imm) asm volatile("global_store_dwordx2 %0, %1, %2 offset:%3 sc1" ::"v"(vo), "v"(v), "s"(sb), "i"(imm) : "memory")
#define ST4C(v, vo, sb, imm) asm volatile("global_store_dword %0, %1, %2 offset:%3 sc1" ::"v"(vo), "v"(v), "s"(sb), "i"(imm) : "memory")
#define TOUCH(vo, sb) asm volatile("global_load_dword %0, %1, %2" : "+v"(sink) : "v"(vo), "s"(sb) : "memory")
#define TOUCHC(vo, sb) asm volatile("global_load_dword %0, %1, %2 sc1" : "+v"(sink) : "v"(vo), "s"(sb) : "memory")
#define COUNT(i)
__device__ __forceinline__ void st_flag(unsigned char* p, unsigned tag) {
  const unsigned z = 0;
  asm volatile("s_mov_b64 exec, 1\n\tglobal_store_dword %0, %1, %2 sc1\n\ts_mov_b64 exec, -1" ::"v"(z), "v"(tag), "s"(p) : "memory");
}
#define GLDS(vo, sb, lds, imm) asm volatile("s_mov_b32 m0, %2\n\ts_nop 0\n\tglobal_load_lds_dwordx4 %0, %1 offset:%3" ::"v"(vo), "s"(sb), "s"(lds), "i"(imm) : "memory")
#define GLDSC(vo, sb, lds, imm) asm volatile("s_mov_b32 m0, %2\n\ts_nop 0\n\tglobal_load_lds_dwordx4 %0, %1 offset:%3 sc1" ::"v"(vo), "s"(sb), "s"(lds), "i"(imm) : "memory")

__device__ __forceinline__ unsigned sum4(unsigned m, v4u v) { return (m + v[0] + v[1]) + v[2] + v[3]; }
__device__ __forceinline__ void poll_flags(cptr base, unsigned tag, int lane) {
  const unsigned vo = lane * 128;
  for (unsigned spin = 0; spin < SPIN_CAP; ++spin) {
    v4u v;
    asm volatile("global_load_dwordx4 %0, %1, %2 sc1\n\ts_waitcnt vmcnt(0)" : "=&v"(v) : "v"(vo), "s"(base) : "memory");
    if (__all(v[0] == tag && v[1] == tag && v[2] == tag && v[3] == tag)) break;
  }
}
__device__ __forceinline__ void last_step_reduce(unsigned char* red, int w, int lane, const v4u (&wt)[4], const v4u (&fc)[4],
                                                 f32x4 (&acc)[4][4], v4u (&o)[2]) {
#pragma unroll
  for (int c = 1; c <= 4; ++c) {
#pragma unroll
    for (int i = 0; i < 4; ++i) {
      acc[i][c & 3] = mfma(wt[i], fc[c & 3], acc[i][c & 3]);
      SCHED();
      if (c >= 2) *(f32x4*)(red + (((c - 1) ^ w) * 3 + c - 2) * 4096 + lane * 16 + i * 1024) = acc[i][c - 1];
      SCHED();
    }
  }
  BAR();
  const unsigned char* s = red + w * 12288 + lane * 16;
  f32x4 pt[3][4];
#pragma unroll
  for (int i = 0; i < 4; ++i)
#pragma unroll
    for (int c = 0; c < 3; ++c) pt[c][i] = *(const f32x4*)(s + c * 4096 + i * 1024);
  SCHED();
#pragma unroll
  for (int i = 0; i < 4; ++i) {
    const f32x4 y = (acc[i][0] + pt[0][i]) + (pt[1][i] + pt[2][i]);
    o[i >> 1][2 * (i & 1)] = pk2(y[0], y[1]);
    o[i >> 1][2 * (i & 1) + 1] = pk2(y[2], y[3]);
  }
}

#define NP0 8
#define FL0_CHK 11
#define CODE_KB 48
#define W1A 0
#define W2A 0
#define W2C 12
#define WDS 12
#define XT0 19
#define DB 5
#define NPA 16
constexpr int D2 = 4, D4 = 5, FL0_PUB = 3, FL0_IT = FL0_CHK - 3, NV = FL0_CHK + DB, NX = 41 - WDS;
constexpr int RA0 = NPA == 16 ? 3 : 0, RAD = NPA - RA0;

template <int I, int N, class F> __device__ __forceinline__ void static_for(F&& f) {
  if constexpr (I < N) { f(std::integral_constant<int, I>{}); static_for<I + 1, N>(f); }
}
constexpr bool p1_ra(int s) { return s >= RA0 && s + RAD < 32; }
constexpr int p1_xbase(int s) { int n = 0; for (int u = XT0; u < s; ++u) n += (u + DB < 32 ? 2 : 6); return n > NX ? NX : n; }
constexpr int p1_extra(int s) { return s < XT0 ? 0 : p1_xbase(s + 1) - p1_xbase(s); }
static_assert(p1_xbase(32) == NX && !p1_ra(XT0) && XT0 > FL0_CHK && (FL0_CHK & 3) == 3 && DB <= 7 && (NPA == 16 || NPA <= 12), "");
constexpr int p1_wait(int it, bool mid) {
  int posA[48] = {}, posB[40] = {}, posF = -1, n = 0, pos0 = 5 * DB;
  for (int s = 0; s < 48; ++s) posA[s] = -1;
  for (int s = 0; s < DB; ++s) { n += 5; posB[s] = n - 1; }
  if (it < 0) return n - 1 - posB[0];
  for (int s = 0; s <= it; ++s) {
    if (s == it && !mid) break;
    if (s == FL0_IT) posF = n - 1;
    if (p1_ra(s)) { n += 2; posA[s + RAD] = n - 1; }
    if (s == it) { n += p1_extra(s) > 2 ? 2 : p1_extra(s); break; }
    n += p1_extra(s);
    if (s + DB < 32) { n += (s + DB < NV ? 5 : 4); posB[s + DB] = n - 1; }
  }
  int req = -1;
  if (mid) req = posB[it + 1];
  else {
    if (it + 4 < 32) req = posA[it + 4];
    if (it == FL0_CHK && posF > req) req = posF;
    if (it == FL0_PUB && pos0 > req) req = pos0;
  }
  return n - 1 - req > 63 ? 63 : n - 1 - req;
}
constexpr int p2_w1(int s) { int n = 28 - W1A - 4 * (s - (8 - D2)); return (s + D2 < 8 || s == 7) ? 0 : n > 4 ? 4 : n < 0 ? 0 : n; }
constexpr int p2_wait(int k) {
  int n = 0;
  for (int g = k + 1; g < 8 && g < k - 1 + D2; ++g) n += 5;
  for (int s = 0; s < k - 1; ++s) n += p2_w1(s);
  return n + ((k - 1 + D2 < 8) ? 3 : (p2_w1(k - 1) > 2 ? 2 : p2_w1(k - 1)));
}
constexpr int W2B = 56 - W2A - W2C;
constexpr int p3_w2(int s) { int n = W2B - 4 * (s - 5); return s < 5 ? 0 : n > 4 ? 4 : n < 0 ? 0 : n; }
constexpr int p3_ops(int s) { return s < 4 ? 6 : s == 4 ? 4 : p3_w2(s); }
constexpr int p3_wait(int m) {
  int n = m < 4 ? 6 * (3 - m) : 0;
  for (int s = (m < 4 ? 0 : m - 3); s < 2 * m; ++s) n += p3_ops(s);
  return n;
}
constexpr int P3_ST = 12;
constexpr int p3_stwait() { int n = 0; for (int s = 5; s < P3_ST; ++s) n += p3_ops(s); return n; }
constexpr int p4_rec(int g) { return (g == 0 || g == 3 || g == 7 || g == 10) ? 2 : 0; }
constexpr int p4_wait(int k) {
  int n = 0;
  for (int g = k + 1; g < 14 && g < k - 1 + D4; ++g) n += 4 + p4_rec(g);
  return n + ((k - 1 + D4 < 14) ? 2 + p4_rec(k - 1 + D4) : 0);
}
static_assert(W2B >= 0 && W2B <= 44 && W1A >= 0 && W1A <= 28 && W1A % 4 == 0, "");
struct Tab { int v[16]; };
template <int (*F)(int)> constexpr Tab mk(int lo, int hi) { Tab t = {}; for (int k = lo; k < hi; ++k) t.v[k] = F(k); return t; }

__global__ __launch_bounds__(256, 1) void fnet_fused(cptr x, cptr wsre, cptr wsim, cptr wre, cptr wim, cptr w1, cptr w2,
                                                     unsigned char* ws, unsigned char* out) {
  extern __shared__ __attribute__((aligned(16))) unsigned char smem[];
  const int bid = blockIdx.x, p = bid & 7, j = bid >> 3;
  const int ti = 4 * p + (j >> 3), cg = j & 7;
#define MAP3 1
  const int t128 = MAP3 ? 4 * (p >> 1) + (j >> 3) : 2 * p + (j >> 4), fs = MAP3 ? 8 * (p & 1) + (j & 7) : j & 15;
  const int tid = threadIdx.x, lane = tid & 63, w = __builtin_amdgcn_readfirstlane(tid >> 6), q = lane >> 4, r = lane & 15;
  const unsigned lds0 = (unsigned)(size_t)(LAS unsigned char*)smem;
  const unsigned vl16 = lane * 16, vl4 = lane * 4;
  unsigned long long ta_ = 0, tb_ = 0;

  const int mat = (j >> 2) & 1, nt = j & 3;
  unsigned epoch, sink = 0;
  v4u xr[2];
  {
    const unsigned vo = (lane >> 5) * 1024 + (lane & 31) * 16, zero = 0;
    cptr xb = x + (size_t)(8 * bid + 2 * w) * 1024;
    LD16(xr[0], vo, xb, 0);
    LD16(xr[1], vo, xb, 512);
    LD4C(epoch, zero, ws + EPOCH_OFF, 0);
  }
  const unsigned va0 = (lane >> 3) * 4096 + (((lane & 7) ^ (lane >> 4)) << 4), va1 = (va0 ^ 64) + 32768 - 1024;
#define ROT 1
  const int rot = ROT ? (nt & 1) : 0;
  cptr abase = (mat ? wsim : wsre) + (size_t)(64 * ti + 16 * w) * 4096;
  cptr abase0 = abase + rot * 2048, abase1 = abase - rot * 2048;
  const unsigned lda = lds0 + w * 2048;
#define A_STAGE0(s) GLDS(va0, ((s) < 16 ? abase0 : abase1) + (s) * 128, lda + ((s) & 15) * 8192, 0)
#define A_STAGE1(s) GLDS(va1, ((s) < 16 ? abase0 : abase1) + (s) * 128, lda + ((s) & 15) * 8192, 1024)
  const int kb1 = bid >> 4, fs1 = bid & 15, ob = bid < 224 ? bid : 223, kbd = bid & 31;
  {
    cptr pc;
    asm volatile("s_getpc_b64 %0" : "=s"(pc));
    const unsigned vo = ((4 * j + w) * (CODE_KB / 16) + (lane < CODE_KB / 16 ? lane : 0)) * 128;
    TOUCH(vo, pc);
  }
#define TLBW 0
  if (TLBW) {
#define TLB_STEP (512 * 1024)
    cptr sb = w == 0 ? ws : w == 1 ? w1 : w == 2 ? w2 : (cptr)out;
    const unsigned lim = w == 0 ? (CKX_OFF + 262144) / TLB_STEP : 1835008 / TLB_STEP;
    const unsigned vo = (lane <= lim ? lane : 0) * TLB_STEP;
    TOUCHC(vo, sb);
    const unsigned z = 0;
    TOUCH(z, w & 1 ? wim : wre);
  }
#pragma unroll 1
  for (int s = 0; s < NP0; ++s) { A_STAGE0(s); A_STAGE1(s); }
  WAITV(2 * NP0 + 1 + (TLBW ? 2 : 0));
  asm volatile("" : "+v"(xr[0]), "+v"(xr[1]), "+v"(epoch));
  const unsigned tag = __builtin_amdgcn_readfirstlane(epoch) + 1u;

  {
    float v[16];
#pragma unroll
    for (int i = 0; i < 2; ++i)
#pragma unroll
      for (int k = 0; k < 4; ++k) { v[8 * i + 2 * k] = bflo(xr[i][k]); v[8 * i + 2 * k + 1] = bfhi(xr[i][k]); }
    float s1[4] = {0.f, 0.f, 0.f, 0.f};
#pragma unroll
    for (int k = 0; k < 16; ++k) s1[k & 3] += v[k];
    const float mean = half_sum((s1[0] + s1[1]) + (s1[2] + s1[3])) * (1.0f / D_);
    float s2[4] = {0.f, 0.f, 0.f, 0.f};
#pragma unroll
    for (int k = 0; k < 16; ++k) { const float c = v[k] - mean; s2[k & 3] += c * c; }
    const float mb = rbf(mean), rd = ln_rstd(half_sum((s2[0] + s2[1]) + (s2[2] + s2[3])) * (1.0f / D_));
#pragma unroll
    for (int i = 0; i < 2; ++i) {
      v4u o;
#pragma unroll
      for (int k = 0; k < 4; ++k) {
        const unsigned c2 = pk2(v[8 * i + 2 * k] - mb, v[8 * i + 2 * k + 1] - mb);
        o[k] = pk2(bflo(c2) * rd, bfhi(c2) * rd);
      }
      *(v4u*)(smem + XS_OFF + (2 * w + (lane >> 5)) * XS_ROW + (lane & 31) * 16 + i * 512) = o;
    }
  }
  BAR();

  auto tr8 = [&](int off, int i) {
    const unsigned char* s = smem + off + ((lane >> 2) & 3) * XS_ROW + 64 * (q >> 1) + 16 * (lane & 3) + 8 * (q & 1) + 128 * i;
    const v2u lo = __builtin_bit_cast(v2u, __builtin_amdgcn_ds_read_tr16_b64_v4i16((LAS v4s*)s));
    const v2u hi = __builtin_bit_cast(v2u, __builtin_amdgcn_ds_read_tr16_b64_v4i16((LAS v4s*)(s + 4 * XS_ROW)));
    return v4u{lo[0], lo[1], hi[0], hi[1]};
  };
  {
    const v4u o0 = tr8(XS_OFF, 2 * w), o1 = tr8(XS_OFF, 2 * w + 1);
    const unsigned vo = (q >> 1) * 131072 + (q & 1) * 1024 + r * 16;
    unsigned char* sb = ws + XNT_OFF + (bid >> 2) * 2048 + (bid & 3) * 256 + w * 524288;
    ST16C(o0, vo, sb, 0);
    ST16C(o1, vo, sb + 262144, 0);
    auto sw = __builtin_amdgcn_permlane16_swap(sum4(0u, o0), sum4(0u, o1), false, false);
    const unsigned ck = tag + sw[0] + sw[1];
    const unsigned vc = ((q >> 1) + 2 * (q & 1)) * 16384 + r * 8;
    ST4C(ck, vc, ws + CKX_OFF + w * 65536 + (bid >> 3) * 512 + (bid & 3) * 128 + ((bid >> 2) & 1) * 4, 0);
  }
  const int wst = WST_OFF + w * 5952;
  if (w < 2) {
    const unsigned vo = (lane >> 5) * 1024 + (lane & 31) * 16;
    cptr sb = w2 + (size_t)(8 * ob) * 1024 + w * 512;
#pragma unroll
    for (int k = 0; k < 4; ++k) GLDS(vo, sb + k * 2048, lds0 + wst + k * 1088, 0);
  } else {
    const unsigned vo = (lane >> 3) * (F_ * 2) + (lane & 7) * 16;
    cptr sb = w1 + (size_t)(32 * kb1) * (F_ * 2) + ((7 * fs1) >> 1) * 64 + (w - 2) * 128;
#pragma unroll
    for (int k = 0; k < 4; ++k) GLDS(vo, sb + k * (8 * F_ * 2), lds0 + wst + k * 1088, 0);
  }
  v4u wdp;
  {
    const unsigned vo = (8 * (r >> 2) + (r & 3)) * 1024 + q * 16;
    LD16(wdp, vo, (kbd < 16 ? wre : wim) + (size_t)(64 * (bid >> 5) + 32 * (w >> 1) + 4 * (w & 1)) * 1024 + (kbd & 15) * 64, 0);
  }
#pragma unroll 1
  for (int s = NP0; s < NPA; ++s) { A_STAGE0(s); A_STAGE1(s); }
  asm volatile("" : "+v"(sink));

  cptr bbase0 = ws + XNT_OFF + (4 * nt + w) * 131072 + rot * 65536, bbase1 = bbase0 - rot * 131072;
  cptr xcb0 = ws + CKX_OFF + (4 * nt + w) * 16384 + rot * 8192, xcb1 = xcb0 - rot * 16384;
#define BB(s) ((s) < 16 ? bbase0 : bbase1)
#define XCB(s) ((s) < 16 ? xcb0 : xcb1)
  const unsigned vl8 = lane * 8, tag2 = 2u * tag;
  v4u bf[8][4];
  v2u bc[8];
#define B_LOAD(s, i) LD16C(bf[(s) & 7][i], vl16, BB(s) + (s) * 4096, (i) * 1024)
#define B_CHK(s) LD8C(bc[(s) & 7], vl8, XCB(s) + ((s) >> 3) * 4096, ((s) & 7) * 512)
#pragma unroll
  for (int s = 0; s < DB; ++s) { B_CHK(s); B_LOAD(s, 0); B_LOAD(s, 1); B_LOAD(s, 2); B_LOAD(s, 3); }

  asm volatile("s_waitcnt vmcnt(%1)" : "+v"(wdp) : "i"(5 * DB) : "memory");
  if (kbd >= 16) wdp ^= 0x80008000u;
  ST16C(wdp, vl16, ws + WD_OFF + bid * 4096 + w * 1024, 0);
  if (w < 2) {
    const int rr = (lane >> 2) & 3;
    const unsigned char* s = smem + wst + (rr >> 1) * 1088 + (rr & 1) * 512 + 64 * (q >> 1) + 16 * (lane & 3) + 8 * (q & 1);
    const unsigned vo = q * 1024 + r * 16;
    unsigned char* sb = ws + W2_OFF + (ob >> 2) * 4096 + (ob & 3) * 256 + w * (4 * 229376);
#pragma unroll
    for (int i = 0; i < 4; ++i) {
      const v2u lo = __builtin_bit_cast(v2u, __builtin_amdgcn_ds_read_tr16_b64_v4i16((LAS v4s*)(s + 128 * i)));
      const v2u hi = __builtin_bit_cast(v2u, __builtin_amdgcn_ds_read_tr16_b64_v4i16((LAS v4s*)(s + 128 * i + 2176)));
      const v4u o = {lo[0], lo[1], hi[0], hi[1]};
      ST16C(o, vo, sb + i * 229376, 0);
    }
  } else {
    const unsigned char* s = smem + wst + q * 1088 + ((lane >> 2) & 3) * 128 + 16 * (lane & 3);
    float csv = 0.f;
    auto pos = [&](int tl) { return (fs1 & 1) ? (tl == 0 ? 7 : tl == 1 ? 6 : tl - 2) : tl; };
#pragma unroll
    for (int i = 0; i < 4; ++i) {
      const int l = pos(4 * (w - 2) + i);
      const unsigned char* sl = s + (i >> 1) * 64 + (i & 1) * 8;
      const v2u lo = __builtin_bit_cast(v2u, __builtin_amdgcn_ds_read_tr16_b64_v4i16((LAS v4s*)sl));
      const v2u hi = __builtin_bit_cast(v2u, __builtin_amdgcn_ds_read_tr16_b64_v4i16((LAS v4s*)(sl + 512)));
      const v4u o = {lo[0], lo[1], hi[0], hi[1]};
      if (l < 7) ST16C(o, vl16, ws + W1_OFF + fs1 * 114688 + kb1 * 7168 + l * 1024, 0);
      const float cs = rows_sum(((bflo(o[0]) + bfhi(o[0])) + (bflo(o[1]) + bfhi(o[1]))) + ((bflo(o[2]) + bfhi(o[2])) + (bflo(o[3]) + bfhi(o[3]))));
      csv = (q == i) ? cs : csv;
    }
    const unsigned vc = pos(4 * (w - 2) + q) * 1024 + r * 4;
    ST4C(csv, vc, ws + CS_OFF + fs1 * 8192 + kb1 * 64, 0);
  }
  f32x4 acc1[2][4];
#pragma unroll
  for (int i = 0; i < 2; ++i)
#pragma unroll
    for (int t = 0; t < 4; ++t) acc1[i][t] = f32x4{0.f, 0.f, 0.f, 0.f};
  const unsigned ra0 = r * 128 + ((q ^ (r >> 1)) << 4), ra1 = ra0 ^ 64;
  v4u af[2][8];
  v4u xres[2], csr[7], wd[8][4];
  v4u flw = {tag, tag, tag, tag};
  const unsigned vl128 = lane * 128;
  cptr xb2 = x + (size_t)(64 * ti + 16 * w) * 1024 + cg * 128;
  const unsigned vx = r * 1024 + q * 16;
  cptr cb2 = ws + CS_OFF + fs * 8192;
  const unsigned vc2 = r * 64 + q * 16;
  cptr wb = ws + WD_OFF + cg * 131072 + w * 32768;
#define BAD(M) (__builtin_amdgcn_ballot_w64((M) != 0u) != 0)
#define SUM16(M0, F) sum4(sum4(sum4(sum4(M0, F[0]), F[1]), F[2]), F[3])
#define B_WAIT(s, N, TAIL) asm volatile("s_waitcnt vmcnt(%5)" TAIL : "+v"(bf[(s) & 7][0]), "+v"(bf[(s) & 7][1]), "+v"(bf[(s) & 7][2]), "+v"(bf[(s) & 7][3]), "+v"(bc[(s) & 7]) : "i"(N) : "memory")
  B_WAIT(0, p1_wait(-1, false), " lgkmcnt(0)\n\ts_barrier");
#pragma unroll
  for (int t = 0; t < 4; ++t) { af[0][t] = *(const v4u*)(smem + ra0 + t * 2048); af[0][4 + t] = *(const v4u*)(smem + ra1 + t * 2048); }
  unsigned bx = SUM16(tag2 - bc[0][0] - bc[0][1], bf[0]);

#define P1_EXTRA(e)                                                                                                     \
  if constexpr ((e) < p1_extra(it)) {                                                                                   \
    constexpr int n_ = p1_xbase(it) + (e);                                                                              \
    if constexpr (n_ < 2) LD16(xres[n_], vx, xb2, n_ * 64);                                                             \
    else if constexpr (n_ < 9) LD16C(csr[n_ - 2], vc2, cb2 + ((n_ - 2) >> 2) * 4096, ((n_ - 2) & 3) * 1024);            \
    else LD16CA(wd[(n_ - 9) >> 2][(n_ - 9) & 3], vl16, wb + ((n_ - 9) >> 2) * 4096, ((n_ - 9) & 3) * 1024);             \
  }
#define MM1(i, t, kb) acc1[i][t] = mfma(bf[U][2 * (kb) + (i)], af[C][4 * (kb) + (t)], acc1[i][t]); SCHED()
#define RD1(n) if constexpr (RN) { af[Nx][n] = *(const v4u*)(sn + ((n) < 4 ? ra0 : ra1) + ((n) & 3) * 2048); SCHED(); }
#define CK1(i) if constexpr (it + 1 < NV) { bx = sum4((i) == 0 ? tag2 - bc[Un][0] - bc[Un][1] : bx, bf[Un][i]); SCHED(); }
  static_for<0, 32>([&](auto IT) {
    constexpr int it = IT, U = it & 7, Un = (it + 1) & 7, C = it & 1, Nx = C ^ 1;
    constexpr bool RA = p1_ra(it), RB = it + DB < 32, RN = it + 1 < 32;
    if constexpr (it < NV) {
      if (__builtin_expect(BAD(bx), 0)) {
        unsigned spin = SPIN_CAP;
        WAITV(0);
        do {
          COUNT(0);
          v2u c_;
          asm volatile("global_load_dwordx2 %4, %6, %8 sc1\n\tglobal_load_dwordx4 %0, %5, %7 sc1\n\tglobal_load_dwordx4 %1, %5, %7 offset:1024 sc1\n\t"
                       "global_load_dwordx4 %2, %5, %7 offset:2048 sc1\n\tglobal_load_dwordx4 %3, %5, %7 offset:3072 sc1\n\ts_waitcnt vmcnt(0)"
                       : "=&v"(bf[U][0]), "=&v"(bf[U][1]), "=&v"(bf[U][2]), "=&v"(bf[U][3]), "=&v"(c_)
                       : "v"(vl16), "v"(vl8), "s"(BB(it) + it * 4096), "s"(XCB(it) + it * 512) : "memory");
          if (!BAD(SUM16(tag2 - c_[0] - c_[1], bf[U]))) spin = 1;
        } while (--spin);
      }
    }
    if constexpr (it == FL0_CHK) {
      asm volatile("s_waitcnt vmcnt(%1)" : "+v"(flw) : "i"(p1_wait(it, false)) : "memory");
      if (!__all(flw[0] == tag && flw[1] == tag && flw[2] == tag && flw[3] == tag)) { COUNT(1); poll_flags(ws + FLAG0_OFF, tag, lane); }
    }
    if constexpr ((it & 3) == 3) WAITV_BAR(p1_wait(it, false));
    if constexpr (it == FL0_PUB) { if (w == 0) st_flag(ws + FLAG0_OFF + (bid & 63) * 128 + (bid >> 6) * 4, tag); }
    if constexpr (it == FL0_IT)
      asm volatile("s_cmp_lg_u32 %3, 0\n\ts_cbranch_scc1 1f\n\tglobal_load_dwordx4 %0, %1, %2 sc1\n1:"
                   : "+v"(flw) : "v"(vl128), "s"(ws + FLAG0_OFF), "s"(w) : "memory", "scc");
    const unsigned char* sn = smem + ((it + 1) & 15) * 8192;
    SCHED();
    MM1(0, 0, 0); RD1(0);
    MM1(1, 0, 0); if constexpr (RA) A_STAGE0(it + RAD); P1_EXTRA(0) SCHED();
    MM1(0, 1, 0); RD1(1);
    MM1(1, 1, 0); RD1(2);
    MM1(0, 2, 0); if constexpr (RA) A_STAGE1(it + RAD); P1_EXTRA(1) SCHED();
    MM1(1, 2, 0); RD1(3); if constexpr (RN) { B_WAIT(it + 1, p1_wait(it, true), ""); SCHED(); }
    MM1(0, 3, 0); if constexpr (RB && it + DB < NV) B_CHK(it + DB); if constexpr (RB) B_LOAD(it + DB, 0); P1_EXTRA(2) SCHED();
    MM1(1, 3, 0); RD1(4); CK1(0);
    MM1(0, 0, 1); RD1(5); CK1(1);
    MM1(1, 0, 1); if constexpr (RB) B_LOAD(it + DB, 1); P1_EXTRA(3) SCHED();
    MM1(0, 1, 1); RD1(6); CK1(2);
    MM1(1, 1, 1); if constexpr (RB) B_LOAD(it + DB, 2); P1_EXTRA(4) SCHED();
    MM1(0, 2, 1); RD1(7); CK1(3);
    MM1(1, 2, 1); if constexpr (RB) B_LOAD(it + DB, 3); P1_EXTRA(5) SCHED();
    MM1(0, 3, 1);
    MM1(1, 3, 1);
  });

  {
    const int kbq = ti * 32 + 16 * mat + 4 * nt + w;
    unsigned char* pq = ws + PQ_OFF + kbq * 4096;
    unsigned ck = tag;
#pragma unroll
    for (int t = 0; t < 4; ++t) {
      const v4u o = {pk2(acc1[0][t][0], acc1[0][t][1]), pk2(acc1[0][t][2], acc1[0][t][3]),
                     pk2(acc1[1][t][0], acc1[1][t][1]), pk2(acc1[1][t][2], acc1[1][t][3])};
      ST16C(o, vl16, pq + t * 1024, 0);
      ck = sum4(ck, o);
    }
    ST4C(ck, vl4, ws + CKP_OFF + kbq * 256, 0);
    if (bid == 0 && w == 0) st_flag(ws + EPOCH_OFF, tag);
  }
#pragma unroll
  for (int n = 32 - WDS; n < 32; ++n) LD16CA(wd[n >> 2][n & 3], vl16, wb + (n >> 2) * 4096, (n & 3) * 1024);
  cptr w1src = ws + W1_OFF + fs * 114688 + w * 28672;
  const unsigned w1dst = lds0 + w * 28672;
#define W1L(n) GLDSC(vl16, w1src + ((n) >> 2) * 4096, w1dst + ((n) >> 2) * 4096, ((n) & 3) * 1024)
#pragma unroll 1
  for (int n = 0; n < W1A / 4; ++n) {
    GLDSC(vl16, w1src + n * 4096, w1dst + n * 4096, 0);
    GLDSC(vl16, w1src + n * 4096, w1dst + n * 4096, 1024);
    GLDSC(vl16, w1src + n * 4096, w1dst + n * 4096, 2048);
    GLDSC(vl16, w1src + n * 4096, w1dst + n * 4096, 3072);
  }

#define LOAD4_WAIT(F, SB, VR)                                                                                               \
  asm volatile("global_load_dwordx4 %0, %4, %8 sc1\n\tglobal_load_dwordx4 %1, %5, %8 sc1\n\t"                           \
               "global_load_dwordx4 %2, %6, %8 sc1\n\tglobal_load_dwordx4 %3, %7, %8 sc1\n\ts_waitcnt vmcnt(0)"         \
               : "=&v"(F[0]), "=&v"(F[1]), "=&v"(F[2]), "=&v"(F[3])                                                     \
               : "v"(VR(0)), "v"(VR(1)), "v"(VR(2)), "v"(VR(3)), "s"(SB) : "memory")
#define STEP64(WT, FC, FN, HASN, WAITN, M0, OP0, OP1, OP2, OP3)                                                         \
  {                                                                                                                     \
    SCHED();                                                                                                            \
    acc[0][0] = mfma(WT[0], FC[0], acc[0][0]); SCHED(); OP0; SCHED();                                                   \
    acc[0][1] = mfma(WT[0], FC[1], acc[0][1]); acc[0][2] = mfma(WT[0], FC[2], acc[0][2]);                               \
    acc[0][3] = mfma(WT[0], FC[3], acc[0][3]); acc[1][0] = mfma(WT[1], FC[0], acc[1][0]); SCHED(); OP1; SCHED();        \
    acc[1][1] = mfma(WT[1], FC[1], acc[1][1]); SCHED();                                                                 \
    if (HASN) { WAITN; }                                                                                                \
    SCHED();                                                                                                            \
    acc[1][2] = mfma(WT[1], FC[2], acc[1][2]); SCHED(); if (HASN) mx = (M0); SCHED();                                   \
    acc[1][3] = mfma(WT[1], FC[3], acc[1][3]); SCHED(); if (HASN) mx = sum4(mx, FN[0]); SCHED();                        \
    acc[2][0] = mfma(WT[2], FC[0], acc[2][0]); SCHED(); OP2; SCHED();                                                   \
    acc[2][1] = mfma(WT[2], FC[1], acc[2][1]); SCHED(); if (HASN) mx = sum4(mx, FN[1]); SCHED();                        \
    acc[2][2] = mfma(WT[2], FC[2], acc[2][2]); SCHED(); if (HASN) mx = sum4(mx, FN[2]); SCHED();                        \
    acc[2][3] = mfma(WT[2], FC[3], acc[2][3]); SCHED(); if (HASN) mx = sum4(mx, FN[3]); SCHED();                        \
    acc[3][0] = mfma(WT[3], FC[0], acc[3][0]); SCHED(); OP3; SCHED();                                                   \
    acc[3][1] = mfma(WT[3], FC[1], acc[3][1]); acc[3][2] = mfma(WT[3], FC[2], acc[3][2]);                               \
    acc[3][3] = mfma(WT[3], FC[3], acc[3][3]); SCHED();                                                                 \
  }

  unsigned vrot[4];
#pragma unroll
  for (int c = 0; c < 4; ++c) vrot[c] = vl16 + (c ^ w) * 1024;
#define VROT(c) vrot[c]
  v4u hres[2];
  v4u w2f[14][4];
  cptr w2base = ws + W2_OFF + cg * 229376 + w * 57344;
#define W2L(n) do { if ((n) < 44) LD16CA(w2f[(n) >> 2][(n) & 3], vl16, w2base + ((n) >> 2) * 4096, ((n) & 3) * 1024);   \
                    else LD16C(w2f[(n) >> 2][(n) & 3], vl16, w2base + ((n) >> 2) * 4096, ((n) & 3) * 1024); } while (0)
  {
    constexpr Tab p2t = mk<p2_wait>(1, 8), p2w = mk<p2_w1>(0, 8);
    f32x4 acc[4][4];
    v4u pq[8][4];
    unsigned ck[8];
    cptr pb = ws + PQ_OFF + ti * 131072 + w * 32768;
    cptr cb = ws + CKP_OFF + (ti * 32 + 8 * w) * 256;
#pragma unroll
    for (int k = 0; k < D2; ++k) {
      LD4C(ck[k], vl4, cb, k * 256);
#pragma unroll
      for (int c = 0; c < 4; ++c) LD16C(pq[k][c], vrot[c], pb + k * 4096, 0);
    }
    asm volatile("s_waitcnt vmcnt(%2)" : "+v"(xres[0]), "+v"(xres[1]) : "i"(5 * D2) : "memory");
#pragma unroll
    for (int i = 0; i < 4; ++i) {
      acc[i][0] = unpk(xres[i >> 1][2 * (i & 1)], xres[i >> 1][2 * (i & 1) + 1]);
#pragma unroll
      for (int c = 1; c < 4; ++c) acc[i][c] = f32x4{0.f, 0.f, 0.f, 0.f};
    }
#define P2WAIT(g, N) asm volatile("s_waitcnt vmcnt(%5)" : "+v"(pq[g][0]), "+v"(pq[g][1]), "+v"(pq[g][2]), "+v"(pq[g][3]), "+v"(ck[g]) : "i"(N) : "memory")
    P2WAIT(0, 5 * D2 - 5);
    unsigned mx = SUM16(tag - ck[0], pq[0]);

#define P2OP(i) do { if (k + D2 < 8) { if ((i) == 0) LD4C(ck[(k + D2) & 7], vl4, cb, ((k + D2) & 7) * 256);              \
                                       LD16C(pq[(k + D2) & 7][i], vrot[i], pb + (k + D2) * 4096, 0); }                  \
                     else if ((i) < p2w.v[k]) W1L(W1A + 4 * (k + D2 - 8) + (i)); } while (0)
#pragma unroll
    for (int k = 0; k < 8; ++k) {
      if (__builtin_expect(BAD(mx), 0)) {
        unsigned spin = SPIN_CAP;
        WAITV(0);
        do {
          COUNT(1);
          unsigned c_;
          LD4C(c_, vl4, cb, k * 256);
          LOAD4_WAIT(pq[k], pb + k * 4096, VROT);
          asm volatile("" : "+v"(c_));
          if (!BAD(SUM16(tag - c_, pq[k]))) spin = 1;
        } while (--spin);
      }
      PIN4A(wd[k][0], wd[k][1], wd[k][2], wd[k][3]);
      if (k < 7) STEP64(wd[k], pq[k], pq[(k + 1) & 7], true, P2WAIT((k + 1) & 7, p2t.v[(k + 1) & 7]), tag - ck[(k + 1) & 7], P2OP(0), P2OP(1), P2OP(2), P2OP(3));
    }

    last_step_reduce(smem + RED_OFF, w, lane, wd[7], pq[7], acc, hres);
    const int hp = (4 * ti + w) * 8 + cg;
    ST16C(hres[0], vl16, ws + H_OFF + hp * 2048, 0);
    ST16C(hres[1], vl16, ws + H_OFF + hp * 2048, 1024);
    const unsigned hck = sum4(sum4(tag, hres[0]), hres[1]);
    ST4C(hck, vl4, ws + CKH_OFF + hp * 256, 0);
    f32x4 hv[4];
#pragma unroll
    for (int i = 0; i < 4; ++i) hv[i] = unpk(hres[i >> 1][2 * (i & 1)], hres[i >> 1][2 * (i & 1) + 1]);
    const f32x4 s4 = (hv[0] + hv[1]) + (hv[2] + hv[3]);
    const float m = rows_sum((s4[0] + s4[1]) + (s4[2] + s4[3])) * (1.0f / 64.0f);
    f32x4 q4 = {0.f, 0.f, 0.f, 0.f};
#pragma unroll
    for (int i = 0; i < 4; ++i) { const f32x4 c = hv[i] - m; q4 += c * c; }
    const float m2 = rows_sum((q4[0] + q4[1]) + (q4[2] + q4[3]));
    const unsigned mu = __builtin_bit_cast(unsigned, m), m2u = __builtin_bit_cast(unsigned, m2);
    const v4u st = {mu, m2u, tag + mu + m2u, 0u};
    asm volatile("s_mov_b64 exec, 0xffff\n\tglobal_store_dwordx4 %0, %1, %2 sc1\n\ts_mov_b64 exec, -1\n\ts_nop 0" ::"v"(vl16), "v"(st), "s"(ws + ST_OFF + hp * 256) : "memory");
  }

#pragma unroll
  for (int n = W1A + 4 * (D2 - 1); n < 28; ++n) W1L(n);
#pragma unroll
  for (int n = 0; n < W2A; ++n) W2L(n);
  v2u csf[7];
  {
    unsigned char* csl = smem + RED_OFF + w * 12288;
#pragma unroll
    for (int l = 0; l < 7; ++l) {
      asm volatile("" : "+v"(csr[l]));
      f32x4 c;
#pragma unroll
      for (int k = 0; k < 4; ++k) c[k] = row16_sum(asf(csr[l][k]));
      *(f32x4*)(csl + q * 16 + l * 64) = c;
    }
#pragma unroll
    for (int l = 0; l < 7; ++l) {
      const float c = *(const float*)(csl + r * 4 + l * 64);
      const float c0 = rbf(c), c1 = rbf(c - c0);
      csf[l] = v2u{pk2(c0, c1), pk2(c - c0 - c1, 0.f)};
    }
  }

  {
    constexpr Tab p3t = mk<p3_wait>(0, 8), p3w = mk<p3_w2>(0, 16);
    v4u hf[8][4];
    unsigned ck[8][2];
    v4u str[2][2];
    cptr hb = ws + H_OFF + (8 * t128 + 2 * w) * 16384;
    cptr cb = ws + CKH_OFF + (8 * t128 + 2 * w) * 2048;
    const unsigned vh1 = vl16 + 16384;
    const unsigned vs = q * 512 + r * 16;
    cptr stb = ws + ST_OFF + (8 * t128 + 2 * w) * 2048;
#define HL(m, i) LD16C(hf[m][i], ((i) & 1) ? vh1 : vl16, hb + ((m) >> 1) * 4096, ((m) & 1) * 2048 + ((i) >> 1) * 1024)
#define HC(m, t) LD4C(ck[m][t], vl4, cb, (t) * 2048 + (m) * 256)
#pragma unroll
    for (int m = 0; m < 4; ++m) { HC(m, 0); HC(m, 1); HL(m, 0); HL(m, 1); HL(m, 2); HL(m, 3); }
    f32x4 acc[7][2];
#pragma unroll
    for (int l = 0; l < 7; ++l) { acc[l][0] = f32x4{0.f, 0.f, 0.f, 0.f}; acc[l][1] = f32x4{0.f, 0.f, 0.f, 0.f}; }
#define P3WAIT(m, N, TAIL) asm volatile("s_waitcnt vmcnt(%6)" TAIL : "+v"(hf[m][0]), "+v"(hf[m][1]), "+v"(hf[m][2]), "+v"(hf[m][3]), "+v"(ck[m][0]), "+v"(ck[m][1]) : "i"(N) : "memory")
    P3WAIT(0, p3t.v[0], " lgkmcnt(0)\n\ts_barrier");
    asm volatile("s_memtime %0\n\ts_waitcnt lgkmcnt(0)" : "=s"(ta_) :: "memory");
    unsigned whi_ = lds0 + vl16 + 57344;
    asm volatile("" : "+v"(whi_));
    const LAS unsigned char* wlo = (const LAS unsigned char*)smem + vl16;
    const LAS unsigned char* whi = (const LAS unsigned char*)(size_t)whi_;
#define W1F(k, l) (*(const LAS v4u*)(((k) < 8 ? wlo + (k) * 7168 : whi + ((k) - 8) * 7168) + (l) * 1024))
    v4u wf[2][7];
#pragma unroll
    for (int l = 0; l < 7; ++l) wf[0][l] = W1F(0, l);
    unsigned mx = SUM16(tag2 - ck[0][0] - ck[0][1], hf[0]);
    float rd[2];
    v2u nmf[2];
    auto stats = [&](int t) {
      const float m0 = asf(str[t][0][0]), m1 = asf(str[t][1][0]);
      const float m = rows_sum(m0 + m1) * 0.125f;
      const float da = m0 - m, db = m1 - m;
      const float m2 = rows_sum(asf(str[t][0][1]) + asf(str[t][1][1]) + 64.0f * (da * da + db * db));
      const float nm = q == 0 ? -m : 0.f;
      nmf[t] = v2u{pk2(nm, nm), pk2(nm, 0.f)};
      rd[t] = ln_rstd(m2 * (1.0f / D_));
    };
#define P3OP(i) do { if (k < 4) { if ((i) < 2) HC((4 + k) & 7, (i) & 1); else HL((4 + k) & 7, ((i) - 2) & 3); }          \
                     else if (k == 4) { if ((i) < 4) LD16C(str[((i) >> 1) & 1][(i) & 1], vs, stb, (((i) >> 1) & 1) * 2048 + ((i) & 1) * 256); } \
                     else if ((i) < p3w.v[k]) W2L(W2A + 4 * (k - 5) + (i)); } while (0)
#pragma unroll
    for (int k = 0; k < 16; ++k) {
      const int m = k >> 1, mn = ((k + 1) >> 1) & 7;
      if ((k & 1) == 0 && __builtin_expect(BAD(mx), 0)) {
        unsigned spin = SPIN_CAP;
        WAITV(0);
        do {
          COUNT(2);
          unsigned c0_, c1_;
          asm volatile("global_load_dword %4, %8, %10 sc1\n\tglobal_load_dword %5, %8, %10 offset:2048 sc1\n\t"
                       "global_load_dwordx4 %0, %6, %9 sc1\n\tglobal_load_dwordx4 %1, %7, %9 sc1\n\t"
                       "global_load_dwordx4 %2, %6, %9 offset:1024 sc1\n\tglobal_load_dwordx4 %3, %7, %9 offset:1024 sc1\n\ts_waitcnt vmcnt(0)"
                       : "=&v"(hf[m][0]), "=&v"(hf[m][1]), "=&v"(hf[m][2]), "=&v"(hf[m][3]), "=&v"(c0_), "=&v"(c1_)
                       : "v"(vl16), "v"(vh1), "v"(vl4), "s"(hb + m * 2048), "s"(cb + m * 256) : "memory");
          if (!BAD(SUM16(tag2 - c0_ - c1_, hf[m]))) spin = 1;
        } while (--spin);
      }
      if (k == P3_ST) {
        asm volatile("s_waitcnt vmcnt(%4)" : "+v"(str[0][0]), "+v"(str[0][1]), "+v"(str[1][0]), "+v"(str[1][1]) : "i"(p3_stwait()) : "memory");
#define STCK(a) (tag + (a)[0] + (a)[1] - (a)[2])
        if (__builtin_expect(BAD(STCK(str[0][0]) | STCK(str[0][1]) | STCK(str[1][0]) | STCK(str[1][1])), 0)) {
          unsigned spin = SPIN_CAP;
          WAITV(0);
          do {
            asm volatile("global_load_dwordx4 %0, %4, %5 sc1\n\tglobal_load_dwordx4 %1, %4, %5 offset:256 sc1\n\t"
                         "global_load_dwordx4 %2, %4, %5 offset:2048 sc1\n\tglobal_load_dwordx4 %3, %4, %5 offset:2304 sc1\n\ts_waitcnt vmcnt(0)"
                         : "=&v"(str[0][0]), "=&v"(str[0][1]), "=&v"(str[1][0]), "=&v"(str[1][1]) : "v"(vs), "s"(stb) : "memory");
            if (!BAD(STCK(str[0][0]) | STCK(str[0][1]) | STCK(str[1][0]) | STCK(str[1][1]))) spin = 1;
          } while (--spin);
        }
      }
      SCHED();
      if (k == P3_ST || k == P3_ST + 2) {
        stats((k - P3_ST) >> 1);
#pragma unroll
        for (int l = 0; l < 7; ++l) {
          acc[l][0] = mfma(wf[k & 1][l], hf[m][0], acc[l][0]);
          wf[(k + 1) & 1][l] = W1F(k + 1, l);
          acc[l][1] = mfma(wf[k & 1][l], hf[m][1], acc[l][1]);
        }
#pragma unroll
        for (int i = 0; i < 14; ++i) {
          __builtin_amdgcn_sched_group_barrier(0x008, 1, 0);
          if (i & 1) __builtin_amdgcn_sched_group_barrier(0x100, 1, 0);
          __builtin_amdgcn_sched_group_barrier(0x002, 2, 0);
        }
        SCHED();
        P3OP(0); P3OP(1); P3OP(2); P3OP(3);
        SCHED();
      } else {
#pragma unroll
        for (int l = 0; l < 7; ++l) {
          acc[l][0] = mfma(wf[k & 1][l], hf[m][2 * (k & 1)], acc[l][0]);
          SCHED();
          if (k + 1 < 16) wf[(k + 1) & 1][l] = W1F(k + 1, l);
          if ((k & 1) && k + 1 < 16 && k >= 4) {
            if (l == 4) P3WAIT(mn, p3t.v[mn], "");
            if (l == 5) mx = sum4(mx, hf[mn][1]);
            if (l == 6) mx = sum4(mx, hf[mn][3]);
          }
          SCHED();
          acc[l][1] = mfma(wf[k & 1][l], hf[m][2 * (k & 1) + 1], acc[l][1]);
          SCHED();
          if (l < 6) P3OP(l);
          if ((k & 1) && k + 1 < 16) {
            if (k >= 4) {
              if (l == 4) mx = sum4(tag2 - ck[mn][0] - ck[mn][1], hf[mn][0]);
              if (l == 5) mx = sum4(mx, hf[mn][2]);
            } else if (l == 5) {
              P3WAIT(mn, p3t.v[mn], "");
              mx = SUM16(tag2 - ck[mn][0] - ck[mn][1], hf[mn]);
            }
          }
          SCHED();
        }
      }
    }
    asm volatile("s_memtime %0\n\ts_waitcnt lgkmcnt(0)" : "=s"(tb_) :: "memory");
    const int kbp = (7 * fs + (fs & 1)) >> 1, taus = 7 * fs + ((fs & 1) ? 0 : 6);
    const int t64 = 2 * t128 + (w >> 1);
    unsigned char* fb = ws + FF_OFF + t64 * 229376 + kbp * 4096 + 2 * (w & 1) * 1024;
    v4u fck = {tag, tag, tag, tag};
#pragma unroll
    for (int l = 0; l < 7; ++l) { acc[l][0] = mfma16(csf[l], nmf[0], acc[l][0]); acc[l][1] = mfma16(csf[l], nmf[1], acc[l][1]); }
    auto act = [&](int l, int t) {
      const f32x4 v = acc[l][t] * rd[t];
      return v2u{relu2(pk2(v[0], v[1])), relu2(pk2(v[2], v[3]))};
    };
#pragma unroll
    for (int m_ = 0; m_ < 3; ++m_)
#pragma unroll
      for (int t = 0; t < 2; ++t) {
        const v2u a = act(2 * m_, t), b = act(2 * m_ + 1, t);
        const v4u oo = {a[0], a[1], b[0], b[1]};
        if (t == 0) ST16C(oo, vl16, fb + m_ * 4096, 0); else ST16C(oo, vl16, fb + m_ * 4096, 1024);
        fck[m_] = sum4(fck[m_], oo);
      }
    const v2u a6 = act(6, 0), b6 = act(6, 1);
    const v4u o6 = {a6[0], a6[1], b6[0], b6[1]};
    ST16C(o6, vl16, ws + FF_OFF + t64 * 229376 + (taus >> 1) * 4096 + (2 * (taus & 1) + (w & 1)) * 1024, 0);
    fck[3] = sum4(fck[3], o6);
    ST16C(fck, vl16, ws + CKF_OFF + ((t64 * 16 + fs) * 2 + (w & 1)) * 1024, 0);
  }

#pragma unroll
  for (int n = 56 - W2C; n < 56; ++n) W2L(n);

  {
    constexpr Tab p4t = mk<p4_wait>(1, 14);
    f32x4 acc[4][4];
    constexpr int R = D4 + 1;
    v4u ff[R][4];
    v4u rec[4][2];
    cptr fb = ws + FF_OFF + ti * 229376 + w * 57344;
    cptr cb = ws + CKF_OFF + (ti * 16 + 4 * w) * 2048;
    unsigned vsh[4];
#pragma unroll
    for (int c = 0; c < 4; ++c) vsh[c] = vl16 + (c ^ (w >> 1)) * 1024;
#define VR4(g, c) ((g) % 7 == 3 ? vsh[c] : vrot[c])
#define RECB(g) ((g) == 0 ? 0 : (g) == 3 ? 1 : (g) == 7 ? 2 : 3)
#define RECL(b) { LD16C(rec[b][0], vl16, cb + ((b) >> 1) * 4096, ((b) & 1) * 2048); LD16C(rec[b][1], vl16, cb + ((b) >> 1) * 4096, ((b) & 1) * 2048 + 1024); }
#define RB0(g) (2 * ((g) / 7) + ((g) % 7 > 3 ? 1 : 0))
#define RC0(g) ((g) % 7 > 3 ? (g) % 7 - 4 : (g) % 7)
#define M4(RE, g) ((g) % 7 == 3 ? 2u * tag2 - RE[RB0(g)][0][3] - RE[RB0(g)][1][3] - RE[RB0(g) + 1][0][3] - RE[RB0(g) + 1][1][3]   \
                                : tag2 - RE[RB0(g)][0][RC0(g)] - RE[RB0(g)][1][RC0(g)])
#pragma unroll
    for (int k = 0; k < D4; ++k) {
      if (p4_rec(k)) RECL(RECB(k));
#pragma unroll
      for (int c = 0; c < 4; ++c) LD16C(ff[k][c], VR4(k, c), fb + k * 4096, 0);
    }
#pragma unroll
    for (int i = 0; i < 4; ++i) {
      acc[i][0] = unpk(hres[i >> 1][2 * (i & 1)], hres[i >> 1][2 * (i & 1) + 1]);
#pragma unroll
      for (int c = 1; c < 4; ++c) acc[i][c] = f32x4{0.f, 0.f, 0.f, 0.f};
    }
#define P4WAIT(g, N) do { asm volatile("s_waitcnt vmcnt(%4)" : "+v"(ff[(g) % R][0]), "+v"(ff[(g) % R][1]), "+v"(ff[(g) % R][2]), "+v"(ff[(g) % R][3]) : "i"(N) : "memory"); \
                          if (p4_rec(g)) asm volatile("" : "+v"(rec[RECB(g)][0]), "+v"(rec[RECB(g)][1])); } while (0)
    P4WAIT(0, 4 * D4 - 4 + 2);
    unsigned mx = SUM16(M4(rec, 0), ff[0]);

#define P4OP(i) do { if (k + D4 < 14) { if ((i) == 0 && p4_rec(k + D4)) RECL(RECB(k + D4));                              \
                                        LD16C(ff[(k + D4) % R][i], VR4(k + D4, i), fb + (k + D4) * 4096, 0); } } while (0)
#pragma unroll
    for (int k = 0; k < 14; ++k) {
      if (__builtin_expect(BAD(mx), 0)) {
        unsigned spin = SPIN_CAP;
        WAITV(0);
        do {
          COUNT(3);
          RECL(RB0(k));
          if (k % 7 == 3) RECL(RB0(k) + 1);
          #define VRK(c) VR4(k, c)
          LOAD4_WAIT(ff[k % R], fb + k * 4096, VRK);
          asm volatile("" : "+v"(rec[RB0(k)][0]), "+v"(rec[RB0(k)][1]));
          if (k % 7 == 3) asm volatile("" : "+v"(rec[(RB0(k) + 1) & 3][0]), "+v"(rec[(RB0(k) + 1) & 3][1]));
          if (!BAD(SUM16(M4(rec, k), ff[k % R]))) spin = 1;
        } while (--spin);
      }
      if (k % 7 == 3) {
        v4u* F = ff[k % R];
        const bool w0 = w & 1;
        const v4u e0 = {F[0][0], F[0][1], F[2][0], F[2][1]}, o0 = {F[0][2], F[0][3], F[2][2], F[2][3]};
        const v4u e1 = {F[1][0], F[1][1], F[3][0], F[3][1]}, o1 = {F[1][2], F[1][3], F[3][2], F[3][3]};
        F[0] = w0 ? o0 : e0; F[1] = w0 ? e0 : o0; F[2] = w0 ? o1 : e1; F[3] = w0 ? e1 : o1;
      }
      if (k < 11) PIN4A(w2f[k][0], w2f[k][1], w2f[k][2], w2f[k][3]);
      else PIN4(w2f[k][0], w2f[k][1], w2f[k][2], w2f[k][3]);
      if (k < 13) STEP64(w2f[k], ff[k % R], ff[(k + 1) % R], true, P4WAIT((k + 1) % 14, p4t.v[(k + 1) % 14]), M4(rec, (k + 1) % 14), P4OP(0), P4OP(1), P4OP(2), P4OP(3));
    }
    v4u o[2];
    last_step_reduce(smem + RED_OFF, w, lane, w2f[13], ff[13 % R], acc, o);
    unsigned char* ob_ = out + (size_t)(64 * ti + 16 * w) * 1024 + cg * 128;
    ST16(o[0], vx, ob_, 0);
    ST16(o[1], vx, ob_, 64);
  }
  { unsigned long long t0_, t1_; const unsigned dl_ = ((unsigned)(tb_ - ta_) * 205u) >> 10; asm volatile("s_memrealtime %0\n\ts_waitcnt lgkmcnt(0)" : "=s"(t0_) :: "memory"); do { asm volatile("s_memrealtime %0\n\ts_waitcnt lgkmcnt(0)" : "=s"(t1_) :: "memory"); } while ((unsigned)(t1_ - t0_) < dl_); }
}

extern "C" void kernel_launch(void* const* d_in, const int* in_sizes, int n_in, void* d_out, int out_size, void* d_ws,
                              size_t ws_size, hipStream_t stream) {
  (void)hipFuncSetAttribute((const void*)fnet_fused, hipFuncAttributeMaxDynamicSharedMemorySize, LDS_BYTES);
  fnet_fused<<<256, 256, LDS_BYTES, stream>>>((cptr)d_in[0], (cptr)d_in[1], (cptr)d_in[2], (cptr)d_in[3], (cptr)d_in[4],
                                              (cptr)d_in[6], (cptr)d_in[7], (unsigned char*)d_ws, (unsigned char*)d_out);
}
```

```cpp
#include <hip/hip_runtime.h>
#include <hip/hip_bf16.h>
#include <stdint.h>
#include <type_traits>

typedef unsigned v4u __attribute__((ext_vector_type(4)));
typedef unsigned v2u __attribute__((ext_vector_type(2)));
typedef float f32x4 __attribute__((ext_vector_type(4)));
typedef float f32x2 __attribute__((ext_vector_type(2)));
typedef short bf16x8 __attribute__((ext_vector_type(8)));
typedef short v4s __attribute__((ext_vector_type(4)));
typedef __bf16 bf2_t __attribute__((ext_vector_type(2)));
typedef const unsigned char* cptr;
#define LAS __attribute__((address_space(3)))

constexpr int S_ = 2048, D_ = 512, F_ = 1792;
constexpr unsigned EPOCH_OFF = 0;
constexpr unsigned FLAG0_OFF = 8192;
constexpr unsigned XNT_OFF = 131072;
constexpr unsigned WD_OFF  = XNT_OFF + (2u << 20);
constexpr unsigned W1_OFF  = WD_OFF + (1u << 20);
constexpr unsigned W2_OFF  = W1_OFF + 1835008u;
constexpr unsigned CS_OFF  = W2_OFF + 1835008u;
constexpr unsigned PQ_OFF  = CS_OFF + 131072;
constexpr unsigned H_OFF   = PQ_OFF + (4u << 20);
constexpr unsigned FF_OFF  = H_OFF + (2u << 20);
constexpr unsigned ST_OFF  = FF_OFF + 7340032u;
constexpr unsigned CKP_OFF = ST_OFF + 262144;
constexpr unsigned CKH_OFF = CKP_OFF + 262144;
constexpr unsigned CKF_OFF = CKH_OFF + 262144;
constexpr unsigned CKX_OFF = CKF_OFF + (1u << 20);

constexpr int XS_ROW = 1088;
constexpr int XS_OFF = 131072, WST_OFF = XS_OFF + 8 * XS_ROW;
constexpr int RED_OFF = 114688;
constexpr int LDS_BYTES = 163840;
constexpr unsigned SPIN_CAP = 1u << 20;

__device__ __forceinline__ unsigned pk2(float a, float b) {
  f32x2 v = {a, b};
  return __builtin_bit_cast(unsigned, __builtin_convertvector(v, bf2_t));
}
__device__ __forceinline__ float asf(unsigned u) { return __builtin_bit_cast(float, u); }
__device__ __forceinline__ float bflo(unsigned u) { return __builtin_bit_cast(float, u << 16); }
__device__ __forceinline__ float bfhi(unsigned u) { return __builtin_bit_cast(float, u & 0xffff0000u); }
__device__ __forceinline__ float rbf(float x) { return (float)(__bf16)x; }
typedef short v2s __attribute__((ext_vector_type(2)));
__device__ __forceinline__ unsigned relu2(unsigned u) {
  return __builtin_bit_cast(unsigned, __builtin_elementwise_max(__builtin_bit_cast(v2s, u), v2s{0, 0}));
}
__device__ __forceinline__ f32x4 unpk(unsigned a, unsigned b) { return f32x4{bflo(a), bfhi(a), bflo(b), bfhi(b)}; }
__device__ __forceinline__ float ln_rstd(float var) {
  return __builtin_amdgcn_rcpf(rbf(__builtin_amdgcn_sqrtf(rbf(rbf(var) + 1e-5f))));
}
__device__ __forceinline__ f32x4 mfma(v4u a, v4u b, f32x4 c) {
  return __builtin_amdgcn_mfma_f32_16x16x32_bf16(__builtin_bit_cast(bf16x8, a), __builtin_bit_cast(bf16x8, b), c, 0, 0, 0);
}
template <int CTRL> __device__ __forceinline__ float dpp_add(float x) {
  return x + __builtin_bit_cast(float, __builtin_amdgcn_update_dpp(0, __builtin_bit_cast(int, x), CTRL, 0xf, 0xf, true));
}
__device__ __forceinline__ float row16_sum(float x) {
  return dpp_add<0x121>(dpp_add<0x122>(dpp_add<0x124>(dpp_add<0x128>(x))));
}
__device__ __forceinline__ float swap16_sum(float x) {
  unsigned a = __builtin_bit_cast(unsigned, x), b = a;
  asm("" : "+v"(b));
  auto r = __builtin_amdgcn_permlane16_swap(a, b, false, false);
  return asf(r[0]) + asf(r[1]);
}
__device__ __forceinline__ float swap32_sum(float x) {
  unsigned a = __builtin_bit_cast(unsigned, x), b = a;
  asm("" : "+v"(b));
  auto r = __builtin_amdgcn_permlane32_swap(a, b, false, false);
  return asf(r[0]) + asf(r[1]);
}
__device__ __forceinline__ float rows_sum(float x) { return swap32_sum(swap16_sum(x)); }
__device__ __forceinline__ float half_sum(float x) { return swap16_sum(row16_sum(x)); }
#define SCHED() __builtin_amdgcn_sched_barrier(0)
#define WAITV(n) asm volatile("s_waitcnt vmcnt(%0)" ::"i"(n) : "memory")
#define WAITV_BAR(n) asm volatile("s_waitcnt vmcnt(%0) lgkmcnt(0)\n\ts_barrier" ::"i"(n) : "memory")
#define BAR() asm volatile("s_waitcnt lgkmcnt(0)\n\ts_barrier" ::: "memory")
#define ACC4(m, m0, v) do { m = sum4(m0, v); asm volatile("" : "+v"(m)); } while (0)
#define PIN4(a, b, c, d) asm volatile("" : "+v"(a), "+v"(b), "+v"(c), "+v"(d))
#define PIN4A(a, b, c, d) asm volatile("" : "+a"(a), "+a"(b), "+a"(c), "+a"(d))

#define LD16(d, vo, sb, imm) asm volatile("global_load_dwordx4 %0, %1, %2 offset:%3" : "=v"(d) : "v"(vo), "s"(sb), "i"(imm) : "memory")
#define LD16C(d, vo, sb, imm) asm volatile("global_load_dwordx4 %0, %1, %2 offset:%3 sc1" : "=v"(d) : "v"(vo), "s"(sb), "i"(imm) : "memory")
#define LD16CA(d, vo, sb, imm) asm volatile("global_load_dwordx4 %0, %1, %2 offset:%3 sc1" : "=a"(d) : "v"(vo), "s"(sb), "i"(imm) : "memory")
#define LD4C(d, vo, sb, imm) asm volatile("global_load_dword %0, %1, %2 offset:%3 sc1" : "=v"(d) : "v"(vo), "s"(sb), "i"(imm) : "memory")
#define ST16C(v, vo, sb, imm) asm volatile("global_store_dwordx4 %0, %1, %2 offset:%3 sc1\n\ts_nop 1" ::"v"(vo), "v"(v), "s"(sb), "i"(imm) : "memory")
#define ST16(v, vo, sb, imm) asm volatile("global_store_dwordx4 %0, %1, %2 offset:%3\n\ts_nop 1" ::"v"(vo), "v"(v), "s"(sb), "i"(imm) : "memory")
#define ST4C(v, vo, sb, imm) asm volatile("global_store_dword %0, %1, %2 offset:%3 sc1" ::"v"(vo), "v"(v), "s"(sb), "i"(imm) : "memory")
#define RLD16(d, vo, sb, imm) asm volatile("global_load_dwordx4 %0, %1, %2 offset:%3 sc1" ::"v"(d), "v"(vo), "s"(sb), "i"(imm) : "memory")
#define RLD4(d, vo, sb, imm) asm volatile("global_load_dword %0, %1, %2 offset:%3 sc1" ::"v"(d), "v"(vo), "s"(sb), "i"(imm) : "memory")
#define VSUB(a, x) asm volatile("v_sub_u32 %0, %0, %1" : "+v"(a) : "v"(x))
#define VADD4(a, f) asm volatile("v_add3_u32 %0, %0, %1, %2\n\tv_add3_u32 %0, %0, %3, %4" : "+v"(a) : "v"((f)[0]), "v"((f)[1]), "v"((f)[2]), "v"((f)[3]))
#define VADD16(a, F) do { VADD4(a, F[0]); VADD4(a, F[1]); VADD4(a, F[2]); VADD4(a, F[3]); } while (0)
#define WRONG 0
#define TOUCH(vo, sb) asm volatile("global_load_dword %0, %1, %2" : "+v"(sink) : "v"(vo), "s"(sb) : "memory")
#define COUNT(i)
__device__ __forceinline__ void st_flag(unsigned char* p, unsigned tag) {
  const unsigned z = 0;
  asm volatile("s_mov_b64 exec, 1\n\tglobal_store_dword %0, %1, %2 sc1\n\ts_mov_b64 exec, -1" ::"v"(z), "v"(tag), "s"(p) : "memory");
}
#define GLDS(vo, sb, lds, imm) asm volatile("s_mov_b32 m0, %2\n\ts_nop 0\n\tglobal_load_lds_dwordx4 %0, %1 offset:%3" ::"v"(vo), "s"(sb), "s"(lds), "i"(imm) : "memory")
#define GLDSC(vo, sb, lds, imm) asm volatile("s_mov_b32 m0, %2\n\ts_nop 0\n\tglobal_load_lds_dwordx4 %0, %1 offset:%3 sc1" ::"v"(vo), "s"(sb), "s"(lds), "i"(imm) : "memory")

__device__ __forceinline__ unsigned sum4(unsigned m, v4u v) { return (m + v[0] + v[1]) + v[2] + v[3]; }
__device__ __forceinline__ void poll_flags(cptr base, unsigned tag, int lane) {
  const unsigned vo = lane * 128;
  for (unsigned spin = 0; spin < SPIN_CAP; ++spin) {
    v4u v;
    asm volatile("global_load_dwordx4 %0, %1, %2 sc1\n\ts_waitcnt vmcnt(0)" : "=&v"(v) : "v"(vo), "s"(base) : "memory");
    if (__all(v[0] == tag && v[1] == tag && v[2] == tag && v[3] == tag)) break;
  }
}
template <class H>
__device__ __forceinline__ void last_step_reduce(unsigned char* red, int w, int lane, const v4u (&wt)[4], const v4u (&fc)[4],
                                                 f32x4 (&acc)[4][4], v4u (&o)[2], H&& hook) {
#pragma unroll
  for (int c = 1; c <= 4; ++c) {
#pragma unroll
    for (int i = 0; i < 4; ++i) {
      acc[i][c & 3] = mfma(wt[i], fc[c & 3], acc[i][c & 3]);
      SCHED();
      if (c >= 2) *(f32x4*)(red + (((c - 1) ^ w) * 3 + c - 2) * 4096 + lane * 16 + i * 1024) = acc[i][c - 1];
      SCHED();
    }
  }
  hook();
  BAR();
  const unsigned char* s = red + w * 12288 + lane * 16;
  f32x4 pt[3][4];
#pragma unroll
  for (int i = 0; i < 4; ++i)
#pragma unroll
    for (int c = 0; c < 3; ++c) pt[c][i] = *(const f32x4*)(s + c * 4096 + i * 1024);
  SCHED();
#pragma unroll
  for (int i = 0; i < 4; ++i) {
    const f32x4 y = (acc[i][0] + pt[0][i]) + (pt[1][i] + pt[2][i]);
    o[i >> 1][2 * (i & 1)] = pk2(y[0], y[1]);
    o[i >> 1][2 * (i & 1) + 1] = pk2(y[2], y[3]);
  }
}

#define NP0 8
#define FL0_CHK 15
#define W1A 0
#define W1C 8
#define GATE 7
#define W2A 4
#define W2C 12
#define WDS 12
#define XT0 19
#define DB 5
#define BP 8
#define RAN 2
constexpr int D2 = 4, D4 = 5, FL0_PUB = FL0_CHK - 8, FL0_IT = FL0_CHK - 3, NV = FL0_CHK + DB, NX = 42 - WDS;

template <int I, int N, class F> __device__ __forceinline__ void static_for(F&& f) {
  if constexpr (I < N) { f(std::integral_constant<int, I>{}); static_for<I + 1, N>(f); }
}
constexpr bool p1_bar(int s) { return (s + 1) % BP == 0; }
constexpr bool p1_ra(int s) { return s >= BP - 1 && s < 15 + BP && (s + 1) % BP < BP / RAN; }
constexpr int p1_as(int s) { return 16 + ((s + 1) / BP - 1) * BP + ((s + 1) % BP) * RAN; }
constexpr int p1_xbase(int s) { int n = 0; for (int u = XT0; u < s; ++u) n += p1_ra(u) ? 0 : (u + DB < 32 ? 2 : 6); return n > NX ? NX : n; }
constexpr int p1_extra(int s) { return s < XT0 ? 0 : p1_xbase(s + 1) - p1_xbase(s); }
static_assert(p1_xbase(32) == NX && XT0 > FL0_CHK && p1_bar(FL0_CHK) && p1_bar(FL0_PUB) && DB <= 7 && RAN <= 2, "");
constexpr int p1_wait(int it, bool mid) {
  int posA[48] = {}, posB[40] = {}, posF = -1, n = 0, pos0 = 6 * DB;
  for (int s = 0; s < 48; ++s) posA[s] = -1;
  for (int s = 0; s < DB; ++s) { n += 6; posB[s] = n - 1; }
  if (it < 0) return n - 1 - posB[0];
  for (int s = 0; s <= it; ++s) {
    if (s == it && !mid) break;
    if (s == FL0_IT) posF = n - 1;
    if (p1_ra(s)) for (int u = 0; u < RAN; ++u) { n += 2; posA[p1_as(s) + u] = n - 1; }
    if (s == it) { n += p1_extra(s) > 2 ? 2 : p1_extra(s); break; }
    n += p1_extra(s);
    if (s + DB < 32) { n += (s + DB < NV ? 6 : 4); posB[s + DB] = n - 1; }
  }
  int req = -1;
  if (mid) req = posB[it + 1];
  else {
    if (it + BP < 32) req = posA[it + BP];
    if (it == FL0_CHK && posF > req) req = posF;
    if (it == FL0_PUB && pos0 > req) req = pos0;
  }
  return n - 1 - req > 63 ? 63 : n - 1 - req;
}
constexpr int p2_w1(int s) { int n = 28 - W1A - 4 * (s - (8 - D2)); return (s + D2 < 8 || s == 7) ? 0 : n > 4 ? 4 : n < 0 ? 0 : n; }
constexpr int p2_wait(int k) {
  int n = 0;
  for (int g = k + 1; g < 8 && g < k - 1 + D2; ++g) n += 5;
  for (int s = 0; s < k - 1; ++s) n += p2_w1(s);
  return n + ((k - 1 + D2 < 8) ? 3 : (p2_w1(k - 1) > 2 ? 2 : p2_w1(k - 1)));
}
constexpr int W2B = 56 - W2A - W2C;
constexpr int p3_w2(int s) { int n = W2B - 4 * (s - 5); return s < 5 ? 0 : n > 4 ? 4 : n < 0 ? 0 : n; }
constexpr int p3_ops(int s) { return s < 4 ? 6 : s == 4 ? 4 : p3_w2(s); }
constexpr int p3_wait(int m) {
  int n = m < 4 ? 6 * (3 - m) : 0;
  for (int s = (m < 4 ? 0 : m - 3); s < 2 * m; ++s) n += p3_ops(s);
  return n;
}
constexpr int P3_ST = 10;
constexpr int p3_stwait() { int n = 0; for (int s = 5; s < P3_ST; ++s) n += p3_ops(s); return n; }
constexpr int p4_rec(int g) { return (g == 0 || g == 3 || g == 7 || g == 10) ? 2 : 0; }
constexpr int p4_wait(int k) {
  int n = 0;
  for (int g = k + 1; g < 14 && g < k - 1 + D4; ++g) n += 4 + p4_rec(g);
  return n + ((k - 1 + D4 < 14) ? 2 + p4_rec(k - 1 + D4) : 0);
}
static_assert(W2B >= 0 && W2B <= 44 && W1A >= 0 && W1A <= 28 && W1A % 4 == 0, "");

__global__ __launch_bounds__(256, 1) void fnet_fused(cptr x, cptr wsre, cptr wsim, cptr wre, cptr wim, cptr w1, cptr w2,
                                                     unsigned char* ws, unsigned char* out) {
  extern __shared__ __attribute__((aligned(16))) unsigned char smem[];
  const int bid = blockIdx.x, p = bid & 7, j = bid >> 3;
  const int ti = 4 * p + (j >> 3), cg = j & 7, t128 = 2 * p + (j >> 4), fs = j & 15;
  const int tid = threadIdx.x, lane = tid & 63, w = __builtin_amdgcn_readfirstlane(tid >> 6), q = lane >> 4, r = lane & 15;
  const unsigned lds0 = (unsigned)(size_t)(LAS unsigned char*)smem;
  const unsigned vl16 = lane * 16, vl4 = lane * 4;

  const int mat = (j >> 2) & 1, nt = j & 3;
  unsigned epoch, sink = 0;
  v4u xr[2];
  {
    const unsigned vo = (lane >> 5) * 1024 + (lane & 31) * 16, zero = 0;
    cptr xb = x + (size_t)(8 * bid + 2 * w) * 1024;
    LD16(xr[0], vo, xb, 0);
    LD16(xr[1], vo, xb, 512);
    LD4C(epoch, zero, ws + EPOCH_OFF, 0);
  }
  const unsigned va0 = (lane >> 3) * 4096 + (((lane & 7) ^ (lane >> 4)) << 4), va1 = (va0 ^ 64) + 32768 - 1024;
  const int rot = nt & 1;
  cptr abase = (mat ? wsim : wsre) + (size_t)(64 * ti + 16 * w) * 4096;
  cptr abase0 = abase + rot * 2048, abase1 = abase - rot * 2048;
  const unsigned lda = lds0 + w * 2048;
#define A_STAGE0(s) GLDS(va0, ((s) < 16 ? abase0 : abase1) + (s) * 128, lda + ((s) & 15) * 8192, 0)
#define A_STAGE1(s) GLDS(va1, ((s) < 16 ? abase0 : abase1) + (s) * 128, lda + ((s) & 15) * 8192, 1024)
  const int kb1 = bid >> 4, fs1 = bid & 15, ob = bid < 224 ? bid : 223, kbd = bid & 31;
  {
    cptr pc;
    unsigned len;
    asm volatile("s_getpc_b64 %0\n.Lfnet_pc:\n\ts_mov_b32 %1, .Lfnet_last-.Lfnet_pc" : "=s"(pc), "=s"(len));
    const unsigned vo = min(((4 * j + w) * 4 + (lane & 3)) * 128u, len);
    TOUCH(vo, pc);
  }
  const int wst = WST_OFF + w * 5952;
  v4u wdp;
  auto wload = [&]() {
    if (w < 2) {
      const unsigned vo = (lane >> 5) * 1024 + (lane & 31) * 16;
      cptr sb = w2 + (size_t)(8 * ob) * 1024 + w * 512;
#pragma unroll
      for (int k = 0; k < 4; ++k) GLDS(vo, sb + k * 2048, lds0 + wst + k * 1088, 0);
    } else {
      const unsigned vo = (lane >> 3) * (F_ * 2) + (lane & 7) * 16;
      cptr sb = w1 + (size_t)(32 * kb1) * (F_ * 2) + ((7 * fs1) >> 1) * 64 + (w - 2) * 128;
#pragma unroll
      for (int k = 0; k < 4; ++k) GLDS(vo, sb + k * (8 * F_ * 2), lds0 + wst + k * 1088, 0);
    }
    const unsigned vo = (8 * (r >> 2) + (r & 3)) * 1024 + q * 16;
    LD16(wdp, vo, (kbd < 16 ? wre : wim) + (size_t)(64 * (bid >> 5) + 32 * (w >> 1) + 4 * (w & 1)) * 1024 + (kbd & 15) * 64, 0);
  };
  auto wstore = [&]() {
    if (kbd >= 16) wdp ^= 0x80008000u;
    ST16C(wdp, vl16, ws + WD_OFF + bid * 4096 + w * 1024, 0);
    if (w < 2) {
      if (bid >= 224) return;
      const int rr = (lane >> 2) & 3;
      const unsigned char* s = smem + wst + (rr >> 1) * 1088 + (rr & 1) * 512 + 64 * (q >> 1) + 16 * (lane & 3) + 8 * (q & 1);
      const unsigned vo = q * 1024 + r * 16;
      unsigned char* sb = ws + W2_OFF + (ob >> 2) * 4096 + (ob & 3) * 256 + w * (4 * 229376);
#pragma unroll
      for (int i = 0; i < 4; ++i) {
        const v2u lo = __builtin_bit_cast(v2u, __builtin_amdgcn_ds_read_tr16_b64_v4i16((LAS v4s*)(s + 128 * i)));
        const v2u hi = __builtin_bit_cast(v2u, __builtin_amdgcn_ds_read_tr16_b64_v4i16((LAS v4s*)(s + 128 * i + 2176)));
        const v4u o = {lo[0], lo[1], hi[0], hi[1]};
        ST16C(o, vo, sb + i * 229376, 0);
      }
    } else {
      const unsigned char* s = smem + wst + q * 1088 + ((lane >> 2) & 3) * 128 + 16 * (lane & 3);
      float csv = 0.f;
      auto pos = [&](int tl) { return (fs1 & 1) ? (tl == 0 ? 7 : tl == 1 ? 6 : tl - 2) : tl; };
#pragma unroll
      for (int i = 0; i < 4; ++i) {
        const int l = pos(4 * (w - 2) + i);
        const unsigned char* sl = s + (i >> 1) * 64 + (i & 1) * 8;
        const v2u lo = __builtin_bit_cast(v2u, __builtin_amdgcn_ds_read_tr16_b64_v4i16((LAS v4s*)sl));
        const v2u hi = __builtin_bit_cast(v2u, __builtin_amdgcn_ds_read_tr16_b64_v4i16((LAS v4s*)(sl + 512)));
        const v4u o = {lo[0], lo[1], hi[0], hi[1]};
        if (l < 7) ST16C(o, vl16, ws + W1_OFF + fs1 * 114688 + kb1 * 7168 + l * 1024, 0);
        const float cs = rows_sum(((bflo(o[0]) + bfhi(o[0])) + (bflo(o[1]) + bfhi(o[1]))) + ((bflo(o[2]) + bfhi(o[2])) + (bflo(o[3]) + bfhi(o[3]))));
        csv = (q == i) ? cs : csv;
      }
      const unsigned vc = r * 16 + q * 4, chl = pk2(csv, csv - rbf(csv));
      ST4C(chl, vc, ws + CS_OFF + fs1 * 8192 + (w - 2) * 4096 + kb1 * 256, 0);
    }
  };
#pragma unroll 1
  for (int s = 0; s < NP0; ++s) { A_STAGE0(s); A_STAGE1(s); }
  WAITV(2 * NP0 + 1);
  asm volatile("" : "+v"(xr[0]), "+v"(xr[1]), "+v"(epoch));
  const unsigned tag = __builtin_amdgcn_readfirstlane(epoch) + 1u;
  {
    float v[16];
#pragma unroll
    for (int i = 0; i < 2; ++i)
#pragma unroll
      for (int k = 0; k < 4; ++k) { v[8 * i + 2 * k] = bflo(xr[i][k]); v[8 * i + 2 * k + 1] = bfhi(xr[i][k]); }
    float s1[4] = {0.f, 0.f, 0.f, 0.f};
#pragma unroll
    for (int k = 0; k < 16; ++k) s1[k & 3] += v[k];
    const float mean = half_sum((s1[0] + s1[1]) + (s1[2] + s1[3])) * (1.0f / D_);
    float s2[4] = {0.f, 0.f, 0.f, 0.f};
#pragma unroll
    for (int k = 0; k < 16; ++k) { const float c = v[k] - mean; s2[k & 3] += c * c; }
    const float mb = rbf(mean), rd = ln_rstd(half_sum((s2[0] + s2[1]) + (s2[2] + s2[3])) * (1.0f / D_));
#pragma unroll
    for (int i = 0; i < 2; ++i) {
      v4u o;
#pragma unroll
      for (int k = 0; k < 4; ++k) {
        const unsigned c2 = pk2(v[8 * i + 2 * k] - mb, v[8 * i + 2 * k + 1] - mb);
        o[k] = pk2(bflo(c2) * rd, bfhi(c2) * rd);
      }
      *(v4u*)(smem + XS_OFF + (2 * w + (lane >> 5)) * XS_ROW + (lane & 31) * 16 + i * 512) = o;
    }
  }
  BAR();
  auto tr8 = [&](int off, int i) {
    const unsigned char* s = smem + off + ((lane >> 2) & 3) * XS_ROW + 64 * (q >> 1) + 16 * (lane & 3) + 8 * (q & 1) + 128 * i;
    const v2u lo = __builtin_bit_cast(v2u, __builtin_amdgcn_ds_read_tr16_b64_v4i16((LAS v4s*)s));
    const v2u hi = __builtin_bit_cast(v2u, __builtin_amdgcn_ds_read_tr16_b64_v4i16((LAS v4s*)(s + 4 * XS_ROW)));
    return v4u{lo[0], lo[1], hi[0], hi[1]};
  };
  {
    const v4u o0 = tr8(XS_OFF, 2 * w), o1 = tr8(XS_OFF, 2 * w + 1);
    const unsigned vo = (q >> 1) * 131072 + (q & 1) * 1024 + r * 16;
    unsigned char* sb = ws + XNT_OFF + (bid >> 2) * 2048 + (bid & 3) * 256 + w * 524288;
    ST16C(o0, vo, sb, 0);
    ST16C(o1, vo, sb + 262144, 0);
    auto sw = __builtin_amdgcn_permlane16_swap(sum4(0u, o0), sum4(0u, o1), false, false);
    const unsigned ck = tag + sw[0] + sw[1];
    const unsigned vc = ((q >> 1) + 2 * (q & 1)) * 64 + r * 4;
    ST4C(ck, vc, ws + CKX_OFF + bid * 1024 + w * 256, 0);
  }
  wload();
#pragma unroll 1
  for (int s = NP0; s < 16; ++s) { A_STAGE0(s); A_STAGE1(s); }

  cptr bbase0 = ws + XNT_OFF + (4 * nt + w) * 131072 + rot * 65536, bbase1 = bbase0 - rot * 131072;
  cptr xcb0 = ws + CKX_OFF + (4 * nt + w) * 64 + rot * 131072, xcb1 = xcb0 - rot * 262144;
#define BB(s) ((s) < 16 ? bbase0 : bbase1)
#define XCB(s) ((s) < 16 ? xcb0 : xcb1)
  const unsigned vxc = q * 1024 + r * 4, tag2 = 2u * tag;
  v4u bf[8][4];
  unsigned bc[8][2];
#define B_LOAD(s, i) LD16C(bf[(s) & 7][i], vl16, BB(s) + (s) * 4096 + ((s) < NV ? WRONG : 0), (i) * 1024)
#define B_CHK(s) do { LD4C(bc[(s) & 7][0], vxc, XCB(s) + (s) * 8192 + 4096, -4096); LD4C(bc[(s) & 7][1], vxc, XCB(s) + (s) * 8192 + 4096, 0); } while (0)
#pragma unroll
  for (int s = 0; s < DB; ++s) { B_CHK(s); B_LOAD(s, 0); B_LOAD(s, 1); B_LOAD(s, 2); B_LOAD(s, 3); }
  asm volatile("s_waitcnt vmcnt(%2)" : "+v"(wdp), "+v"(sink) : "i"(6 * DB) : "memory");
  wstore();
  f32x4 acc1[2][4];
#pragma unroll
  for (int i = 0; i < 2; ++i)
#pragma unroll
    for (int t = 0; t < 4; ++t) acc1[i][t] = f32x4{0.f, 0.f, 0.f, 0.f};
  const unsigned ra0 = r * 128 + ((q ^ (r >> 1)) << 4), ra1 = ra0 ^ 64;
  v4u af[2][8];
  v4u xres[2], csr[8], wd[8][4];
  v4u flw = {tag, tag, tag, tag};
  const unsigned vl128 = lane * 128;
  cptr xb2 = x + (size_t)(64 * ti + 16 * w) * 1024 + cg * 128;
  const unsigned vx = r * 1024 + q * 16;
  cptr cb2 = ws + CS_OFF + fs * 8192;
  const unsigned vc2 = q * 1024 + r * 16;
  cptr wb = ws + WD_OFF + cg * 131072 + w * 32768;
#define BAD(M) (__builtin_amdgcn_ballot_w64((M) != 0u) != 0)
#define SUM16(M0, F) sum4(sum4(sum4(sum4(M0, F[0]), F[1]), F[2]), F[3])
#define B_WAIT(s, N, TAIL) asm volatile("s_waitcnt vmcnt(%6)" TAIL : "+v"(bf[(s) & 7][0]), "+v"(bf[(s) & 7][1]), "+v"(bf[(s) & 7][2]), "+v"(bf[(s) & 7][3]), "+v"(bc[(s) & 7][0]), "+v"(bc[(s) & 7][1]) : "i"(N) : "memory")
  B_WAIT(0, p1_wait(-1, false), " lgkmcnt(0)\n\ts_barrier");
#pragma unroll
  for (int t = 0; t < 4; ++t) { af[0][t] = *(const v4u*)(smem + ra0 + t * 2048); af[0][4 + t] = *(const v4u*)(smem + ra1 + t * 2048); }
  unsigned bx = SUM16(tag2 - bc[0][0] - bc[0][1], bf[0]);
#define P1_EXTRA(e)                                                                                                     \
  if constexpr ((e) < p1_extra(it)) {                                                                                   \
    constexpr int n_ = p1_xbase(it) + (e);                                                                              \
    if constexpr (n_ < 2) LD16(xres[n_], vx, xb2, n_ * 64);                                                             \
    else if constexpr (n_ < 10) LD16C(csr[n_ - 2], vc2, cb2 + ((n_ - 2) >> 2) * 4096, ((n_ - 2) & 3) * 256);            \
    else LD16CA(wd[(n_ - 10) >> 2][(n_ - 10) & 3], vl16, wb + ((n_ - 10) >> 2) * 4096, ((n_ - 10) & 3) * 1024);         \
  }
#define MM1(i, t, kb) acc1[i][t] = mfma(bf[U][2 * (kb) + (i)], af[C][4 * (kb) + (t)], acc1[i][t]); SCHED()
#define RD1(n) if constexpr (RN) { af[Nx][n] = *(const v4u*)(sn + ((n) < 4 ? ra0 : ra1) + ((n) & 3) * 2048); SCHED(); }
#define CK1(i) if constexpr (it + 1 < NV) { ACC4(bx, (i) == 0 ? tag2 - bc[Un][0] - bc[Un][1] : bx, bf[Un][i]); SCHED(); }
  static_for<0, 32>([&](auto IT) {
    constexpr int it = IT, U = it & 7, Un = (it + 1) & 7, C = it & 1, Nx = C ^ 1;
    constexpr bool RA = p1_ra(it), RB = it + DB < 32, RN = it + 1 < 32;
    if constexpr (it < NV) {
      if (__builtin_expect(BAD(bx), 0)) {
        constexpr int hi = it + DB < NV ? it + DB : NV;
        unsigned spin = SPIN_CAP;
        WAITV(0);
        do {
          COUNT(0);
#pragma unroll
          for (int g = it; g < hi; ++g) {
            RLD4(bc[g & 7][0], vxc, XCB(g) + g * 8192 + 4096, -4096); RLD4(bc[g & 7][1], vxc, XCB(g) + g * 8192 + 4096, 0);
#pragma unroll
            for (int c = 0; c < 4; ++c) RLD16(bf[g & 7][c], vl16, BB(g) + g * 4096, c * 1024);
          }
          WAITV(0);
          unsigned bad = 0;
#pragma unroll
          for (int g = it; g < hi; ++g) { unsigned a = tag2; VSUB(a, bc[g & 7][0]); VSUB(a, bc[g & 7][1]); VADD16(a, bf[g & 7]); bad |= a; }
          if (!BAD(bad)) spin = 1;
        } while (--spin);
      }
    }
    if constexpr (it == FL0_CHK) {
      asm volatile("s_waitcnt vmcnt(%1)" : "+v"(flw) : "i"(p1_wait(it, false)) : "memory");
      if (!__all(flw[0] == tag && flw[1] == tag && flw[2] == tag && flw[3] == tag)) { COUNT(1); poll_flags(ws + FLAG0_OFF, tag, lane); }
    }
    if constexpr (p1_bar(it)) WAITV_BAR(p1_wait(it, false));
    if constexpr (it == FL0_PUB) { if (w == 0) st_flag(ws + FLAG0_OFF + (bid & 63) * 128 + (bid >> 6) * 4, tag); }
    if constexpr (it == FL0_IT)
      asm volatile("s_cmp_lg_u32 %3, 0\n\ts_cbranch_scc1 1f\n\tglobal_load_dwordx4 %0, %1, %2 sc1\n1:"
                   : "+v"(flw) : "v"(vl128), "s"(ws + FLAG0_OFF), "s"(w) : "memory", "scc");
    const unsigned char* sn = smem + ((it + 1) & 15) * 8192;
    SCHED();
    MM1(0, 0, 0); RD1(0);
    MM1(1, 0, 0); if constexpr (RA) A_STAGE0(p1_as(it)); P1_EXTRA(0) SCHED();
    MM1(0, 1, 0); RD1(1); if constexpr (RA && RAN == 2) { A_STAGE0(p1_as(it) + 1); SCHED(); }
    MM1(1, 1, 0); RD1(2); if constexpr (RA && RAN == 2) { A_STAGE1(p1_as(it)); SCHED(); }
    MM1(0, 2, 0); if constexpr (RA) A_STAGE1(p1_as(it) + RAN - 1); P1_EXTRA(1) SCHED();
    MM1(1, 2, 0); RD1(3); if constexpr (RN) { B_WAIT(it + 1, p1_wait(it, true), ""); SCHED(); }
    MM1(0, 3, 0); if constexpr (RB && it + DB < NV) B_CHK(it + DB); if constexpr (RB) B_LOAD(it + DB, 0); P1_EXTRA(2) SCHED();
    MM1(1, 3, 0); RD1(4); CK1(0);
    MM1(0, 0, 1); RD1(5); CK1(1);
    MM1(1, 0, 1); if constexpr (RB) B_LOAD(it + DB, 1); P1_EXTRA(3) SCHED();
    MM1(0, 1, 1); RD1(6); CK1(2);
    MM1(1, 1, 1); if constexpr (RB) B_LOAD(it + DB, 2); P1_EXTRA(4) SCHED();
    MM1(0, 2, 1); RD1(7); CK1(3);
    MM1(1, 2, 1); if constexpr (RB) B_LOAD(it + DB, 3); P1_EXTRA(5) SCHED();
    MM1(0, 3, 1);
    MM1(1, 3, 1);
  });
  {
    const int kbq = ti * 32 + 16 * mat + 4 * nt + w;
    unsigned char* pq = ws + PQ_OFF + kbq * 4096;
    unsigned ck = tag;
#pragma unroll
    for (int t = 0; t < 4; ++t) {
      const v4u o = {pk2(acc1[0][t][0], acc1[0][t][1]), pk2(acc1[0][t][2], acc1[0][t][3]),
                     pk2(acc1[1][t][0], acc1[1][t][1]), pk2(acc1[1][t][2], acc1[1][t][3])};
      ST16C(o, vl16, pq + t * 1024, 0);
      ck = sum4(ck, o);
    }
    ST4C(ck, vl4, ws + CKP_OFF + kbq * 256, 0);
    if (bid == 0 && w == 0) st_flag(ws + EPOCH_OFF, tag);
  }
#pragma unroll
  for (int n = 32 - WDS; n < 32; ++n) LD16CA(wd[n >> 2][n & 3], vl16, wb + (n >> 2) * 4096, (n & 3) * 1024);
  cptr w1src = ws + W1_OFF + fs * 114688 + w * 28672;
  const unsigned w1dst = lds0 + w * 28672;
#define W1L(n) GLDSC(vl16, w1src + ((n) >> 2) * 4096, w1dst + ((n) >> 2) * 4096, ((n) & 3) * 1024)
#pragma unroll 1
  for (int n = 0; n < W1A / 4; ++n) {
    GLDSC(vl16, w1src + n * 4096, w1dst + n * 4096, 0);
    GLDSC(vl16, w1src + n * 4096, w1dst + n * 4096, 1024);
    GLDSC(vl16, w1src + n * 4096, w1dst + n * 4096, 2048);
    GLDSC(vl16, w1src + n * 4096, w1dst + n * 4096, 3072);
  }
  v4u csf[7];
  {
    static_assert(p1_xbase(27) >= 10, "the wait for the last XNT stage covers the column-sum loads");
    PIN4(csr[0], csr[1], csr[2], csr[3]);
    PIN4(csr[4], csr[5], csr[6], csr[7]);
    const bool odd = fs & 1;
#pragma unroll
    for (int l = 0; l < 7; ++l) {
      const int te = l, to = l < 6 ? l + 2 : 1;
#pragma unroll
      for (int i = 0; i < 4; ++i) csf[l][i] = odd ? csr[(to >> 2) * 4 + i][to & 3] : csr[(te >> 2) * 4 + i][te & 3];
      asm volatile("" : "+a"(csf[l]));
    }
  }
#define STEP64(WT, FC, FN, HASN, WAITN, M0, OP0, OP1, OP2, OP3)                                                         \
  {                                                                                                                     \
    SCHED();                                                                                                            \
    acc[0][0] = mfma(WT[0], FC[0], acc[0][0]); SCHED(); OP0; SCHED();                                                   \
    acc[0][1] = mfma(WT[0], FC[1], acc[0][1]); acc[0][2] = mfma(WT[0], FC[2], acc[0][2]);                               \
    acc[0][3] = mfma(WT[0], FC[3], acc[0][3]); acc[1][0] = mfma(WT[1], FC[0], acc[1][0]); SCHED(); OP1; SCHED();        \
    acc[1][1] = mfma(WT[1], FC[1], acc[1][1]); SCHED();                                                                 \
    if (HASN) { WAITN; }                                                                                                \
    SCHED();                                                                                                            \
    acc[1][2] = mfma(WT[1], FC[2], acc[1][2]); SCHED(); if (HASN) { mx = (M0); asm volatile("" : "+v"(mx)); } SCHED(); \
    acc[1][3] = mfma(WT[1], FC[3], acc[1][3]); SCHED(); if (HASN) ACC4(mx, mx, FN[0]); SCHED();                        \
    acc[2][0] = mfma(WT[2], FC[0], acc[2][0]); SCHED(); OP2; SCHED();                                                   \
    acc[2][1] = mfma(WT[2], FC[1], acc[2][1]); SCHED(); if (HASN) ACC4(mx, mx, FN[1]); SCHED();                        \
    acc[2][2] = mfma(WT[2], FC[2], acc[2][2]); SCHED(); if (HASN) ACC4(mx, mx, FN[2]); SCHED();                        \
    acc[2][3] = mfma(WT[2], FC[3], acc[2][3]); SCHED(); if (HASN) ACC4(mx, mx, FN[3]); SCHED();                        \
    acc[3][0] = mfma(WT[3], FC[0], acc[3][0]); SCHED(); OP3; SCHED();                                                   \
    acc[3][1] = mfma(WT[3], FC[1], acc[3][1]); acc[3][2] = mfma(WT[3], FC[2], acc[3][2]);                               \
    acc[3][3] = mfma(WT[3], FC[3], acc[3][3]); SCHED();                                                                 \
  }

  unsigned vrot[4];
#pragma unroll
  for (int c = 0; c < 4; ++c) vrot[c] = vl16 + (c ^ w) * 1024;
  v4u hres[2];
  v4u w2f[14][4];
  cptr w2base = ws + W2_OFF + cg * 229376 + w * 57344;
#define W2L(n) do { if ((n) < 32) LD16CA(w2f[(n) >> 2][(n) & 3], vl16, w2base + ((n) >> 2) * 4096, ((n) & 3) * 1024);   \
                    else LD16C(w2f[(n) >> 2][(n) & 3], vl16, w2base + ((n) >> 2) * 4096, ((n) & 3) * 1024); } while (0)
  {
    f32x4 acc[4][4];
    v4u pq[8][4];
    unsigned ck[8];
    cptr pb = ws + PQ_OFF + ti * 131072 + w * 32768;
    cptr cb = ws + CKP_OFF + (ti * 32 + 8 * w) * 256;
    if (GATE & 1) WAITV(WDS + W1A);
#pragma unroll
    for (int k = 0; k < D2; ++k) {
      LD4C(ck[k], vl4, cb, k * 256);
#pragma unroll
      for (int c = 0; c < 4; ++c) LD16C(pq[k][c], vrot[c], pb + k * 4096 + WRONG, 0);
    }
    asm volatile("s_waitcnt vmcnt(%2)" : "+v"(xres[0]), "+v"(xres[1]) : "i"(5 * D2) : "memory");
#pragma unroll
    for (int i = 0; i < 4; ++i) {
      acc[i][0] = unpk(xres[i >> 1][2 * (i & 1)], xres[i >> 1][2 * (i & 1) + 1]);
#pragma unroll
      for (int c = 1; c < 4; ++c) acc[i][c] = f32x4{0.f, 0.f, 0.f, 0.f};
    }
#define P2WAIT(g, N) asm volatile("s_waitcnt vmcnt(%5)" : "+v"(pq[g][0]), "+v"(pq[g][1]), "+v"(pq[g][2]), "+v"(pq[g][3]), "+v"(ck[g]) : "i"(N) : "memory")
    P2WAIT(0, 5 * D2 - 5);
    unsigned mx = SUM16(tag - ck[0], pq[0]);
#define P2OP(i) do { if (k + D2 < 8) { if ((i) == 0) LD4C(ck[(k + D2) & 7], vl4, cb, ((k + D2) & 7) * 256);              \
                                       LD16C(pq[(k + D2) & 7][i], vrot[i], pb + (k + D2) * 4096 + WRONG, 0); }                  \
                     else if ((i) < p2_w1(k)) W1L(W1A + 4 * (k + D2 - 8) + (i)); } while (0)
    static_for<0, 8>([&](auto K_) {
      constexpr int k = K_;
      if (__builtin_expect(BAD(mx), 0)) {
        constexpr int hi = k + D2 < 8 ? k + D2 : 8;
        unsigned spin = SPIN_CAP;
        WAITV(0);
        do {
          COUNT(1);
#pragma unroll
          for (int g = k; g < hi; ++g) {
            RLD4(ck[g], vl4, cb, g * 256);
#pragma unroll
            for (int c = 0; c < 4; ++c) RLD16(pq[g][c], vrot[c], pb + g * 4096, 0);
          }
          WAITV(0);
          unsigned bad = 0;
#pragma unroll
          for (int g = k; g < hi; ++g) { unsigned a = tag; VSUB(a, ck[g]); VADD16(a, pq[g]); bad |= a; }
          if (!BAD(bad)) spin = 1;
        } while (--spin);
      }
      PIN4A(wd[k][0], wd[k][1], wd[k][2], wd[k][3]);
      if (k < 7) STEP64(wd[k], pq[k], pq[(k + 1) & 7], true, P2WAIT((k + 1) & 7, p2_wait((k + 1) & 7)), tag - ck[(k + 1) & 7], P2OP(0), P2OP(1), P2OP(2), P2OP(3));
    });
    last_step_reduce(smem + RED_OFF, w, lane, wd[7], pq[7], acc, hres, [&]() {
#pragma unroll
      for (int n = W1A + 4 * (D2 - 1); n < 28 - W1C; ++n) W1L(n);
    });
    const int hp = (4 * ti + w) * 8 + cg;
    ST16C(hres[0], vl16, ws + H_OFF + hp * 2048, 0);
    ST16C(hres[1], vl16, ws + H_OFF + hp * 2048, 1024);
    const unsigned hck = sum4(sum4(tag, hres[0]), hres[1]);
    ST4C(hck, vl4, ws + CKH_OFF + hp * 256, 0);
    f32x4 hv[4];
#pragma unroll
    for (int i = 0; i < 4; ++i) hv[i] = unpk(hres[i >> 1][2 * (i & 1)], hres[i >> 1][2 * (i & 1) + 1]);
    const f32x4 s4 = (hv[0] + hv[1]) + (hv[2] + hv[3]);
    const float m = rows_sum((s4[0] + s4[1]) + (s4[2] + s4[3])) * (1.0f / 64.0f);
    f32x4 q4 = {0.f, 0.f, 0.f, 0.f};
#pragma unroll
    for (int i = 0; i < 4; ++i) { const f32x4 c = hv[i] - m; q4 += c * c; }
    const float m2 = rows_sum((q4[0] + q4[1]) + (q4[2] + q4[3]));
    const unsigned mu = __builtin_bit_cast(unsigned, m), m2u = __builtin_bit_cast(unsigned, m2);
    const v4u st = {mu, m2u, tag + mu + m2u, 0u};
    asm volatile("s_mov_b64 exec, 0xffff\n\tglobal_store_dwordx4 %0, %1, %2 sc1\n\ts_mov_b64 exec, -1\n\ts_nop 0" ::"v"(vl16), "v"(st), "s"(ws + ST_OFF + hp * 256) : "memory");
  }
#pragma unroll
  for (int n = 28 - W1C; n < 28; ++n) W1L(n);
#pragma unroll
  for (int n = 0; n < W2A; ++n) W2L(n);
  {
    v4u hf[8][4];
    unsigned ck[8][2];
    v4u str[2][2];
    cptr hb = ws + H_OFF + (8 * t128 + 2 * w) * 16384;
    cptr cb = ws + CKH_OFF + (8 * t128 + 2 * w) * 2048;
    const unsigned vh1 = vl16 + 16384;
    const unsigned vs = q * 512 + r * 16;
    cptr stb = ws + ST_OFF + (8 * t128 + 2 * w) * 2048;
#define HL(m, i) LD16C(hf[m][i], ((i) & 1) ? vh1 : vl16, hb + ((m) >> 1) * 4096 + WRONG, ((m) & 1) * 2048 + ((i) >> 1) * 1024)
#define HC(m, t) LD4C(ck[m][t], vl4, cb, (t) * 2048 + (m) * 256)
    if (GATE & 2) WAITV(W1C + W2A + 1);
#pragma unroll
    for (int m = 0; m < 4; ++m) { HC(m, 0); HC(m, 1); HL(m, 0); HL(m, 1); HL(m, 2); HL(m, 3); }
    f32x4 acc[7][2];
#pragma unroll
    for (int l = 0; l < 7; ++l) { acc[l][0] = f32x4{0.f, 0.f, 0.f, 0.f}; acc[l][1] = f32x4{0.f, 0.f, 0.f, 0.f}; }
#define P3WAIT(m, N, TAIL) asm volatile("s_waitcnt vmcnt(%6)" TAIL : "+v"(hf[m][0]), "+v"(hf[m][1]), "+v"(hf[m][2]), "+v"(hf[m][3]), "+v"(ck[m][0]), "+v"(ck[m][1]) : "i"(N) : "memory")
    P3WAIT(0, p3_wait(0), " lgkmcnt(0)\n\ts_barrier");
    unsigned whi_ = lds0 + vl16 + 57344;
    asm volatile("" : "+v"(whi_));
    const LAS unsigned char* wlo = (const LAS unsigned char*)smem + vl16;
    const LAS unsigned char* whi = (const LAS unsigned char*)(size_t)whi_;
#define W1F(k, l) (*(const LAS v4u*)(((k) < 8 ? wlo + (k) * 7168 : whi + ((k) - 8) * 7168) + (l) * 1024))
    v4u wf[2][7];
#pragma unroll
    for (int l = 0; l < 7; ++l) wf[0][l] = W1F(0, l);
    unsigned mx = SUM16(tag2 - ck[0][0] - ck[0][1], hf[0]);
    float rd[2], sm[2], sq[2];
    unsigned nmf[2];
    auto stat_piece = [&](auto N_) {
      constexpr int n = N_ >> 1, t = N_ & 1;
      if constexpr (n == 0) sm[t] = swap16_sum(asf(str[t][0][0]) + asf(str[t][1][0]));
      if constexpr (n == 1) sm[t] = swap32_sum(sm[t]) * 0.125f;
      if constexpr (n == 2) { const float da = asf(str[t][0][0]) - sm[t], db = asf(str[t][1][0]) - sm[t]; sq[t] = da * da + db * db; }
      if constexpr (n == 3) sq[t] = asf(str[t][0][1]) + asf(str[t][1][1]) + 64.0f * sq[t];
      if constexpr (n == 4) sq[t] = swap16_sum(sq[t]);
      if constexpr (n == 5) sq[t] = swap32_sum(sq[t]) * (1.0f / D_);
      if constexpr (n == 6) nmf[t] = pk2(-sm[t], -sm[t]);
      if constexpr (n == 7) sq[t] = rbf(sq[t]) + 1e-5f;
      if constexpr (n == 8) sq[t] = __builtin_amdgcn_sqrtf(rbf(sq[t]));
      if constexpr (n == 9) rd[t] = __builtin_amdgcn_rcpf(rbf(sq[t]));
      if constexpr (n < 2) asm volatile("" : "+v"(sm[t]));
      else if constexpr (n == 6) asm volatile("" : "+v"(nmf[t]));
      else if constexpr (n == 9) asm volatile("" : "+v"(rd[t]));
      else if constexpr (n < 9) asm volatile("" : "+v"(sq[t]));
    };
#define P3OP(i) do { if (k < 4) { if ((i) < 2) HC((4 + k) & 7, (i) & 1); else HL((4 + k) & 7, ((i) - 2) & 3); }          \
                     else if (k == 4) { if ((i) < 4) LD16C(str[((i) >> 1) & 1][(i) & 1], vs, stb + WRONG, (((i) >> 1) & 1) * 2048 + ((i) & 1) * 256); } \
                     else if ((i) < p3_w2(k)) W2L(W2A + 4 * (k - 5) + (i)); } while (0)
    static_for<0, 16>([&](auto K_) {
      constexpr int k = K_, m = k >> 1, mn = ((k + 1) >> 1) & 7;
      if ((k & 1) == 0 && __builtin_expect(BAD(mx), 0)) {
        constexpr int hi = 4 + k < 8 ? 4 + k : 8;
        unsigned spin = SPIN_CAP;
        WAITV(0);
        do {
          COUNT(2);
#pragma unroll
          for (int g = m; g < hi; ++g) {
            RLD4(ck[g][0], vl4, cb, g * 256); RLD4(ck[g][1], vl4, cb, 2048 + g * 256);
#pragma unroll
            for (int c = 0; c < 4; ++c) RLD16(hf[g][c], (c & 1) ? vh1 : vl16, hb + g * 2048, (c >> 1) * 1024);
          }
          WAITV(0);
          unsigned bad = 0;
#pragma unroll
          for (int g = m; g < hi; ++g) { unsigned a = tag2; VSUB(a, ck[g][0]); VSUB(a, ck[g][1]); VADD16(a, hf[g]); bad |= a; }
          if (!BAD(bad)) spin = 1;
        } while (--spin);
      }
      if constexpr (k == P3_ST) {
        asm volatile("s_waitcnt vmcnt(%4)" : "+v"(str[0][0]), "+v"(str[0][1]), "+v"(str[1][0]), "+v"(str[1][1]) : "i"(p3_stwait()) : "memory");
#define STCK(a) (tag + (a)[0] + (a)[1] - (a)[2])
        if (__builtin_expect(BAD(STCK(str[0][0]) | STCK(str[0][1]) | STCK(str[1][0]) | STCK(str[1][1])), 0)) {
          unsigned spin = SPIN_CAP;
          WAITV(0);
          do {
            asm volatile("global_load_dwordx4 %0, %4, %5 sc1\n\tglobal_load_dwordx4 %1, %4, %5 offset:256 sc1\n\t"
                         "global_load_dwordx4 %2, %4, %5 offset:2048 sc1\n\tglobal_load_dwordx4 %3, %4, %5 offset:2304 sc1\n\ts_waitcnt vmcnt(0)"
                         : "=&v"(str[0][0]), "=&v"(str[0][1]), "=&v"(str[1][0]), "=&v"(str[1][1]) : "v"(vs), "s"(stb) : "memory");
            if (!BAD(STCK(str[0][0]) | STCK(str[0][1]) | STCK(str[1][0]) | STCK(str[1][1]))) spin = 1;
          } while (--spin);
        }
      }
      SCHED();
      static_for<0, 7>([&](auto L_) {
        constexpr int l = L_;
        constexpr bool CHK = (k & 1) && k + 1 < 16;
        acc[l][0] = mfma(wf[k & 1][l], hf[m][2 * (k & 1)], acc[l][0]);
        SCHED();
        if constexpr (k + 1 < 16) wf[(k + 1) & 1][l] = W1F(k + 1, l);
        if constexpr (CHK && k >= 4) {
          if constexpr (l == 4) P3WAIT(mn, p3_wait(mn), "");
          if constexpr (l == 5) ACC4(mx, mx, hf[mn][1]);
          if constexpr (l == 6) ACC4(mx, mx, hf[mn][3]);
        }
        if constexpr (CHK && k < 4 && l == 6) { ACC4(mx, mx, hf[mn][2]); ACC4(mx, mx, hf[mn][3]); }
        if constexpr (k >= P3_ST && !(k & 1)) stat_piece(std::integral_constant<int, 7 * ((k - P3_ST) / 2) + l>{});
        SCHED();
        acc[l][1] = mfma(wf[k & 1][l], hf[m][2 * (k & 1) + 1], acc[l][1]);
        SCHED();
        if constexpr (l < 6) P3OP(l);
        if constexpr (CHK && k >= 4) {
          if constexpr (l == 4) ACC4(mx, tag2 - ck[mn][0] - ck[mn][1], hf[mn][0]);
          if constexpr (l == 5) ACC4(mx, mx, hf[mn][2]);
        }
        if constexpr (CHK && k < 4 && l == 5) {
          P3WAIT(mn, p3_wait(mn), "");
          ACC4(mx, tag2 - ck[mn][0] - ck[mn][1], hf[mn][0]);
          ACC4(mx, mx, hf[mn][1]);
        }
        SCHED();
      });
    });
    const int kbp = (7 * fs + (fs & 1)) >> 1, taus = 7 * fs + ((fs & 1) ? 0 : 6);
    const int t64 = 2 * t128 + (w >> 1);
    unsigned char* fb = ws + FF_OFF + t64 * 229376 + kbp * 4096 + 2 * (w & 1) * 1024;
    v4u fck = {tag, tag, tag, tag};
    {
      const v4u n0 = {nmf[0], nmf[0], nmf[0], nmf[0]}, n1 = {nmf[1], nmf[1], nmf[1], nmf[1]};
#pragma unroll
      for (int l = 0; l < 7; ++l) {
        acc[l][0] = mfma(csf[l], n0, acc[l][0]);
        acc[l][1] = mfma(csf[l], n1, acc[l][1]);
      }
    }
    auto act = [&](int l, int t) {
      const f32x4 v = acc[l][t] * rd[t];
      return v2u{relu2(pk2(v[0], v[1])), relu2(pk2(v[2], v[3]))};
    };
#pragma unroll
    for (int m_ = 0; m_ < 3; ++m_)
#pragma unroll
      for (int t = 0; t < 2; ++t) {
        const v2u a = act(2 * m_, t), b = act(2 * m_ + 1, t);
        const v4u oo = {a[0], a[1], b[0], b[1]};
        if (t == 0) ST16C(oo, vl16, fb + m_ * 4096, 0); else ST16C(oo, vl16, fb + m_ * 4096, 1024);
        fck[m_] = sum4(fck[m_], oo);
      }
    const v2u a6 = act(6, 0), b6 = act(6, 1);
    const v4u o6 = {a6[0], a6[1], b6[0], b6[1]};
    ST16C(o6, vl16, ws + FF_OFF + t64 * 229376 + (taus >> 1) * 4096 + (2 * (taus & 1) + (w & 1)) * 1024, 0);
    fck[3] = sum4(fck[3], o6);
    ST16C(fck, vl16, ws + CKF_OFF + ((t64 * 16 + fs) * 2 + (w & 1)) * 1024, 0);
  }
#pragma unroll
  for (int n = 56 - W2C; n < 56; ++n) W2L(n);

  {
    f32x4 acc[4][4];
    constexpr int R = D4 + 1;
    v4u ff[R][4];
    v4u rec[4][2];
    cptr fb = ws + FF_OFF + ti * 229376 + w * 57344;
    cptr cb = ws + CKF_OFF + (ti * 16 + 4 * w) * 2048;
    unsigned vsh[4];
#pragma unroll
    for (int c = 0; c < 4; ++c) vsh[c] = vl16 + (c ^ (w >> 1)) * 1024;
#define VR4(g, c) ((g) % 7 == 3 ? vsh[c] : vrot[c])
#define RECB(g) ((g) == 0 ? 0 : (g) == 3 ? 1 : (g) == 7 ? 2 : 3)
#define RECL(b) { LD16C(rec[b][0], vl16, cb + ((b) >> 1) * 4096 + WRONG, ((b) & 1) * 2048); LD16C(rec[b][1], vl16, cb + ((b) >> 1) * 4096, ((b) & 1) * 2048 + 1024); }
#define RB0(g) (2 * ((g) / 7) + ((g) % 7 > 3 ? 1 : 0))
#define RC0(g) ((g) % 7 > 3 ? (g) % 7 - 4 : (g) % 7)
#define M4(RE, g) ((g) % 7 == 3 ? 2u * tag2 - RE[RB0(g)][0][3] - RE[RB0(g)][1][3] - RE[(RB0(g) + 1) & 3][0][3] - RE[(RB0(g) + 1) & 3][1][3]   \
                                : tag2 - RE[RB0(g)][0][RC0(g)] - RE[RB0(g)][1][RC0(g)])
    if (GATE & 4) WAITV(W2C);
#pragma unroll
    for (int k = 0; k < D4; ++k) {
      if (p4_rec(k)) RECL(RECB(k));
#pragma unroll
      for (int c = 0; c < 4; ++c) LD16C(ff[k][c], VR4(k, c), fb + k * 4096 + WRONG, 0);
    }
#pragma unroll
    for (int i = 0; i < 4; ++i) {
      acc[i][0] = unpk(hres[i >> 1][2 * (i & 1)], hres[i >> 1][2 * (i & 1) + 1]);
#pragma unroll
      for (int c = 1; c < 4; ++c) acc[i][c] = f32x4{0.f, 0.f, 0.f, 0.f};
    }
#define P4WAIT(g, N) do { asm volatile("s_waitcnt vmcnt(%4)" : "+v"(ff[(g) % R][0]), "+v"(ff[(g) % R][1]), "+v"(ff[(g) % R][2]), "+v"(ff[(g) % R][3]) : "i"(N) : "memory"); \
                          if (p4_rec(g)) asm volatile("" : "+v"(rec[RECB(g)][0]), "+v"(rec[RECB(g)][1])); } while (0)
    P4WAIT(0, 4 * D4 - 4 + 2);
    unsigned mx = SUM16(M4(rec, 0), ff[0]);
#define P4OP(i) do { if (k + D4 < 14) { if ((i) == 0 && p4_rec(k + D4)) RECL(RECB(k + D4));                              \
                                        LD16C(ff[(k + D4) % R][i], VR4(k + D4, i), fb + (k + D4) * 4096 + WRONG, 0); } } while (0)
    static_for<0, 14>([&](auto K_) {
      constexpr int k = K_;
      if (__builtin_expect(BAD(mx), 0)) {
        constexpr int hi = k + D4 < 14 ? k + D4 : 14, b0 = RB0(k), b1 = RB0(hi - 1) + ((hi - 1) % 7 == 3 ? 1 : 0);
        unsigned spin = SPIN_CAP;
        WAITV(0);
        do {
          COUNT(3);
          if constexpr (k == 13) asm volatile(".Lfnet_last:");
#pragma unroll
          for (int b = b0; b <= b1; ++b) { RLD16(rec[b][0], vl16, cb + b * 2048, 0); RLD16(rec[b][1], vl16, cb + b * 2048, 1024); }
#pragma unroll
          for (int g = k; g < hi; ++g)
#pragma unroll
            for (int c = 0; c < 4; ++c) RLD16(ff[g % R][c], VR4(g, c), fb + g * 4096, 0);
          WAITV(0);
          unsigned bad = 0;
#pragma unroll
          for (int g = k; g < hi; ++g) {
            unsigned a = g % 7 == 3 ? 2u * tag2 : tag2;
            VSUB(a, rec[RB0(g)][0][RC0(g)]); VSUB(a, rec[RB0(g)][1][RC0(g)]);
            if (g % 7 == 3) { VSUB(a, rec[(RB0(g) + 1) & 3][0][3]); VSUB(a, rec[(RB0(g) + 1) & 3][1][3]); }
            VADD16(a, ff[g % R]);
            bad |= a;
          }
          if (!BAD(bad)) spin = 1;
        } while (--spin);
      }
      if (k % 7 == 3) {
        v4u* F = ff[k % R];
        const bool w0 = w & 1;
        const v4u e0 = {F[0][0], F[0][1], F[2][0], F[2][1]}, o0 = {F[0][2], F[0][3], F[2][2], F[2][3]};
        const v4u e1 = {F[1][0], F[1][1], F[3][0], F[3][1]}, o1 = {F[1][2], F[1][3], F[3][2], F[3][3]};
        F[0] = w0 ? o0 : e0; F[1] = w0 ? e0 : o0; F[2] = w0 ? o1 : e1; F[3] = w0 ? e1 : o1;
      }
      if (k < 8) PIN4A(w2f[k][0], w2f[k][1], w2f[k][2], w2f[k][3]);
      else PIN4(w2f[k][0], w2f[k][1], w2f[k][2], w2f[k][3]);
      if (k < 13) STEP64(w2f[k], ff[k % R], ff[(k + 1) % R], true, P4WAIT((k + 1) % 14, p4_wait((k + 1) % 14)), M4(rec, (k + 1) % 14), P4OP(0), P4OP(1), P4OP(2), P4OP(3));
    });
    v4u o[2];
    last_step_reduce(smem + RED_OFF, w, lane, w2f[13], ff[13 % R], acc, o, []() {});
    unsigned char* ob_ = out + (size_t)(64 * ti + 16 * w) * 1024 + cg * 128;
    ST16(o[0], vx, ob_, 0);
    ST16(o[1], vx, ob_, 64);
  }
}

extern "C" void kernel_launch(void* const* d_in, const int* in_sizes, int n_in, void* d_out, int out_size, void* d_ws,
                              size_t ws_size, hipStream_t stream) {
  (void)hipFuncSetAttribute((const void*)fnet_fused, hipFuncAttributeMaxDynamicSharedMemorySize, LDS_BYTES);
  fnet_fused<<<256, 256, LDS_BYTES, stream>>>((cptr)d_in[0], (cptr)d_in[1], (cptr)d_in[2], (cptr)d_in[3], (cptr)d_in[4],
                                              (cptr)d_in[6], (cptr)d_in[7], (unsigned char*)d_ws, (unsigned char*)d_out);
}
```

```cpp
#include <hip/hip_runtime.h>
#include <hip/hip_bf16.h>
#include <stdint.h>
#include <type_traits>

typedef unsigned v4u __attribute__((ext_vector_type(4)));
typedef unsigned v2u __attribute__((ext_vector_type(2)));
typedef float f32x4 __attribute__((ext_vector_type(4)));
typedef float f32x2 __attribute__((ext_vector_type(2)));
typedef short bf16x8 __attribute__((ext_vector_type(8)));
typedef short v4s __attribute__((ext_vector_type(4)));
typedef __bf16 bf2_t __attribute__((ext_vector_type(2)));
typedef const unsigned char* cptr;
#define LAS __attribute__((address_space(3)))

constexpr int S_ = 2048, D_ = 512, F_ = 1792;
constexpr unsigned EPOCH_OFF = 0;
constexpr unsigned FLAG0_OFF = 32768;
constexpr unsigned XNT_OFF = 131072;
constexpr unsigned WD_OFF  = XNT_OFF + (2u << 20);
constexpr unsigned W1_OFF  = WD_OFF + (1u << 20);
constexpr unsigned W2_OFF  = W1_OFF + 1835008u;
constexpr unsigned CS_OFF  = W2_OFF + 1835008u;
constexpr unsigned PQ_OFF  = CS_OFF + 131072;
constexpr unsigned H_OFF   = PQ_OFF + (4u << 20);
constexpr unsigned FF_OFF  = H_OFF + (2u << 20);
constexpr unsigned ST_OFF  = FF_OFF + 7340032u;
constexpr unsigned CKP_OFF = ST_OFF + 262144;
constexpr unsigned CKH_OFF = CKP_OFF + 131072;
constexpr unsigned CKF_OFF = CKH_OFF + 131072;
constexpr unsigned CKX_OFF = CKF_OFF + 131072;

constexpr int XS_ROW = 1088;
constexpr int XS_OFF = 131072, WST_OFF = XS_OFF + 8 * XS_ROW;
constexpr int XT_OFF = WST_OFF + 4352;
constexpr int RED_OFF = 114688;
constexpr int LDS_BYTES = 163840;
constexpr unsigned SPIN_CAP = 1u << 20;

__device__ __forceinline__ unsigned pk2(float a, float b) {
  f32x2 v = {a, b};
  return __builtin_bit_cast(unsigned, __builtin_convertvector(v, bf2_t));
}
__device__ __forceinline__ float asf(unsigned u) { return __builtin_bit_cast(float, u); }
__device__ __forceinline__ float bflo(unsigned u) { return __builtin_bit_cast(float, u << 16); }
__device__ __forceinline__ float bfhi(unsigned u) { return __builtin_bit_cast(float, u & 0xffff0000u); }
__device__ __forceinline__ float rbf(float x) { return (float)(__bf16)x; }
typedef short v2s __attribute__((ext_vector_type(2)));
__device__ __forceinline__ unsigned relu2(unsigned u) {
  return __builtin_bit_cast(unsigned, __builtin_elementwise_max(__builtin_bit_cast(v2s, u), v2s{0, 0}));
}
__device__ __forceinline__ f32x4 unpk(unsigned a, unsigned b) { return f32x4{bflo(a), bfhi(a), bflo(b), bfhi(b)}; }
__device__ __forceinline__ float ln_rstd(float var) {
  return __builtin_amdgcn_rcpf(rbf(__builtin_amdgcn_sqrtf(rbf(rbf(var) + 1e-5f))));
}
__device__ __forceinline__ f32x4 mfma(v4u a, v4u b, f32x4 c) {
  return __builtin_amdgcn_mfma_f32_16x16x32_bf16(__builtin_bit_cast(bf16x8, a), __builtin_bit_cast(bf16x8, b), c, 0, 0, 0);
}
template <int CTRL> __device__ __forceinline__ float dpp_add(float x) {
  return x + __builtin_bit_cast(float, __builtin_amdgcn_update_dpp(0, __builtin_bit_cast(int, x), CTRL, 0xf, 0xf, true));
}
__device__ __forceinline__ float row16_sum(float x) {
  return dpp_add<0x121>(dpp_add<0x122>(dpp_add<0x124>(dpp_add<0x128>(x))));
}
__device__ __forceinline__ float swap16_sum(float x) {
  unsigned a = __builtin_bit_cast(unsigned, x), b = a;
  asm("" : "+v"(b));
  auto r = __builtin_amdgcn_permlane16_swap(a, b, false, false);
  return asf(r[0]) + asf(r[1]);
}
__device__ __forceinline__ float swap32_sum(float x) {
  unsigned a = __builtin_bit_cast(unsigned, x), b = a;
  asm("" : "+v"(b));
  auto r = __builtin_amdgcn_permlane32_swap(a, b, false, false);
  return asf(r[0]) + asf(r[1]);
}
__device__ __forceinline__ float rows_sum(float x) { return swap32_sum(swap16_sum(x)); }
__device__ __forceinline__ float half_sum(float x) { return swap16_sum(row16_sum(x)); }
#define SCHED() __builtin_amdgcn_sched_barrier(0)
#define WAITV(n) asm volatile("s_waitcnt vmcnt(%0)" ::"i"(n) : "memory")
#define WAITV_BAR(n) asm volatile("s_waitcnt vmcnt(%0) lgkmcnt(0)\n\ts_barrier" ::"i"(n) : "memory")
#define BAR() asm volatile("s_waitcnt lgkmcnt(0)\n\ts_barrier" ::: "memory")
#define ACC4(m, m0, v) do { m = sum4(m0, v); asm volatile("" : "+v"(m)); } while (0)
#define PIN4(a, b, c, d) asm volatile("" : "+v"(a), "+v"(b), "+v"(c), "+v"(d))
#define PIN4A(a, b, c, d) asm volatile("" : "+a"(a), "+a"(b), "+a"(c), "+a"(d))

#define LD16(d, vo, sb, imm) asm volatile("global_load_dwordx4 %0, %1, %2 offset:%3" : "=v"(d) : "v"(vo), "s"(sb), "i"(imm) : "memory")
#define LD16C(d, vo, sb, imm) asm volatile("global_load_dwordx4 %0, %1, %2 offset:%3 sc1" : "=v"(d) : "v"(vo), "s"(sb), "i"(imm) : "memory")
#define LD16CA(d, vo, sb, imm) asm volatile("global_load_dwordx4 %0, %1, %2 offset:%3 sc1" : "=a"(d) : "v"(vo), "s"(sb), "i"(imm) : "memory")
#define LD4C(d, vo, sb, imm) asm volatile("global_load_dword %0, %1, %2 offset:%3 sc1" : "=v"(d) : "v"(vo), "s"(sb), "i"(imm) : "memory")
#define ST16C(v, vo, sb, imm) asm volatile("global_store_dwordx4 %0, %1, %2 offset:%3 sc1\n\ts_nop 1" ::"v"(vo), "v"(v), "s"(sb), "i"(imm) : "memory")
#define ST16(v, vo, sb, imm) asm volatile("global_store_dwordx4 %0, %1, %2 offset:%3\n\ts_nop 1" ::"v"(vo), "v"(v), "s"(sb), "i"(imm) : "memory")
#define ST4C(v, vo, sb, imm) asm volatile("global_store_dword %0, %1, %2 offset:%3 sc1" ::"v"(vo), "v"(v), "s"(sb), "i"(imm) : "memory")
#define RLD16(d, vo, sb, imm) asm volatile("global_load_dwordx4 %0, %1, %2 offset:%3 sc1" ::"v"(d), "v"(vo), "s"(sb), "i"(imm) : "memory")
#define RLD4(d, vo, sb, imm) asm volatile("global_load_dword %0, %1, %2 offset:%3 sc1" ::"v"(d), "v"(vo), "s"(sb), "i"(imm) : "memory")
#define CK_INIT(a, T, MASK) asm volatile("v_sub_u32 %0, %2, %1\n\tv_cndmask_b32 %0, 0, %0, %3" : "=&v"(a) : "v"(T), "s"(tag), "s"((unsigned long long)(MASK)))
#define CK_RED(a, i) do { a = isum<i>(a); asm volatile("" : "+v"(a)); } while (0)
#define CK_END(sv, a) do { sv = last_lane(a); asm volatile("" : "+s"(sv)); } while (0)
#define VADD4(a, f) asm volatile("v_add3_u32 %0, %0, %1, %2\n\tv_add3_u32 %0, %0, %3, %4" : "+v"(a) : "v"((f)[0]), "v"((f)[1]), "v"((f)[2]), "v"((f)[3]))
#define CK_ALL(sv, T, MASK, F) do { unsigned a_; CK_INIT(a_, T, MASK); VADD4(a_, F[0]); VADD4(a_, F[1]); VADD4(a_, F[2]); VADD4(a_, F[3]);   \
                                    asm volatile("s_nop 1" : "+v"(a_)); sv = last_lane(wave_isum(a_)); } while (0)
#define WRONG 0
#define TOUCH(vo, sb) asm volatile("global_load_dword %0, %1, %2" : "+v"(sink) : "v"(vo), "s"(sb) : "memory")
#define COUNT(i)
#define ST_TAG(v, p) asm volatile("s_mov_b64 exec, 0xff\n\tglobal_store_dwordx4 %0, %1, %2 sc1\n\ts_mov_b64 exec, -1\n\ts_nop 0" ::"v"(vl16), "v"(v), "s"(p) : "memory")
#define GLDS(vo, sb, lds, imm) asm volatile("s_mov_b32 m0, %2\n\ts_nop 0\n\tglobal_load_lds_dwordx4 %0, %1 offset:%3" ::"v"(vo), "s"(sb), "s"(lds), "i"(imm) : "memory")
#define GLDSC(vo, sb, lds, imm) asm volatile("s_mov_b32 m0, %2\n\ts_nop 0\n\tglobal_load_lds_dwordx4 %0, %1 offset:%3 sc1" ::"v"(vo), "s"(sb), "s"(lds), "i"(imm) : "memory")

__device__ __forceinline__ unsigned sum4(unsigned m, v4u v) { return (m + v[0] + v[1]) + v[2] + v[3]; }
template <int I> __device__ __forceinline__ unsigned isum(unsigned x) {
  constexpr int ctrl = I < 4 ? 0x110 + (1 << I) : 0x142 + (I - 4), rm = I < 4 ? 0xf : I == 4 ? 0xa : 0xc;
  return x + (unsigned)__builtin_amdgcn_update_dpp(0, (int)x, ctrl, rm, 0xf, I < 4);
}
__device__ __forceinline__ unsigned row_isum(unsigned x) { return isum<3>(isum<2>(isum<1>(isum<0>(x)))); }
__device__ __forceinline__ unsigned wave_isum(unsigned x) { return isum<5>(isum<4>(row_isum(x))); }
__device__ __forceinline__ unsigned last_lane(unsigned x) { return (unsigned)__builtin_amdgcn_readlane((int)x, 63); }
__device__ __forceinline__ void poll_flags(cptr base, unsigned vo, unsigned tag) {
  for (unsigned spin = 0; spin < SPIN_CAP; ++spin) {
    unsigned v;
    asm volatile("global_load_dword %0, %1, %2 sc1\n\ts_waitcnt vmcnt(0)" : "=&v"(v) : "v"(vo), "s"(base) : "memory");
    if (__all(v == tag)) break;
  }
}
template <class H>
__device__ __forceinline__ void last_step_reduce(unsigned char* red, int w, int lane, const v4u (&wt)[4], const v4u (&fc)[4],
                                                 f32x4 (&acc)[4][4], v4u (&o)[2], H&& hook) {
#pragma unroll
  for (int c = 1; c <= 4; ++c) {
#pragma unroll
    for (int i = 0; i < 4; ++i) {
      acc[i][c & 3] = mfma(wt[i], fc[c & 3], acc[i][c & 3]);
      SCHED();
      if (c >= 2) *(f32x4*)(red + (((c - 1) ^ w) * 3 + c - 2) * 4096 + lane * 16 + i * 1024) = acc[i][c - 1];
      SCHED();
    }
  }
  hook();
  BAR();
  const unsigned char* s = red + w * 12288 + lane * 16;
  f32x4 pt[3][4];
#pragma unroll
  for (int i = 0; i < 4; ++i)
#pragma unroll
    for (int c = 0; c < 3; ++c) pt[c][i] = *(const f32x4*)(s + c * 4096 + i * 1024);
  SCHED();
#pragma unroll
  for (int i = 0; i < 4; ++i) {
    const f32x4 y = (acc[i][0] + pt[0][i]) + (pt[1][i] + pt[2][i]);
    o[i >> 1][2 * (i & 1)] = pk2(y[0], y[1]);
    o[i >> 1][2 * (i & 1) + 1] = pk2(y[2], y[3]);
  }
}

#define NP0 8
#define FL0_CHK 19
#define W1A 0
#define W1C 8
#define GATE 7
#define W2A 4
#define W2C 12
#define WDS 12
#define XT0 20
#define DB 5
#define XTP 1
#define XTD 3
#define BP 4
#define RAN 1
#define D2 4
#define D4 5
#define FL0_PUB 11
constexpr int FL0_IT = FL0_CHK - 3, NV = 32, NX = 42 - WDS;

template <int I, int N, class F> __device__ __forceinline__ void static_for(F&& f) {
  if constexpr (I < N) { f(std::integral_constant<int, I>{}); static_for<I + 1, N>(f); }
}
constexpr bool p1_xt(int s) { return (s & 7) == 7 - XTD && s + XTD + 1 < NV; }
constexpr bool p1_bar(int s) { return (s + 1) % BP == 0; }
constexpr bool p1_ra(int s) { return s >= BP - 1 && s < 15 + BP && (s + 1) % BP < BP / RAN; }
constexpr int p1_as(int s) { return 16 + ((s + 1) / BP - 1) * BP + ((s + 1) % BP) * RAN; }
constexpr int p1_xbase(int s) { int n = 0; for (int u = XT0; u < s; ++u) n += p1_ra(u) ? 0 : (u + DB < 32 ? 2 : 6); return n > NX ? NX : n; }
constexpr int p1_extra(int s) { return s < XT0 ? 0 : p1_xbase(s + 1) - p1_xbase(s); }
static_assert(p1_xbase(32) == NX && XT0 > FL0_CHK && p1_bar(FL0_CHK) && p1_bar(FL0_PUB) && p1_bar(7) && XTP < DB && DB <= 7 && RAN <= 2, "");
constexpr int p1_wait(int it, bool mid) {
  int posA[48] = {}, posB[40] = {}, posF = -1, n = 0, pos0 = 4 * DB, posT = -1;
  for (int s = 0; s < 48; ++s) posA[s] = -1;
  for (int s = 0; s < DB; ++s) { n += 4; posB[s] = n - 1; }
  if (it < 0) return n - 1 - posB[XTP];
  for (int s = 0; s <= it; ++s) {
    if (s == FL0_IT && s < it) posF = n - 1;
    if (p1_xt(s) && s < it) posT = n - 1;
    if (s == it) break;
    if (p1_ra(s)) for (int u = 0; u < RAN; ++u) { n += 2; posA[p1_as(s) + u] = n - 1; }
    n += p1_extra(s);
    if (s + DB < 32) { n += 4; posB[s + DB] = n - 1; }
  }
  int req = -1;
  if (mid) req = posB[it + 1];
  else {
    if (it + BP < 32) req = posA[it + BP];
    if (it == FL0_CHK && posF > req) req = posF;
    if (it == FL0_PUB && pos0 > req) req = pos0;
    if ((it & 7) == 7 && it + 1 < NV && posT > req) req = posT;
  }
  return n - 1 - req > 63 ? 63 : n - 1 - req;
}
constexpr int p2_w1(int s) { int n = 28 - W1A - 4 * (s - (8 - D2)); return (s + D2 < 8 || s == 7) ? 0 : n > 4 ? 4 : n < 0 ? 0 : n; }
constexpr int pk_wait(int k, int D, int NG) {
  int n = (k < D && k >= 2) ? 1 : 0;
  for (int g = k + 1; g < NG && g < k - 1 + D; ++g) n += 4;
  return n;
}
constexpr int p2_wait(int k) { int n = pk_wait(k, D2, 8); for (int s = 0; s < k - 1; ++s) n += p2_w1(s); return n; }
constexpr int p4_wait(int k) { return pk_wait(k, D4, 14); }
constexpr int W2B = 56 - W2A - W2C;
constexpr int p3_w2(int s) { int n = W2B - 4 * (s - 5); return s < 5 ? 0 : n > 4 ? 4 : n < 0 ? 0 : n; }
constexpr int p3_ops(int s) { return s == 0 ? 5 : s < 5 ? 4 : p3_w2(s); }
constexpr int p3_wait(int m) {
  int n = m < 4 ? 4 * (3 - m) : 0;
  for (int s = (m < 4 ? 0 : m - 3); s < 2 * m - 1; ++s) n += p3_ops(s);
  return n;
}
constexpr int P3_ST = 10;
constexpr int p3_stwait() { int n = 0; for (int s = 5; s < P3_ST; ++s) n += p3_ops(s); return n; }
static_assert(W2B >= 0 && W2B <= 44 && W1A >= 0 && W1A <= 28 && W1A % 4 == 0, "");

__global__ __launch_bounds__(256, 1) void fnet_fused(cptr x, cptr wsre, cptr wsim, cptr wre, cptr wim, cptr w1, cptr w2,
                                                     unsigned char* ws, unsigned char* out) {
  extern __shared__ __attribute__((aligned(16))) unsigned char smem[];
  const int bid = blockIdx.x, p = bid & 7, j = bid >> 3;
  const int ti = 4 * p + (j >> 3), cg = j & 7, t128 = 2 * p + (j >> 4), fs = j & 15;
  const int tid = threadIdx.x, lane = tid & 63, w = __builtin_amdgcn_readfirstlane(tid >> 6), q = lane >> 4, r = lane & 15;
  const unsigned lds0 = (unsigned)(size_t)(LAS unsigned char*)smem;
  const unsigned vl16 = lane * 16;

  const int mat = (j >> 2) & 1, nt = j & 3;
  unsigned epoch, sink = 0;
  v4u xr[2];
  {
    const unsigned vo = (lane >> 5) * 1024 + (lane & 31) * 16, zero = 0;
    cptr xb = x + (size_t)(8 * bid + 2 * w) * 1024;
    LD16(xr[0], vo, xb, 0);
    LD16(xr[1], vo, xb, 512);
    LD4C(epoch, zero, ws + EPOCH_OFF, 0);
  }
  const unsigned va0 = (lane >> 3) * 4096 + (((lane & 7) ^ (lane >> 4)) << 4), va1 = (va0 ^ 64) + 32768 - 1024;
  const int rot = nt & 1;
  cptr abase = (mat ? wsim : wsre) + (size_t)(64 * ti + 16 * w) * 4096;
  cptr abase0 = abase + rot * 2048, abase1 = abase - rot * 2048;
  const unsigned lda = lds0 + w * 2048;
#define A_STAGE0(s) GLDS(va0, ((s) < 16 ? abase0 : abase1) + (s) * 128, lda + ((s) & 15) * 8192, 0)
#define A_STAGE1(s) GLDS(va1, ((s) < 16 ? abase0 : abase1) + (s) * 128, lda + ((s) & 15) * 8192, 1024)
  const int kb1 = bid >> 4, fs1 = bid & 15, kbd = bid & 31, kb2 = 2 * (kbd < 28 ? kbd : 27) + (w & 1);
  {
    cptr pc;
    unsigned len;
    asm volatile("s_getpc_b64 %0\n.Lfnet_pc:\n\ts_mov_b32 %1, .Lfnet_last-.Lfnet_pc" : "=s"(pc), "=s"(len));
    const unsigned vo = min(((4 * j + w) * 4 + (lane & 3)) * 128u, len);
    TOUCH(vo, pc);
  }
  const int wst = WST_OFF + w * 5952;
  v4u wdp;
  auto wload = [&]() {
    const unsigned rs = w < 2 ? D_ * 2 : F_ * 2, vs = (lane >> 3) * rs + (lane & 7) * 16;
    cptr sb = w < 2 ? w2 + (size_t)(32 * kb2) * (D_ * 2) + (bid >> 5) * 128
                    : w1 + (size_t)(32 * kb1) * (F_ * 2) + ((7 * fs1) >> 1) * 64 + (w - 2) * 128;
#pragma unroll
    for (int k = 0; k < 4; ++k) GLDS(vs, sb + k * 8 * rs, lds0 + wst + k * 1088, 0);
    const unsigned vo = (8 * (r >> 2) + (r & 3)) * 1024 + q * 16;
    LD16(wdp, vo, (kbd < 16 ? wre : wim) + (size_t)(64 * (bid >> 5) + 32 * (w >> 1) + 4 * (w & 1)) * 1024 + (kbd & 15) * 64, 0);
  };
  auto wstore = [&]() {
    if (kbd >= 16) wdp ^= 0x80008000u;
    ST16C(wdp, vl16, ws + WD_OFF + bid * 4096 + w * 1024, 0);
    const unsigned char* s = smem + wst + q * 1088 + ((lane >> 2) & 3) * 128 + 16 * (lane & 3);
    unsigned char* db = w < 2 ? ws + W2_OFF + ((bid >> 5) * 56 + kb2) * 4096 : ws + W1_OFF + fs1 * 114688 + kb1 * 7168;
    float csv = 0.f;
#pragma unroll
    for (int i = 0; i < 4; ++i) {
      const int tl = 4 * (w - 2) + i, l = w < 2 ? (kbd < 28 ? i : 7) : (fs1 & 1) ? (tl == 0 ? 7 : tl == 1 ? 6 : tl - 2) : tl;
      const unsigned char* sl = s + (i >> 1) * 64 + (i & 1) * 8;
      const v2u lo = __builtin_bit_cast(v2u, __builtin_amdgcn_ds_read_tr16_b64_v4i16((LAS v4s*)sl));
      const v2u hi = __builtin_bit_cast(v2u, __builtin_amdgcn_ds_read_tr16_b64_v4i16((LAS v4s*)(sl + 512)));
      const v4u o = {lo[0], lo[1], hi[0], hi[1]};
      if (l < 7) ST16C(o, vl16, db + l * 1024, 0);
      const float cs = rows_sum(((bflo(o[0]) + bfhi(o[0])) + (bflo(o[1]) + bfhi(o[1]))) + ((bflo(o[2]) + bfhi(o[2])) + (bflo(o[3]) + bfhi(o[3]))));
      csv = (q == i) ? cs : csv;
    }
    const unsigned vc = r * 16 + q * 4, chl = pk2(csv, csv - rbf(csv));
    if (w >= 2) ST4C(chl, vc, ws + CS_OFF + fs1 * 8192 + (w - 2) * 4096 + kb1 * 256, 0);
  };
#pragma unroll 1
  for (int s = 0; s < NP0; ++s) { A_STAGE0(s); A_STAGE1(s); }
  WAITV(2 * NP0 + 1);
  asm volatile("" : "+v"(xr[0]), "+v"(xr[1]), "+v"(epoch));
  const unsigned tag = __builtin_amdgcn_readfirstlane(epoch) + 1u;
  {
    float v[16];
#pragma unroll
    for (int i = 0; i < 2; ++i)
#pragma unroll
      for (int k = 0; k < 4; ++k) { v[8 * i + 2 * k] = bflo(xr[i][k]); v[8 * i + 2 * k + 1] = bfhi(xr[i][k]); }
    float s1[4] = {0.f, 0.f, 0.f, 0.f};
#pragma unroll
    for (int k = 0; k < 16; ++k) s1[k & 3] += v[k];
    const float mean = half_sum((s1[0] + s1[1]) + (s1[2] + s1[3])) * (1.0f / D_);
    float s2[4] = {0.f, 0.f, 0.f, 0.f};
#pragma unroll
    for (int k = 0; k < 16; ++k) { const float c = v[k] - mean; s2[k & 3] += c * c; }
    const float mb = rbf(mean), rd = ln_rstd(half_sum((s2[0] + s2[1]) + (s2[2] + s2[3])) * (1.0f / D_));
#pragma unroll
    for (int i = 0; i < 2; ++i) {
      v4u o;
#pragma unroll
      for (int k = 0; k < 4; ++k) {
        const unsigned c2 = pk2(v[8 * i + 2 * k] - mb, v[8 * i + 2 * k + 1] - mb);
        o[k] = pk2(bflo(c2) * rd, bfhi(c2) * rd);
      }
      *(v4u*)(smem + XS_OFF + (2 * w + (lane >> 5)) * XS_ROW + (lane & 31) * 16 + i * 512) = o;
    }
  }
  BAR();
  auto tr8 = [&](int off, int i) {
    const unsigned char* s = smem + off + ((lane >> 2) & 3) * XS_ROW + 64 * (q >> 1) + 16 * (lane & 3) + 8 * (q & 1) + 128 * i;
    const v2u lo = __builtin_bit_cast(v2u, __builtin_amdgcn_ds_read_tr16_b64_v4i16((LAS v4s*)s));
    const v2u hi = __builtin_bit_cast(v2u, __builtin_amdgcn_ds_read_tr16_b64_v4i16((LAS v4s*)(s + 4 * XS_ROW)));
    return v4u{lo[0], lo[1], hi[0], hi[1]};
  };
  {
    const v4u o0 = tr8(XS_OFF, 2 * w), o1 = tr8(XS_OFF, 2 * w + 1);
    const unsigned vo = (q >> 1) * 131072 + (q & 1) * 1024 + r * 16;
    unsigned char* sb = ws + XNT_OFF + (bid >> 2) * 2048 + (bid & 3) * 256 + w * 524288;
    ST16C(o0, vo, sb, 0);
    ST16C(o1, vo, sb + 262144, 0);
    auto sw = __builtin_amdgcn_permlane16_swap(sum4(0u, o0), sum4(0u, o1), false, false);
    const int ck = (int)row_isum(sw[0] + sw[1]);
    const v4u c4 = {tag + __builtin_amdgcn_readlane(ck, 15), tag + __builtin_amdgcn_readlane(ck, 47),
                    tag + __builtin_amdgcn_readlane(ck, 31), tag + __builtin_amdgcn_readlane(ck, 63)};
    ST_TAG(c4, ws + CKX_OFF + bid * 512 + w * 128);
  }
  wload();
#pragma unroll 1
  for (int s = NP0; s < 16; ++s) { A_STAGE0(s); A_STAGE1(s); }

  cptr bbase0 = ws + XNT_OFF + (4 * nt + w) * 131072 + rot * 65536, bbase1 = bbase0 - rot * 131072;
  cptr xcb = ws + CKX_OFF + nt * 128;
#define BB(s) ((s) < 16 ? bbase0 : bbase1)
#define XCB(r8) (xcb + (((r8) + 2 * rot) & 3) * 32768)
  const unsigned vl512 = lane * 512;
  v4u bf[8][4];
  unsigned xt[(NV + 7) / 8];
#define B_LOAD(s, i) LD16C(bf[(s) & 7][i], vl16, BB(s) + (s) * 4096 + ((s) < NV ? WRONG : 0), (i) * 1024)
#pragma unroll
  for (int s = 0; s < DB; ++s) {
    B_LOAD(s, 0); B_LOAD(s, 1); B_LOAD(s, 2); B_LOAD(s, 3);
    if (s == XTP && w == 0) GLDSC(vl512, XCB(0), lds0 + XT_OFF, 0);
  }
  asm volatile("s_waitcnt vmcnt(%2)" : "+v"(wdp), "+v"(sink) : "i"(4 * DB) : "memory");
  wstore();
  f32x4 acc1[2][4];
#pragma unroll
  for (int i = 0; i < 2; ++i)
#pragma unroll
    for (int t = 0; t < 4; ++t) acc1[i][t] = f32x4{0.f, 0.f, 0.f, 0.f};
  const unsigned ra0 = r * 128 + ((q ^ (r >> 1)) << 4), ra1 = ra0 ^ 64;
  v4u af[2][8];
  v4u xres[2], csr[8], wd[8][4];
  unsigned flw = tag;
  const v4u tag4 = {tag, tag, tag, tag};
  const unsigned vfl = (lane < 32 ? 32 * cg + lane : 16 * r + fs) * 128;
  cptr xb2 = x + (size_t)(64 * ti + 16 * w) * 1024 + cg * 128;
  const unsigned vx = r * 1024 + q * 16;
  cptr cb2 = ws + CS_OFF + fs * 8192;
  const unsigned vc2 = q * 1024 + r * 16;
  cptr wb = ws + WD_OFF + cg * 131072 + w * 32768;
#define BAD(M) (__builtin_amdgcn_ballot_w64((M) != 0u) != 0)
#define WAIT4(F, N, TAIL) asm volatile("s_waitcnt vmcnt(%4)" TAIL : "+v"(F[0]), "+v"(F[1]), "+v"(F[2]), "+v"(F[3]) : "i"(N) : "memory")
#define XMASK(s) (0xffull << (8 * ((s) & 7)))
  WAIT4(bf[0], p1_wait(-1, false), " lgkmcnt(0)\n\ts_barrier");
#define XT_READ(i) do { xt[i] = *(const unsigned*)(smem + XT_OFF + (i) * 5952 + vl16 + w * 4); asm volatile("" : "+v"(xt[i])); } while (0)
  XT_READ(0);
#pragma unroll
  for (int t = 0; t < 4; ++t) { af[0][t] = *(const v4u*)(smem + ra0 + t * 2048); af[0][4 + t] = *(const v4u*)(smem + ra1 + t * 2048); }
  unsigned bx, bs;
  CK_ALL(bs, xt[0], XMASK(0), bf[0]);
#define P1_EXTRA(e)                                                                                                     \
  if constexpr ((e) < p1_extra(it)) {                                                                                   \
    constexpr int n_ = p1_xbase(it) + (e);                                                                              \
    if constexpr (n_ < 2) LD16(xres[n_], vx, xb2, n_ * 64);                                                             \
    else if constexpr (n_ < 10) LD16C(csr[n_ - 2], vc2, cb2 + ((n_ - 2) >> 2) * 4096, ((n_ - 2) & 3) * 256);            \
    else LD16CA(wd[(n_ - 10) >> 2][(n_ - 10) & 3], vl16, wb + ((n_ - 10) >> 2) * 4096, ((n_ - 10) & 3) * 1024);         \
  }
#define MM1(i, t, kb) acc1[i][t] = mfma(bf[U][2 * (kb) + (i)], af[C][4 * (kb) + (t)], acc1[i][t]); SCHED()
#define RD1(n) if constexpr (RN) { af[Nx][n] = *(const v4u*)(sn + ((n) < 4 ? ra0 : ra1) + ((n) & 3) * 2048); SCHED(); }
#define CK1(what) if constexpr (it + 1 < NV) { what; SCHED(); }
  static_for<0, 32>([&](auto IT) {
    constexpr int it = IT, U = it & 7, Un = (it + 1) & 7, C = it & 1, Nx = C ^ 1;
    constexpr bool RA = p1_ra(it), RB = it + DB < 32, RN = it + 1 < 32;
    if constexpr (it < NV) {
      if (__builtin_expect(bs != 0, 0)) {
        constexpr int hi0 = it + DB < NV ? it + DB : NV, hi = hi0 < (it / 8 + 1) * 8 ? hi0 : (it / 8 + 1) * 8;
        unsigned spin = SPIN_CAP;
        WAITV(0);
        do {
          COUNT(0);
#pragma unroll
          for (int g = it; g < hi0; ++g)
#pragma unroll
            for (int c = 0; c < 4; ++c) RLD16(bf[g & 7][c], vl16, BB(g) + g * 4096, c * 1024);
          RLD4(xt[it / 8], vl512, XCB(it / 8) + w * 4, 0);
          WAITV(0);
          unsigned bad = 0;
#pragma unroll
          for (int g = it; g < hi; ++g) { unsigned v_; CK_ALL(v_, xt[g / 8], XMASK(g), bf[g & 7]); bad |= v_; }
          if (bad == 0) spin = 1;
        } while (--spin);
      }
    }
    if constexpr (it == FL0_CHK) {
      asm volatile("s_waitcnt vmcnt(%1)" : "+v"(flw) : "i"(p1_wait(it, false)) : "memory");
      if (!__all(flw == tag)) { COUNT(1); poll_flags(ws + FLAG0_OFF, vfl, tag); }
    }
    if constexpr (p1_bar(it)) WAITV_BAR(p1_wait(it, false));
    if constexpr ((it & 7) == 7 && it + 1 < NV) XT_READ((it + 1) / 8);
    if constexpr (p1_xt(it)) { if (w == it / 8 + 1) GLDSC(vl512, XCB(it / 8 + 1), lds0 + XT_OFF + (it / 8 + 1) * 5952, 0); }
    if constexpr (it == FL0_PUB) { if (w == 0) ST_TAG(tag4, ws + FLAG0_OFF + bid * 128); }
    if constexpr (it == FL0_IT)
      asm volatile("s_cmp_lg_u32 %3, 0\n\ts_cbranch_scc1 1f\n\tglobal_load_dword %0, %1, %2 sc1\n1:"
                   : "+v"(flw) : "v"(vfl), "s"(ws + FLAG0_OFF), "s"(w) : "memory", "scc");
    const unsigned char* sn = smem + ((it + 1) & 15) * 8192;
    SCHED();
    MM1(0, 0, 0); RD1(0); if constexpr (RN) { WAIT4(bf[Un], p1_wait(it, true), ""); SCHED(); }
    MM1(1, 0, 0); if constexpr (RA) A_STAGE0(p1_as(it)); P1_EXTRA(0) SCHED();
    MM1(0, 1, 0); RD1(1); if constexpr (RA && RAN == 2) { A_STAGE0(p1_as(it) + 1); SCHED(); } CK1(CK_INIT(bx, xt[(it + 1) / 8], XMASK(it + 1)));
    MM1(1, 1, 0); RD1(2); if constexpr (RA && RAN == 2) { A_STAGE1(p1_as(it)); SCHED(); } CK1(ACC4(bx, bx, bf[Un][0]));
    MM1(0, 2, 0); if constexpr (RA) A_STAGE1(p1_as(it) + RAN - 1); P1_EXTRA(1) SCHED();
    MM1(1, 2, 0); RD1(3); CK1(ACC4(bx, bx, bf[Un][1]));
    MM1(0, 3, 0); if constexpr (RB) B_LOAD(it + DB, 0); P1_EXTRA(2) SCHED(); CK1(ACC4(bx, bx, bf[Un][2]));
    MM1(1, 3, 0); RD1(4); CK1(ACC4(bx, bx, bf[Un][3]));
    MM1(0, 0, 1); RD1(5); CK1(CK_RED(bx, 0));
    MM1(1, 0, 1); if constexpr (RB) B_LOAD(it + DB, 1); P1_EXTRA(3) SCHED(); CK1(CK_RED(bx, 1));
    MM1(0, 1, 1); RD1(6); CK1(CK_RED(bx, 2));
    MM1(1, 1, 1); if constexpr (RB) B_LOAD(it + DB, 2); P1_EXTRA(4) SCHED(); CK1(CK_RED(bx, 3));
    MM1(0, 2, 1); RD1(7); CK1(CK_RED(bx, 4));
    MM1(1, 2, 1); if constexpr (RB) B_LOAD(it + DB, 3); P1_EXTRA(5) SCHED(); CK1(CK_RED(bx, 5));
    MM1(0, 3, 1); CK1(CK_END(bs, bx));
    MM1(1, 3, 1);
  });
  {
    const int kbq = ti * 32 + 16 * mat + 4 * nt + w;
    unsigned char* pq = ws + PQ_OFF + kbq * 4096;
    unsigned ck = 0;
#pragma unroll
    for (int t = 0; t < 4; ++t) {
      const v4u o = {pk2(acc1[0][t][0], acc1[0][t][1]), pk2(acc1[0][t][2], acc1[0][t][3]),
                     pk2(acc1[1][t][0], acc1[1][t][1]), pk2(acc1[1][t][2], acc1[1][t][3])};
      ST16C(o, vl16, pq + t * 1024, 0);
      ck = sum4(ck, o);
    }
    ck = tag + last_lane(wave_isum(ck));
    const v4u c4 = {ck, ck, ck, ck};
    ST_TAG(c4, ws + CKP_OFF + kbq * 128);
    if (bid == 0 && w == 0) ST_TAG(tag4, ws + EPOCH_OFF);
  }
#pragma unroll
  for (int n = 32 - WDS; n < 32; ++n) LD16CA(wd[n >> 2][n & 3], vl16, wb + (n >> 2) * 4096, (n & 3) * 1024);
  cptr w1src = ws + W1_OFF + fs * 114688 + w * 28672;
  const unsigned w1dst = lds0 + w * 28672;
#define W1L(n) GLDSC(vl16, w1src + ((n) >> 2) * 4096, w1dst + ((n) >> 2) * 4096, ((n) & 3) * 1024)
#pragma unroll 1
  for (int n = 0; n < W1A / 4; ++n) {
    GLDSC(vl16, w1src + n * 4096, w1dst + n * 4096, 0);
    GLDSC(vl16, w1src + n * 4096, w1dst + n * 4096, 1024);
    GLDSC(vl16, w1src + n * 4096, w1dst + n * 4096, 2048);
    GLDSC(vl16, w1src + n * 4096, w1dst + n * 4096, 3072);
  }
  v4u csf[7];
  {
    static_assert(p1_xbase(26) >= 10, "the wait for the last XNT stage covers the column-sum loads");
    PIN4(csr[0], csr[1], csr[2], csr[3]);
    PIN4(csr[4], csr[5], csr[6], csr[7]);
    const bool odd = fs & 1;
#pragma unroll
    for (int l = 0; l < 7; ++l) {
      const int te = l, to = l < 6 ? l + 2 : 1;
#pragma unroll
      for (int i = 0; i < 4; ++i) csf[l][i] = odd ? csr[(to >> 2) * 4 + i][to & 3] : csr[(te >> 2) * 4 + i][te & 3];
      asm volatile("" : "+a"(csf[l]));
    }
  }
#define MM64(i, c) acc[i][c] = mfma(WT_[i], FC_[c], acc[i][c]); SCHED()
#define STEP64(WT, FC, FN, WAITN, T, MASK, OP)                                                                          \
  {                                                                                                                     \
    auto& WT_ = WT; auto& FC_ = FC;                                                                                     \
    SCHED();                                                                                                            \
    MM64(0, 0); WAITN; SCHED();                                                                                         \
    MM64(0, 1); OP(0); SCHED();                                                                                         \
    MM64(0, 2); CK_INIT(mx, T, MASK); SCHED();                                                                          \
    MM64(0, 3); ACC4(mx, mx, FN[0]); SCHED();                                                                           \
    MM64(1, 0); ACC4(mx, mx, FN[1]); SCHED();                                                                           \
    MM64(1, 1); OP(1); SCHED();                                                                                         \
    MM64(1, 2); ACC4(mx, mx, FN[2]); SCHED();                                                                           \
    MM64(1, 3); ACC4(mx, mx, FN[3]); SCHED();                                                                           \
    MM64(2, 0); CK_RED(mx, 0); SCHED();                                                                                 \
    MM64(2, 1); OP(2); CK_RED(mx, 1); SCHED();                                                                          \
    MM64(2, 2); CK_RED(mx, 2); SCHED();                                                                                 \
    MM64(2, 3); CK_RED(mx, 3); SCHED();                                                                                 \
    MM64(3, 0); CK_RED(mx, 4); SCHED();                                                                                 \
    MM64(3, 1); OP(3); CK_RED(mx, 5); SCHED();                                                                          \
    MM64(3, 2); CK_END(ms, mx); SCHED();                                                                                \
    MM64(3, 3);                                                                                                         \
  }
#define WAIT4T(F, T, N) asm volatile("s_waitcnt vmcnt(%5)" : "+v"(F[0]), "+v"(F[1]), "+v"(F[2]), "+v"(F[3]), "+v"(T) : "i"(N) : "memory")

  unsigned vrot[4];
#pragma unroll
  for (int c = 0; c < 4; ++c) vrot[c] = vl16 + (c ^ w) * 1024;
  v4u hres[2];
  v4u w2f[14][4];
  cptr w2base = ws + W2_OFF + cg * 229376 + w * 57344;
#define W2L(n) do { if ((n) < 32) LD16CA(w2f[(n) >> 2][(n) & 3], vl16, w2base + ((n) >> 2) * 4096, ((n) & 3) * 1024);   \
                    else LD16C(w2f[(n) >> 2][(n) & 3], vl16, w2base + ((n) >> 2) * 4096, ((n) & 3) * 1024); } while (0)
  {
    f32x4 acc[4][4];
    v4u pq[8][4];
    unsigned tw[2];
    cptr pb = ws + PQ_OFF + ti * 131072 + w * 32768;
    cptr cb = ws + CKP_OFF + (ti * 32 + 8 * w) * 128;
    const unsigned vt = (lane & 7) * 128;
    if (GATE & 1) WAITV(WDS + W1A);
#pragma unroll
    for (int k = 0; k < D2; ++k) {
#pragma unroll
      for (int c = 0; c < 4; ++c) LD16C(pq[k][c], vrot[c], pb + k * 4096 + WRONG, 0);
      if (k == 0) LD4C(tw[0], vt, cb, 0);
    }
    asm volatile("s_waitcnt vmcnt(%2)" : "+v"(xres[0]), "+v"(xres[1]) : "i"(4 * D2 + 1) : "memory");
#pragma unroll
    for (int i = 0; i < 4; ++i) {
      acc[i][0] = unpk(xres[i >> 1][2 * (i & 1)], xres[i >> 1][2 * (i & 1) + 1]);
#pragma unroll
      for (int c = 1; c < 4; ++c) acc[i][c] = f32x4{0.f, 0.f, 0.f, 0.f};
    }
    WAIT4T(pq[0], tw[0], 4 * D2 - 4);
    unsigned mx, ms;
    CK_ALL(ms, tw[0], 1ull, pq[0]);
#define P2OP(i) do { if constexpr (k == 0 && (i) == 0) LD4C(tw[1], vt, cb, 0);                                           \
                     if constexpr (k + D2 < 8) LD16C(pq[(k + D2) & 7][i], vrot[i], pb + (k + D2) * 4096 + WRONG, 0);     \
                     else if constexpr ((i) < p2_w1(k)) W1L(W1A + 4 * (k + D2 - 8) + (i)); } while (0)
    static_for<0, 8>([&](auto K_) {
      constexpr int k = K_, kn = (k + 1) & 7;
      if (__builtin_expect(ms != 0, 0)) {
        constexpr int hi = k + D2 < 8 ? k + D2 : 8;
        unsigned spin = SPIN_CAP;
        WAITV(0);
        do {
          COUNT(1);
#pragma unroll
          for (int g = k; g < hi; ++g)
#pragma unroll
            for (int c = 0; c < 4; ++c) RLD16(pq[g][c], vrot[c], pb + g * 4096, 0);
          if constexpr (k < D2) RLD4(tw[0], vt, cb, 0);
          if constexpr (k > 0) RLD4(tw[1], vt, cb, 0);
          WAITV(0);
          unsigned bad = 0;
#pragma unroll
          for (int g = k; g < hi; ++g) { unsigned v_; CK_ALL(v_, tw[g >= D2], 1ull << g, pq[g]); bad |= v_; }
          if (bad == 0) spin = 1;
        } while (--spin);
      }
      PIN4A(wd[k][0], wd[k][1], wd[k][2], wd[k][3]);
      if constexpr (k < 7) STEP64(wd[k], pq[k], pq[kn], WAIT4T(pq[kn], tw[kn >= D2], p2_wait(kn)), tw[kn >= D2], 1ull << kn, P2OP);
    });
    last_step_reduce(smem + RED_OFF, w, lane, wd[7], pq[7], acc, hres, [&]() {
#pragma unroll
      for (int n = W1A + 4 * (D2 - 1); n < 28 - W1C; ++n) W1L(n);
    });
    const int hp = (4 * ti + w) * 8 + cg;
    ST16C(hres[0], vl16, ws + H_OFF + hp * 2048, 0);
    ST16C(hres[1], vl16, ws + H_OFF + hp * 2048, 1024);
    const unsigned hck = tag + last_lane(wave_isum(sum4(sum4(0u, hres[0]), hres[1])));
    const v4u c4 = {hck, hck, hck, hck};
    ST_TAG(c4, ws + CKH_OFF + hp * 128);
    f32x4 hv[4];
#pragma unroll
    for (int i = 0; i < 4; ++i) hv[i] = unpk(hres[i >> 1][2 * (i & 1)], hres[i >> 1][2 * (i & 1) + 1]);
    const f32x4 s4 = (hv[0] + hv[1]) + (hv[2] + hv[3]);
    const float m = rows_sum((s4[0] + s4[1]) + (s4[2] + s4[3])) * (1.0f / 64.0f);
    f32x4 q4 = {0.f, 0.f, 0.f, 0.f};
#pragma unroll
    for (int i = 0; i < 4; ++i) { const f32x4 c = hv[i] - m; q4 += c * c; }
    const float m2 = rows_sum((q4[0] + q4[1]) + (q4[2] + q4[3]));
    const unsigned mu = __builtin_bit_cast(unsigned, m), m2u = __builtin_bit_cast(unsigned, m2);
    const v4u st = {mu, m2u, tag + mu + m2u, 0u};
    asm volatile("s_mov_b64 exec, 0xffff\n\tglobal_store_dwordx4 %0, %1, %2 sc1\n\ts_mov_b64 exec, -1\n\ts_nop 0" ::"v"(vl16), "v"(st), "s"(ws + ST_OFF + hp * 256) : "memory");
  }
#pragma unroll
  for (int n = 28 - W1C; n < 28; ++n) W1L(n);
#pragma unroll
  for (int n = 0; n < W2A; ++n) W2L(n);
  {
    v4u hf[8][4];
    unsigned tw[2];
    v4u str[2][2];
    cptr hb = ws + H_OFF + (8 * t128 + 2 * w) * 16384;
    cptr cb = ws + CKH_OFF + (8 * t128 + 2 * w) * 1024;
    const unsigned vt = r * 128;
    const unsigned vh1 = vl16 + 16384;
    const unsigned vs = q * 512 + r * 16;
    cptr stb = ws + ST_OFF + (8 * t128 + 2 * w) * 2048;
#define HL(m, i) LD16C(hf[m][i], ((i) & 1) ? vh1 : vl16, hb + ((m) >> 1) * 4096 + WRONG, ((m) & 1) * 2048 + ((i) >> 1) * 1024)
#define HMASK(m) (0x101ull << (m))
    if (GATE & 2) WAITV(W1C + W2A + 1);
#pragma unroll
    for (int m = 0; m < 4; ++m) { HL(m, 0); HL(m, 1); HL(m, 2); HL(m, 3); if (m == 0) LD4C(tw[0], vt, cb, 0); }
    f32x4 acc[7][2];
#pragma unroll
    for (int l = 0; l < 7; ++l) { acc[l][0] = f32x4{0.f, 0.f, 0.f, 0.f}; acc[l][1] = f32x4{0.f, 0.f, 0.f, 0.f}; }
    WAIT4T(hf[0], tw[0], 12);
    BAR();
    unsigned whi_ = lds0 + vl16 + 57344;
    asm volatile("" : "+v"(whi_));
    const LAS unsigned char* wlo = (const LAS unsigned char*)smem + vl16;
    const LAS unsigned char* whi = (const LAS unsigned char*)(size_t)whi_;
#define W1F(k, l) (*(const LAS v4u*)(((k) < 8 ? wlo + (k) * 7168 : whi + ((k) - 8) * 7168) + (l) * 1024))
    v4u wf[2][7];
#pragma unroll
    for (int l = 0; l < 7; ++l) wf[0][l] = W1F(0, l);
    unsigned mx, ms;
    CK_ALL(ms, tw[0], HMASK(0), hf[0]);
    float rd[2], sm[2], sq[2];
    unsigned nmf[2];
    auto stat_piece = [&](auto N_) {
      constexpr int n = N_ >> 1, t = N_ & 1;
      if constexpr (n == 0) sm[t] = swap16_sum(asf(str[t][0][0]) + asf(str[t][1][0]));
      if constexpr (n == 1) sm[t] = swap32_sum(sm[t]) * 0.125f;
      if constexpr (n == 2) { const float da = asf(str[t][0][0]) - sm[t], db = asf(str[t][1][0]) - sm[t]; sq[t] = da * da + db * db; }
      if constexpr (n == 3) sq[t] = asf(str[t][0][1]) + asf(str[t][1][1]) + 64.0f * sq[t];
      if constexpr (n == 4) sq[t] = swap16_sum(sq[t]);
      if constexpr (n == 5) sq[t] = swap32_sum(sq[t]) * (1.0f / D_);
      if constexpr (n == 6) nmf[t] = pk2(-sm[t], -sm[t]);
      if constexpr (n == 7) sq[t] = rbf(sq[t]) + 1e-5f;
      if constexpr (n == 8) sq[t] = __builtin_amdgcn_sqrtf(rbf(sq[t]));
      if constexpr (n == 9) rd[t] = __builtin_amdgcn_rcpf(rbf(sq[t]));
      if constexpr (n < 2) asm volatile("" : "+v"(sm[t]));
      else if constexpr (n == 6) asm volatile("" : "+v"(nmf[t]));
      else if constexpr (n == 9) asm volatile("" : "+v"(rd[t]));
      else if constexpr (n < 9) asm volatile("" : "+v"(sq[t]));
    };
#define P3OP(i) do { if constexpr (k == 0 && (i) == 0) LD4C(tw[1], vt, cb, 0);                                           \
                     if constexpr (k < 4) HL((4 + k) & 7, i);                                                           \
                     else if constexpr (k == 4) LD16C(str[(i) >> 1][(i) & 1], vs, stb + WRONG, ((i) >> 1) * 2048 + ((i) & 1) * 256); \
                     else if constexpr ((i) < p3_w2(k)) W2L(W2A + 4 * (k - 5) + (i)); } while (0)
    static_for<0, 16>([&](auto K_) {
      constexpr int k = K_, m = k >> 1, mn = ((k + 1) >> 1) & 7;
      if ((k & 1) == 0 && __builtin_expect(ms != 0, 0)) {
        constexpr int hi = 4 + k < 8 ? 4 + k : 8;
        unsigned spin = SPIN_CAP;
        WAITV(0);
        do {
          COUNT(2);
#pragma unroll
          for (int g = m; g < hi; ++g)
#pragma unroll
            for (int c = 0; c < 4; ++c) RLD16(hf[g][c], (c & 1) ? vh1 : vl16, hb + g * 2048, (c >> 1) * 1024);
          if constexpr (m < 4) RLD4(tw[0], vt, cb, 0);
          if constexpr (m > 0) RLD4(tw[1], vt, cb, 0);
          WAITV(0);
          unsigned bad = 0;
#pragma unroll
          for (int g = m; g < hi; ++g) { unsigned v_; CK_ALL(v_, tw[g >= 4], HMASK(g), hf[g]); bad |= v_; }
          if (bad == 0) spin = 1;
        } while (--spin);
      }
      if constexpr (k == P3_ST) {
        asm volatile("s_waitcnt vmcnt(%4)" : "+v"(str[0][0]), "+v"(str[0][1]), "+v"(str[1][0]), "+v"(str[1][1]) : "i"(p3_stwait()) : "memory");
#define STCK(a) (tag + (a)[0] + (a)[1] - (a)[2])
        if (__builtin_expect(BAD(STCK(str[0][0]) | STCK(str[0][1]) | STCK(str[1][0]) | STCK(str[1][1])), 0)) {
          unsigned spin = SPIN_CAP;
          WAITV(0);
          do {
            asm volatile("global_load_dwordx4 %0, %4, %5 sc1\n\tglobal_load_dwordx4 %1, %4, %5 offset:256 sc1\n\t"
                         "global_load_dwordx4 %2, %4, %5 offset:2048 sc1\n\tglobal_load_dwordx4 %3, %4, %5 offset:2304 sc1\n\ts_waitcnt vmcnt(0)"
                         : "=&v"(str[0][0]), "=&v"(str[0][1]), "=&v"(str[1][0]), "=&v"(str[1][1]) : "v"(vs), "s"(stb) : "memory");
            if (!BAD(STCK(str[0][0]) | STCK(str[0][1]) | STCK(str[1][0]) | STCK(str[1][1]))) spin = 1;
          } while (--spin);
        }
      }
      SCHED();
      static_for<0, 7>([&](auto L_) {
        constexpr int l = L_;
        constexpr bool CHK = (k & 1) && k + 1 < 16;
        acc[l][0] = mfma(wf[k & 1][l], hf[m][2 * (k & 1)], acc[l][0]);
        SCHED();
        if constexpr (k + 1 < 16) wf[(k + 1) & 1][l] = W1F(k + 1, l);
        if constexpr (CHK) {
          if constexpr (l == 0) WAIT4T(hf[mn], tw[mn >= 4], p3_wait(mn));
          if constexpr (l == 1) ACC4(mx, mx, hf[mn][0]);
          if constexpr (l == 2) ACC4(mx, mx, hf[mn][2]);
          if constexpr (l >= 3 && l < 6) CK_RED(mx, 2 * (l - 3));
          if constexpr (l == 6) CK_END(ms, mx);
        }
        if constexpr (k >= P3_ST && !(k & 1)) stat_piece(std::integral_constant<int, 7 * ((k - P3_ST) / 2) + l>{});
        SCHED();
        acc[l][1] = mfma(wf[k & 1][l], hf[m][2 * (k & 1) + 1], acc[l][1]);
        SCHED();
        if constexpr (l == 0 || l == 2 || l == 4 || l == 5) P3OP(l == 5 ? 3 : l / 2);
        if constexpr (CHK) {
          if constexpr (l == 0) CK_INIT(mx, tw[mn >= 4], HMASK(mn));
          if constexpr (l == 1) ACC4(mx, mx, hf[mn][1]);
          if constexpr (l == 2) ACC4(mx, mx, hf[mn][3]);
          if constexpr (l >= 3 && l < 6) CK_RED(mx, 2 * (l - 3) + 1);
        }
        SCHED();
      });
    });
    const int kbp = (7 * fs + (fs & 1)) >> 1, taus = 7 * fs + ((fs & 1) ? 0 : 6);
    const int t64 = 2 * t128 + (w >> 1);
    unsigned char* fb = ws + FF_OFF + t64 * 229376 + kbp * 4096 + 2 * (w & 1) * 1024;
    v4u fck = {0u, 0u, 0u, 0u};
    {
      const v4u n0 = {nmf[0], nmf[0], nmf[0], nmf[0]}, n1 = {nmf[1], nmf[1], nmf[1], nmf[1]};
#pragma unroll
      for (int l = 0; l < 7; ++l) {
        acc[l][0] = mfma(csf[l], n0, acc[l][0]);
        acc[l][1] = mfma(csf[l], n1, acc[l][1]);
      }
    }
    auto act = [&](int l, int t) {
      const f32x4 v = acc[l][t] * rd[t];
      return v2u{relu2(pk2(v[0], v[1])), relu2(pk2(v[2], v[3]))};
    };
#pragma unroll
    for (int m_ = 0; m_ < 3; ++m_)
#pragma unroll
      for (int t = 0; t < 2; ++t) {
        const v2u a = act(2 * m_, t), b = act(2 * m_ + 1, t);
        const v4u oo = {a[0], a[1], b[0], b[1]};
        if (t == 0) ST16C(oo, vl16, fb + m_ * 4096, 0); else ST16C(oo, vl16, fb + m_ * 4096, 1024);
        fck[m_] = sum4(fck[m_], oo);
      }
    const v2u a6 = act(6, 0), b6 = act(6, 1);
    const v4u o6 = {a6[0], a6[1], b6[0], b6[1]};
    ST16C(o6, vl16, ws + FF_OFF + t64 * 229376 + (taus >> 1) * 4096 + (2 * (taus & 1) + (w & 1)) * 1024, 0);
    fck[3] = sum4(fck[3], o6);
#pragma unroll
    for (int i = 0; i < 4; ++i) fck[i] = tag + last_lane(wave_isum(fck[i]));
    ST_TAG(fck, ws + CKF_OFF + ((t64 * 16 + fs) * 2 + (w & 1)) * 128);
  }
#pragma unroll
  for (int n = 56 - W2C; n < 56; ++n) W2L(n);

  {
    f32x4 acc[4][4];
    constexpr int R = D4 + 1;
    v4u ff[R][4];
    unsigned tw[2];
    cptr fb = ws + FF_OFF + ti * 229376 + w * 57344;
    cptr cb = ws + CKF_OFF + (ti * 16 + 4 * w) * 256;
    const unsigned vt = ((lane >> 2) & 7) * 128 + (lane & 3) * 4;
    unsigned vsh[4];
#pragma unroll
    for (int c = 0; c < 4; ++c) vsh[c] = vl16 + (c ^ (w >> 1)) * 1024;
#define VR4(g, c) ((g) % 7 == 3 ? vsh[c] : vrot[c])
#define FMASK(g) (((g) % 7 == 3 ? 0x8888ull : (g) % 7 < 3 ? 0x11ull << ((g) % 7) : 0x1100ull << (((g) % 7) & 3)) << (16 * ((g) / 7)))
    if (GATE & 4) WAITV(W2C);
#pragma unroll
    for (int k = 0; k < D4; ++k) {
#pragma unroll
      for (int c = 0; c < 4; ++c) LD16C(ff[k][c], VR4(k, c), fb + k * 4096 + WRONG, 0);
      if (k == 0) LD4C(tw[0], vt, cb, 0);
    }
#pragma unroll
    for (int i = 0; i < 4; ++i) {
      acc[i][0] = unpk(hres[i >> 1][2 * (i & 1)], hres[i >> 1][2 * (i & 1) + 1]);
#pragma unroll
      for (int c = 1; c < 4; ++c) acc[i][c] = f32x4{0.f, 0.f, 0.f, 0.f};
    }
    WAIT4T(ff[0], tw[0], 4 * D4 - 4);
    unsigned mx, ms;
    CK_ALL(ms, tw[0], FMASK(0), ff[0]);
#define P4OP(i) do { if constexpr (k == 0 && (i) == 0) LD4C(tw[1], vt, cb, 0);                                           \
                     if constexpr (k + D4 < 14) LD16C(ff[(k + D4) % R][i], VR4(k + D4, i), fb + (k + D4) * 4096 + WRONG, 0); } while (0)
    static_for<0, 14>([&](auto K_) {
      constexpr int k = K_, kn = (k + 1) % 14;
      if (__builtin_expect(ms != 0, 0)) {
        constexpr int hi = k + D4 < 14 ? k + D4 : 14;
        unsigned spin = SPIN_CAP;
        WAITV(0);
        do {
          COUNT(3);
          if constexpr (k == 13) asm volatile(".Lfnet_last:");
#pragma unroll
          for (int g = k; g < hi; ++g)
#pragma unroll
            for (int c = 0; c < 4; ++c) RLD16(ff[g % R][c], VR4(g, c), fb + g * 4096, 0);
          if constexpr (k < D4) RLD4(tw[0], vt, cb, 0);
          if constexpr (k > 0) RLD4(tw[1], vt, cb, 0);
          WAITV(0);
          unsigned bad = 0;
#pragma unroll
          for (int g = k; g < hi; ++g) { unsigned v_; CK_ALL(v_, tw[g >= D4], FMASK(g), ff[g % R]); bad |= v_; }
          if (bad == 0) spin = 1;
        } while (--spin);
      }
      if (k % 7 == 3) {
        v4u* F = ff[k % R];
        const bool w0 = w & 1;
        const v4u e0 = {F[0][0], F[0][1], F[2][0], F[2][1]}, o0 = {F[0][2], F[0][3], F[2][2], F[2][3]};
        const v4u e1 = {F[1][0], F[1][1], F[3][0], F[3][1]}, o1 = {F[1][2], F[1][3], F[3][2], F[3][3]};
        F[0] = w0 ? o0 : e0; F[1] = w0 ? e0 : o0; F[2] = w0 ? o1 : e1; F[3] = w0 ? e1 : o1;
      }
      if (k < 8) PIN4A(w2f[k][0], w2f[k][1], w2f[k][2], w2f[k][3]);
      else PIN4(w2f[k][0], w2f[k][1], w2f[k][2], w2f[k][3]);
      if constexpr (k < 13) STEP64(w2f[k], ff[k % R], ff[kn % R], WAIT4T(ff[kn % R], tw[kn >= D4], p4_wait(kn)), tw[kn >= D4], FMASK(kn), P4OP);
    });
    v4u o[2];
    last_step_reduce(smem + RED_OFF, w, lane, w2f[13], ff[13 % R], acc, o, []() {});
    unsigned char* ob_ = out + (size_t)(64 * ti + 16 * w) * 1024 + cg * 128;
    ST16(o[0], vx, ob_, 0);
    ST16(o[1], vx, ob_, 64);
  }
}

extern "C" void kernel_launch(void* const* d_in, const int* in_sizes, int n_in, void* d_out, int out_size, void* d_ws,
                              size_t ws_size, hipStream_t stream) {
  (void)hipFuncSetAttribute((const void*)fnet_fused, hipFuncAttributeMaxDynamicSharedMemorySize, LDS_BYTES);
  fnet_fused<<<256, 256, LDS_BYTES, stream>>>((cptr)d_in[0], (cptr)d_in[1], (cptr)d_in[2], (cptr)d_in[3], (cptr)d_in[4],
                                              (cptr)d_in[6], (cptr)d_in[7], (unsigned char*)d_ws, (unsigned char*)d_out);
}
```

```cpp
#include <hip/hip_runtime.h>
#include <hip/hip_bf16.h>
#include <stdint.h>
#include <type_traits>

typedef unsigned v4u __attribute__((ext_vector_type(4)));
typedef unsigned v2u __attribute__((ext_vector_type(2)));
typedef float f32x4 __attribute__((ext_vector_type(4)));
typedef float f32x2 __attribute__((ext_vector_type(2)));
typedef short bf16x8 __attribute__((ext_vector_type(8)));
typedef short v4s __attribute__((ext_vector_type(4)));
typedef __bf16 bf2_t __attribute__((ext_vector_type(2)));
typedef const unsigned char* cptr;
#define LAS __attribute__((address_space(3)))

constexpr int S_ = 2048, D_ = 512, F_ = 1792;
constexpr unsigned EPOCH_OFF = 0;
constexpr unsigned FLAG0_OFF = 32768;
constexpr unsigned XNT_OFF = 131072;
constexpr unsigned WD_OFF  = XNT_OFF + (2u << 20);
constexpr unsigned W1_OFF  = WD_OFF + (1u << 20);
constexpr unsigned W2_OFF  = W1_OFF + 1835008u;
constexpr unsigned CS_OFF  = W2_OFF + 1835008u;
constexpr unsigned PQ_OFF  = CS_OFF + 131072;
constexpr unsigned H_OFF   = PQ_OFF + (4u << 20);
constexpr unsigned FF_OFF  = H_OFF + (2u << 20);
constexpr unsigned ST_OFF  = FF_OFF + 7340032u;
constexpr unsigned CKP_OFF = ST_OFF + 262144;
constexpr unsigned CKH_OFF = CKP_OFF + 131072;
constexpr unsigned CKF_OFF = CKH_OFF + 131072;
constexpr unsigned CKX_OFF = CKF_OFF + 131072;

constexpr int XS_ROW = 1088;
constexpr int XS_OFF = 131072, WST_OFF = XS_OFF + 8 * XS_ROW;
constexpr int XT_OFF = WST_OFF + 4352;
constexpr int RED_OFF = 114688;
constexpr int LDS_BYTES = 163840;
constexpr unsigned SPIN_CAP = 1u << 20;

__device__ __forceinline__ unsigned pk2(float a, float b) {
  f32x2 v = {a, b};
  return __builtin_bit_cast(unsigned, __builtin_convertvector(v, bf2_t));
}
__device__ __forceinline__ float asf(unsigned u) { return __builtin_bit_cast(float, u); }
__device__ __forceinline__ float bflo(unsigned u) { return __builtin_bit_cast(float, u << 16); }
__device__ __forceinline__ float bfhi(unsigned u) { return __builtin_bit_cast(float, u & 0xffff0000u); }
__device__ __forceinline__ float rbf(float x) { return (float)(__bf16)x; }
typedef short v2s __attribute__((ext_vector_type(2)));
__device__ __forceinline__ unsigned relu2(unsigned u) {
  return __builtin_bit_cast(unsigned, __builtin_elementwise_max(__builtin_bit_cast(v2s, u), v2s{0, 0}));
}
__device__ __forceinline__ f32x4 unpk(unsigned a, unsigned b) { return f32x4{bflo(a), bfhi(a), bflo(b), bfhi(b)}; }
__device__ __forceinline__ float ln_rstd(float var) {
  return __builtin_amdgcn_rcpf(rbf(__builtin_amdgcn_sqrtf(rbf(rbf(var) + 1e-5f))));
}
__device__ __forceinline__ f32x4 mfma(v4u a, v4u b, f32x4 c) {
  return __builtin_amdgcn_mfma_f32_16x16x32_bf16(__builtin_bit_cast(bf16x8, a), __builtin_bit_cast(bf16x8, b), c, 0, 0, 0);
}
template <int CTRL> __device__ __forceinline__ float dpp_add(float x) {
  return x + __builtin_bit_cast(float, __builtin_amdgcn_update_dpp(0, __builtin_bit_cast(int, x), CTRL, 0xf, 0xf, true));
}
__device__ __forceinline__ float row16_sum(float x) {
  return dpp_add<0x121>(dpp_add<0x122>(dpp_add<0x124>(dpp_add<0x128>(x))));
}
__device__ __forceinline__ float swap16_sum(float x) {
  unsigned a = __builtin_bit_cast(unsigned, x), b = a;
  asm("" : "+v"(b));
  auto r = __builtin_amdgcn_permlane16_swap(a, b, false, false);
  return asf(r[0]) + asf(r[1]);
}
__device__ __forceinline__ float swap32_sum(float x) {
  unsigned a = __builtin_bit_cast(unsigned, x), b = a;
  asm("" : "+v"(b));
  auto r = __builtin_amdgcn_permlane32_swap(a, b, false, false);
  return asf(r[0]) + asf(r[1]);
}
__device__ __forceinline__ float rows_sum(float x) { return swap32_sum(swap16_sum(x)); }
__device__ __forceinline__ float half_sum(float x) { return swap16_sum(row16_sum(x)); }
#define SCHED() __builtin_amdgcn_sched_barrier(0)
#define WAITV(n) asm volatile("s_waitcnt vmcnt(%0)" ::"i"(n) : "memory")
#define WAITV_BAR(n) asm volatile("s_waitcnt vmcnt(%0) lgkmcnt(0)\n\ts_barrier" ::"i"(n) : "memory")
#define BAR() asm volatile("s_waitcnt lgkmcnt(0)\n\ts_barrier" ::: "memory")
#define ACC4(m, m0, v) do { m = sum4(m0, v); asm volatile("" : "+v"(m)); } while (0)
#define PIN4(a, b, c, d) asm volatile("" : "+v"(a), "+v"(b), "+v"(c), "+v"(d))
#define PIN4A(a, b, c, d) asm volatile("" : "+a"(a), "+a"(b), "+a"(c), "+a"(d))

#define LD16(d, vo, sb, imm) asm volatile("global_load_dwordx4 %0, %1, %2 offset:%3" : "=v"(d) : "v"(vo), "s"(sb), "i"(imm) : "memory")
#define LD16C(d, vo, sb, imm) asm volatile("global_load_dwordx4 %0, %1, %2 offset:%3 sc1" : "=v"(d) : "v"(vo), "s"(sb), "i"(imm) : "memory")
#define LD16CA(d, vo, sb, imm) asm volatile("global_load_dwordx4 %0, %1, %2 offset:%3 sc1" : "=a"(d) : "v"(vo), "s"(sb), "i"(imm) : "memory")
#define LD4C(d, vo, sb, imm) asm volatile("global_load_dword %0, %1, %2 offset:%3 sc1" : "=v"(d) : "v"(vo), "s"(sb), "i"(imm) : "memory")
#define ST16C(v, vo, sb, imm) asm volatile("global_store_dwordx4 %0, %1, %2 offset:%3 sc1\n\ts_nop 1" ::"v"(vo), "v"(v), "s"(sb), "i"(imm) : "memory")
#define ST16(v, vo, sb, imm) asm volatile("global_store_dwordx4 %0, %1, %2 offset:%3\n\ts_nop 1" ::"v"(vo), "v"(v), "s"(sb), "i"(imm) : "memory")
#define ST4C(v, vo, sb, imm) asm volatile("global_store_dword %0, %1, %2 offset:%3 sc1" ::"v"(vo), "v"(v), "s"(sb), "i"(imm) : "memory")
#define RLD16(d, vo, sb, imm) asm volatile("global_load_dwordx4 %0, %1, %2 offset:%3 sc1" ::"v"(d), "v"(vo), "s"(sb), "i"(imm) : "memory")
#define RLD4(d, vo, sb, imm) asm volatile("global_load_dword %0, %1, %2 offset:%3 sc1" ::"v"(d), "v"(vo), "s"(sb), "i"(imm) : "memory")
#define CK_INIT(a, T, MASK) asm volatile("v_sub_u32 %0, %2, %1\n\tv_cndmask_b32 %0, 0, %0, %3" : "=&v"(a) : "v"(T), "s"(tag), "s"((unsigned long long)(MASK)))
#define CK_RED(a, i) do { a = isum<i>(a); asm volatile("" : "+v"(a)); } while (0)
#define CK_END(sv, a) do { sv = last_lane(a); asm volatile("" : "+s"(sv)); } while (0)
#define VADD4(a, f) asm volatile("v_add3_u32 %0, %0, %1, %2\n\tv_add3_u32 %0, %0, %3, %4" : "+v"(a) : "v"((f)[0]), "v"((f)[1]), "v"((f)[2]), "v"((f)[3]))
#define CK_ALL(sv, T, MASK, F) do { unsigned a_; CK_INIT(a_, T, MASK); VADD4(a_, F[0]); VADD4(a_, F[1]); VADD4(a_, F[2]); VADD4(a_, F[3]);   \
                                    asm volatile("s_nop 1" : "+v"(a_)); sv = last_lane(wave_isum(a_)); } while (0)
#define WRONG 0
#define TOUCH(vo, sb) asm volatile("global_load_dword %0, %1, %2" : "+v"(sink) : "v"(vo), "s"(sb) : "memory")
#define ST_TAG(v, p) asm volatile("s_mov_b64 exec, 0xff\n\tglobal_store_dwordx4 %0, %1, %2 sc1\n\ts_mov_b64 exec, -1\n\ts_nop 0" ::"v"(vl16), "v"(v), "s"(p) : "memory")
#define GLDS(vo, sb, lds, imm) asm volatile("s_mov_b32 m0, %2\n\ts_nop 0\n\tglobal_load_lds_dwordx4 %0, %1 offset:%3" ::"v"(vo), "s"(sb), "s"(lds), "i"(imm) : "memory")
#define GLDSC(vo, sb, lds, imm) asm volatile("s_mov_b32 m0, %2\n\ts_nop 0\n\tglobal_load_lds_dwordx4 %0, %1 offset:%3 sc1" ::"v"(vo), "s"(sb), "s"(lds), "i"(imm) : "memory")

__device__ __forceinline__ unsigned sum4(unsigned m, v4u v) { return (m + v[0] + v[1]) + v[2] + v[3]; }
template <int I> __device__ __forceinline__ unsigned isum(unsigned x) {
  constexpr int ctrl = I < 4 ? 0x110 + (1 << I) : 0x142 + (I - 4), rm = I < 4 ? 0xf : I == 4 ? 0xa : 0xc;
  return x + (unsigned)__builtin_amdgcn_update_dpp(0, (int)x, ctrl, rm, 0xf, I < 4);
}
__device__ __forceinline__ unsigned row_isum(unsigned x) { return isum<3>(isum<2>(isum<1>(isum<0>(x)))); }
__device__ __forceinline__ unsigned wave_isum(unsigned x) { return isum<5>(isum<4>(row_isum(x))); }
__device__ __forceinline__ unsigned last_lane(unsigned x) { return (unsigned)__builtin_amdgcn_readlane((int)x, 63); }
__device__ __forceinline__ void poll_flags(cptr base, unsigned vo, unsigned tag) {
  for (unsigned spin = 0; spin < SPIN_CAP; ++spin) {
    unsigned v;
    asm volatile("global_load_dword %0, %1, %2 sc1\n\ts_waitcnt vmcnt(0)" : "=&v"(v) : "v"(vo), "s"(base) : "memory");
    if (__all(v == tag)) break;
  }
}
template <class H>
__device__ __forceinline__ void last_step_reduce(unsigned char* red, int w, int lane, const v4u (&wt)[4], const v4u (&fc)[4],
                                                 f32x4 (&acc)[4][4], v4u (&o)[2], H&& hook) {
#pragma unroll
  for (int c = 1; c <= 4; ++c) {
#pragma unroll
    for (int i = 0; i < 4; ++i) {
      acc[i][c & 3] = mfma(wt[i], fc[c & 3], acc[i][c & 3]);
      SCHED();
      if (c >= 2) *(f32x4*)(red + (((c - 1) ^ w) * 3 + c - 2) * 4096 + lane * 16 + i * 1024) = acc[i][c - 1];
      SCHED();
    }
  }
  hook();
  BAR();
  const unsigned char* s = red + w * 12288 + lane * 16;
  f32x4 pt[3][4];
#pragma unroll
  for (int i = 0; i < 4; ++i)
#pragma unroll
    for (int c = 0; c < 3; ++c) pt[c][i] = *(const f32x4*)(s + c * 4096 + i * 1024);
  SCHED();
#pragma unroll
  for (int i = 0; i < 4; ++i) {
    const f32x4 y = (acc[i][0] + pt[0][i]) + (pt[1][i] + pt[2][i]);
    o[i >> 1][2 * (i & 1)] = pk2(y[0], y[1]);
    o[i >> 1][2 * (i & 1) + 1] = pk2(y[2], y[3]);
  }
}

#define NP0 8
#define FL0_CHK 19
#define W1A 0
#define W1C 8
#define GATE 7
#define W2A 4
#define W2C 12
#define WDS 12
#define XT0 20
#define DB 5
#define XTP 1
#define XTD 3
#define BP 4
#define RAN 1
#define D2 4
#define D4 5
#define FL0_PUB 11
constexpr int FL0_IT = FL0_CHK - 3, NV = 32, NX = 42 - WDS;

template <int I, int N, class F> __device__ __forceinline__ void static_for(F&& f) {
  if constexpr (I < N) { f(std::integral_constant<int, I>{}); static_for<I + 1, N>(f); }
}
constexpr bool p1_xt(int s) { return (s & 7) == 7 - XTD && s + XTD + 1 < NV; }
constexpr bool p1_bar(int s) { return (s + 1) % BP == 0; }
constexpr bool p1_ra(int s) { return s >= BP - 1 && s < 15 + BP && (s + 1) % BP < BP / RAN; }
constexpr int p1_as(int s) { return 16 + ((s + 1) / BP - 1) * BP + ((s + 1) % BP) * RAN; }
constexpr int p1_xbase(int s) { int n = 0; for (int u = XT0; u < s; ++u) n += p1_ra(u) ? 0 : (u + DB < 32 ? 2 : 6); return n > NX ? NX : n; }
constexpr int p1_extra(int s) { return s < XT0 ? 0 : p1_xbase(s + 1) - p1_xbase(s); }
static_assert(p1_xbase(32) == NX && XT0 > FL0_CHK && p1_bar(FL0_CHK) && p1_bar(FL0_PUB) && p1_bar(7) && XTP < DB && DB <= 7 && RAN <= 2, "");
constexpr int p1_wait(int it, bool mid) {
  int posA[48] = {}, posB[40] = {}, posF = -1, n = 0, pos0 = 4 * DB, posT = -1;
  for (int s = 0; s < 48; ++s) posA[s] = -1;
  for (int s = 0; s < DB; ++s) { n += 4; posB[s] = n - 1; }
  if (it < 0) return n - 1 - posB[XTP];
  for (int s = 0; s <= it; ++s) {
    if (s == FL0_IT && s < it) posF = n - 1;
    if (p1_xt(s) && s < it) posT = n - 1;
    if (s == it) break;
    if (p1_ra(s)) for (int u = 0; u < RAN; ++u) { n += 2; posA[p1_as(s) + u] = n - 1; }
    n += p1_extra(s);
    if (s + DB < 32) { n += 4; posB[s + DB] = n - 1; }
  }
  int req = -1;
  if (mid) req = posB[it + 1];
  else {
    if (it + BP < 32) req = posA[it + BP];
    if (it == FL0_CHK && posF > req) req = posF;
    if (it == FL0_PUB && pos0 > req) req = pos0;
    if ((it & 7) == 7 && it + 1 < NV && posT > req) req = posT;
  }
  return n - 1 - req > 63 ? 63 : n - 1 - req;
}
constexpr int p2_w1(int s) { int n = 28 - W1A - 4 * (s - (8 - D2)); return (s + D2 < 8 || s == 7) ? 0 : n > 4 ? 4 : n < 0 ? 0 : n; }
constexpr int pk_wait(int k, int D, int NG) {
  int n = (k < D && k >= 2) ? 1 : 0;
  for (int g = k + 1; g < NG && g < k - 1 + D; ++g) n += 4;
  return n;
}
constexpr int p2_wait(int k) { int n = pk_wait(k, D2, 8); for (int s = 0; s < k - 1; ++s) n += p2_w1(s); return n; }
constexpr int p4_wait(int k) { return pk_wait(k, D4, 14); }
constexpr int W2B = 56 - W2A - W2C;
constexpr int p3_w2(int s) { int n = W2B - 4 * (s - 5); return s < 5 ? 0 : n > 4 ? 4 : n < 0 ? 0 : n; }
constexpr int p3_ops(int s) { return s == 0 ? 5 : s < 5 ? 4 : p3_w2(s); }
constexpr int p3_wait(int m) {
  int n = m < 4 ? 4 * (3 - m) : 0;
  for (int s = (m < 4 ? 0 : m - 3); s < 2 * m - 1; ++s) n += p3_ops(s);
  return n;
}
constexpr int P3_ST = 10;
constexpr int p3_stwait() { int n = 0; for (int s = 5; s < P3_ST; ++s) n += p3_ops(s); return n; }
static_assert(W2B >= 0 && W2B <= 44 && W1A >= 0 && W1A <= 28 && W1A % 4 == 0, "");

__global__ __launch_bounds__(256, 1) void fnet_fused(cptr x, cptr wsre, cptr wsim, cptr wre, cptr wim, cptr w1, cptr w2,
                                                     unsigned char* ws, unsigned char* out) {
  extern __shared__ __attribute__((aligned(16))) unsigned char smem[];
  const int bid = blockIdx.x, p = bid & 7, j = bid >> 3;
  const int ti = 4 * p + (j >> 3), cg = j & 7, t128 = 2 * p + (j >> 4), fs = j & 15;
  const int tid = threadIdx.x, lane = tid & 63, w = __builtin_amdgcn_readfirstlane(tid >> 6), q = lane >> 4, r = lane & 15;
  const unsigned lds0 = (unsigned)(size_t)(LAS unsigned char*)smem;
  const unsigned vl16 = lane * 16;
  unsigned cap = SPIN_CAP;

  const int mat = (j >> 2) & 1, nt = j & 3;
  unsigned epoch, sink = 0;
  v4u xr[2];
  {
    const unsigned vo = (lane >> 5) * 1024 + (lane & 31) * 16, zero = 0;
    cptr xb = x + (size_t)(8 * bid + 2 * w) * 1024;
    LD16(xr[0], vo, xb, 0);
    LD16(xr[1], vo, xb, 512);
    LD4C(epoch, zero, ws + EPOCH_OFF, 0);
  }
  const unsigned va0 = (lane >> 3) * 4096 + (((lane & 7) ^ (lane >> 4)) << 4), va1 = (va0 ^ 64) + 32768 - 1024;
  const int rot = nt & 1;
  cptr abase = (mat ? wsim : wsre) + (size_t)(64 * ti + 16 * w) * 4096;
  cptr abase0 = abase + rot * 2048, abase1 = abase - rot * 2048;
  const unsigned lda = lds0 + w * 2048;
#define A_STAGE0(s) GLDS(va0, ((s) < 16 ? abase0 : abase1) + (s) * 128, lda + ((s) & 15) * 8192, 0)
#define A_STAGE1(s) GLDS(va1, ((s) < 16 ? abase0 : abase1) + (s) * 128, lda + ((s) & 15) * 8192, 1024)
  const int kb1 = bid >> 4, fs1 = bid & 15, kbd = bid & 31, kb2 = 2 * (kbd < 28 ? kbd : 27) + (w & 1);
  {
    cptr pc;
    unsigned len;
    asm volatile("s_getpc_b64 %0\n.Lfnet_pc:\n\ts_mov_b32 %1, .Lfnet_last-.Lfnet_pc" : "=s"(pc), "=s"(len));
    const unsigned vo = min(((4 * j + w) * 8 + (lane & 7)) * 128u, len);
    TOUCH(vo, pc);
  }
  const int wst = WST_OFF + w * 5952;
  v4u wdp;
  auto wload = [&]() {
    const unsigned rs = w < 2 ? D_ * 2 : F_ * 2, vs = (lane >> 3) * rs + (lane & 7) * 16;
    cptr sb = w < 2 ? w2 + (size_t)(32 * kb2) * (D_ * 2) + (bid >> 5) * 128
                    : w1 + (size_t)(32 * kb1) * (F_ * 2) + ((7 * fs1) >> 1) * 64 + (w - 2) * 128;
#pragma unroll
    for (int k = 0; k < 4; ++k) GLDS(vs, sb + k * 8 * rs, lds0 + wst + k * 1088, 0);
    const unsigned vo = (8 * (r >> 2) + (r & 3)) * 1024 + q * 16;
    LD16(wdp, vo, (kbd < 16 ? wre : wim) + (size_t)(64 * (bid >> 5) + 32 * (w >> 1) + 4 * (w & 1)) * 1024 + (kbd & 15) * 64, 0);
  };
  auto wstore = [&]() {
    if (kbd >= 16) wdp ^= 0x80008000u;
    ST16C(wdp, vl16, ws + WD_OFF + bid * 4096 + w * 1024, 0);
    const unsigned char* s = smem + wst + q * 1088 + ((lane >> 2) & 3) * 128 + 16 * (lane & 3);
    unsigned char* db = w < 2 ? ws + W2_OFF + ((bid >> 5) * 56 + kb2) * 4096 : ws + W1_OFF + fs1 * 114688 + kb1 * 7168;
    float csv = 0.f;
#pragma unroll
    for (int i = 0; i < 4; ++i) {
      const int tl = 4 * (w - 2) + i, l = w < 2 ? (kbd < 28 ? i : 7) : (fs1 & 1) ? (tl == 0 ? 7 : tl == 1 ? 6 : tl - 2) : tl;
      const unsigned char* sl = s + (i >> 1) * 64 + (i & 1) * 8;
      const v2u lo = __builtin_bit_cast(v2u, __builtin_amdgcn_ds_read_tr16_b64_v4i16((LAS v4s*)sl));
      const v2u hi = __builtin_bit_cast(v2u, __builtin_amdgcn_ds_read_tr16_b64_v4i16((LAS v4s*)(sl + 512)));
      const v4u o = {lo[0], lo[1], hi[0], hi[1]};
      if (l < 7) ST16C(o, vl16, db + l * 1024, 0);
      const float cs = rows_sum(((bflo(o[0]) + bfhi(o[0])) + (bflo(o[1]) + bfhi(o[1]))) + ((bflo(o[2]) + bfhi(o[2])) + (bflo(o[3]) + bfhi(o[3]))));
      csv = (q == i) ? cs : csv;
    }
    const unsigned vc = r * 16 + q * 4, chl = pk2(csv, csv - rbf(csv));
    if (w >= 2) ST4C(chl, vc, ws + CS_OFF + fs1 * 8192 + (w - 2) * 4096 + kb1 * 256, 0);
  };
#pragma unroll 1
  for (int s = 0; s < NP0; ++s) { A_STAGE0(s); A_STAGE1(s); }
  WAITV(2 * NP0 + 1);
  asm volatile("" : "+v"(xr[0]), "+v"(xr[1]), "+v"(epoch));
  const unsigned tag = __builtin_amdgcn_readfirstlane(epoch) + 1u;
  {
    float v[16];
#pragma unroll
    for (int i = 0; i < 2; ++i)
#pragma unroll
      for (int k = 0; k < 4; ++k) { v[8 * i + 2 * k] = bflo(xr[i][k]); v[8 * i + 2 * k + 1] = bfhi(xr[i][k]); }
    float s1[4] = {0.f, 0.f, 0.f, 0.f};
#pragma unroll
    for (int k = 0; k < 16; ++k) s1[k & 3] += v[k];
    const float mean = half_sum((s1[0] + s1[1]) + (s1[2] + s1[3])) * (1.0f / D_);
    float s2[4] = {0.f, 0.f, 0.f, 0.f};
#pragma unroll
    for (int k = 0; k < 16; ++k) { const float c = v[k] - mean; s2[k & 3] += c * c; }
    const float mb = rbf(mean), rd = ln_rstd(half_sum((s2[0] + s2[1]) + (s2[2] + s2[3])) * (1.0f / D_));
#pragma unroll
    for (int i = 0; i < 2; ++i) {
      v4u o;
#pragma unroll
      for (int k = 0; k < 4; ++k) {
        const unsigned c2 = pk2(v[8 * i + 2 * k] - mb, v[8 * i + 2 * k + 1] - mb);
        o[k] = pk2(bflo(c2) * rd, bfhi(c2) * rd);
      }
      *(v4u*)(smem + XS_OFF + (2 * w + (lane >> 5)) * XS_ROW + (lane & 31) * 16 + i * 512) = o;
    }
  }
  BAR();
  auto tr8 = [&](int off, int i) {
    const unsigned char* s = smem + off + ((lane >> 2) & 3) * XS_ROW + 64 * (q >> 1) + 16 * (lane & 3) + 8 * (q & 1) + 128 * i;
    const v2u lo = __builtin_bit_cast(v2u, __builtin_amdgcn_ds_read_tr16_b64_v4i16((LAS v4s*)s));
    const v2u hi = __builtin_bit_cast(v2u, __builtin_amdgcn_ds_read_tr16_b64_v4i16((LAS v4s*)(s + 4 * XS_ROW)));
    return v4u{lo[0], lo[1], hi[0], hi[1]};
  };
  {
    const v4u o0 = tr8(XS_OFF, 2 * w), o1 = tr8(XS_OFF, 2 * w + 1);
    const unsigned vo = (q >> 1) * 131072 + (q & 1) * 1024 + r * 16;
    unsigned char* sb = ws + XNT_OFF + (bid >> 2) * 2048 + (bid & 3) * 256 + w * 524288;
    ST16C(o0, vo, sb, 0);
    ST16C(o1, vo, sb + 262144, 0);
    auto sw = __builtin_amdgcn_permlane16_swap(sum4(0u, o0), sum4(0u, o1), false, false);
    const int ck = (int)row_isum(sw[0] + sw[1]);
    const v4u c4 = {tag + __builtin_amdgcn_readlane(ck, 15), tag + __builtin_amdgcn_readlane(ck, 47),
                    tag + __builtin_amdgcn_readlane(ck, 31), tag + __builtin_amdgcn_readlane(ck, 63)};
    ST_TAG(c4, ws + CKX_OFF + bid * 512 + w * 128);
  }
  wload();
#pragma unroll 1
  for (int s = NP0; s < 16; ++s) { A_STAGE0(s); A_STAGE1(s); }

  cptr bbase0 = ws + XNT_OFF + (4 * nt + w) * 131072 + rot * 65536, bbase1 = bbase0 - rot * 131072;
  cptr xcb = ws + CKX_OFF + nt * 128;
#define BB(s) ((s) < 16 ? bbase0 : bbase1)
#define XCB(r8) (xcb + (((r8) + 2 * rot) & 3) * 32768)
  const unsigned vl512 = lane * 512;
  v4u bf[8][4];
  unsigned xt[(NV + 7) / 8];
#define B_LOAD(s, i) LD16C(bf[(s) & 7][i], vl16, BB(s) + (s) * 4096 + ((s) < NV ? WRONG : 0), (i) * 1024)
#pragma unroll
  for (int s = 0; s < DB; ++s) {
    B_LOAD(s, 0); B_LOAD(s, 1); B_LOAD(s, 2); B_LOAD(s, 3);
    if (s == XTP && w == 0) GLDSC(vl512, XCB(0), lds0 + XT_OFF, 0);
  }
  asm volatile("s_waitcnt vmcnt(%2)" : "+v"(wdp), "+v"(sink) : "i"(4 * DB) : "memory");
  wstore();
  f32x4 acc1[2][4];
#pragma unroll
  for (int i = 0; i < 2; ++i)
#pragma unroll
    for (int t = 0; t < 4; ++t) acc1[i][t] = f32x4{0.f, 0.f, 0.f, 0.f};
  const unsigned ra0 = r * 128 + ((q ^ (r >> 1)) << 4), ra1 = ra0 ^ 64;
  v4u af[2][8];
  v4u xres[2], csr[8], wd[8][4];
  unsigned flw = tag;
  const v4u tag4 = {tag, tag, tag, tag};
  const unsigned vfl = (lane < 32 ? 32 * cg + lane : 16 * r + fs) * 128;
  cptr xb2 = x + (size_t)(64 * ti + 16 * w) * 1024 + cg * 128;
  const unsigned vx = r * 1024 + q * 16;
  cptr cb2 = ws + CS_OFF + fs * 8192;
  const unsigned vc2 = q * 1024 + r * 16;
  cptr wb = ws + WD_OFF + cg * 131072 + w * 32768;
#define BAD(M) (__builtin_amdgcn_ballot_w64((M) != 0u) != 0)
#define WAIT4(F, N, TAIL) asm volatile("s_waitcnt vmcnt(%4)" TAIL : "+v"(F[0]), "+v"(F[1]), "+v"(F[2]), "+v"(F[3]) : "i"(N) : "memory")
#define XMASK(s) (0xffull << (8 * ((s) & 7)))
  WAIT4(bf[0], p1_wait(-1, false), " lgkmcnt(0)\n\ts_barrier");
#define XT_READ(i) do { xt[i] = *(const unsigned*)(smem + XT_OFF + (i) * 5952 + vl16 + w * 4); asm volatile("" : "+v"(xt[i])); } while (0)
  XT_READ(0);
#pragma unroll
  for (int t = 0; t < 4; ++t) { af[0][t] = *(const v4u*)(smem + ra0 + t * 2048); af[0][4 + t] = *(const v4u*)(smem + ra1 + t * 2048); }
  unsigned bx, bs;
  CK_ALL(bs, xt[0], XMASK(0), bf[0]);
#define P1_EXTRA(e)                                                                                                     \
  if constexpr ((e) < p1_extra(it)) {                                                                                   \
    constexpr int n_ = p1_xbase(it) + (e);                                                                              \
    if constexpr (n_ < 2) LD16(xres[n_], vx, xb2, n_ * 64);                                                             \
    else if constexpr (n_ < 10) LD16C(csr[n_ - 2], vc2, cb2 + ((n_ - 2) >> 2) * 4096, ((n_ - 2) & 3) * 256);            \
    else LD16CA(wd[(n_ - 10) >> 2][(n_ - 10) & 3], vl16, wb + ((n_ - 10) >> 2) * 4096, ((n_ - 10) & 3) * 1024);         \
  }
#define MM1(i, t, kb) acc1[i][t] = mfma(bf[U][2 * (kb) + (i)], af[C][4 * (kb) + (t)], acc1[i][t]); SCHED()
#define RD1(n) if constexpr (RN) { af[Nx][n] = *(const v4u*)(sn + ((n) < 4 ? ra0 : ra1) + ((n) & 3) * 2048); SCHED(); }
#define CK1(what) if constexpr (it + 1 < NV) { what; SCHED(); }
  static_for<0, 32>([&](auto IT) {
    constexpr int it = IT, U = it & 7, Un = (it + 1) & 7, C = it & 1, Nx = C ^ 1;
    constexpr bool RA = p1_ra(it), RB = it + DB < 32, RN = it + 1 < 32;
    if constexpr (it < NV) {
      if (__builtin_expect(bs != 0, 0)) {
        constexpr int hi0 = it + DB < NV ? it + DB : NV, hi = hi0 < (it / 8 + 1) * 8 ? hi0 : (it / 8 + 1) * 8;
        unsigned spin = cap;
        WAITV(0);
        do {
#pragma unroll
          for (int g = it; g < hi0; ++g)
#pragma unroll
            for (int c = 0; c < 4; ++c) RLD16(bf[g & 7][c], vl16, BB(g) + g * 4096, c * 1024);
          RLD4(xt[it / 8], vl512, XCB(it / 8) + w * 4, 0);
          WAITV(0);
          unsigned bad = 0;
#pragma unroll
          for (int g = it; g < hi; ++g) { unsigned v_; CK_ALL(v_, xt[g / 8], XMASK(g), bf[g & 7]); bad |= v_; }
          if (bad == 0) break;
          if (--spin == 0) { cap = 1; break; }
        } while (true);
      }
    }
    if constexpr (it == FL0_CHK) {
      asm volatile("s_waitcnt vmcnt(%1)" : "+v"(flw) : "i"(p1_wait(it, false)) : "memory");
      if (!__all(flw == tag)) poll_flags(ws + FLAG0_OFF, vfl, tag);
    }
    if constexpr (p1_bar(it)) WAITV_BAR(p1_wait(it, false));
    if constexpr ((it & 7) == 7 && it + 1 < NV) XT_READ((it + 1) / 8);
    if constexpr (p1_xt(it)) { if (w == it / 8 + 1) GLDSC(vl512, XCB(it / 8 + 1), lds0 + XT_OFF + (it / 8 + 1) * 5952, 0); }
    if constexpr (it == FL0_PUB) { if (w == 0) ST_TAG(tag4, ws + FLAG0_OFF + bid * 128); }
    if constexpr (it == FL0_IT)
      asm volatile("s_cmp_lg_u32 %3, 0\n\ts_cbranch_scc1 1f\n\tglobal_load_dword %0, %1, %2 sc1\n1:"
                   : "+v"(flw) : "v"(vfl), "s"(ws + FLAG0_OFF), "s"(w) : "memory", "scc");
    const unsigned char* sn = smem + ((it + 1) & 15) * 8192;
    SCHED();
    MM1(0, 0, 0); RD1(0); if constexpr (RN) { WAIT4(bf[Un], p1_wait(it, true), ""); SCHED(); }
    MM1(1, 0, 0); if constexpr (RA) A_STAGE0(p1_as(it)); P1_EXTRA(0) SCHED();
    MM1(0, 1, 0); RD1(1); if constexpr (RA && RAN == 2) { A_STAGE0(p1_as(it) + 1); SCHED(); } CK1(CK_INIT(bx, xt[(it + 1) / 8], XMASK(it + 1)));
    MM1(1, 1, 0); RD1(2); if constexpr (RA && RAN == 2) { A_STAGE1(p1_as(it)); SCHED(); } CK1(ACC4(bx, bx, bf[Un][0]));
    MM1(0, 2, 0); if constexpr (RA) A_STAGE1(p1_as(it) + RAN - 1); P1_EXTRA(1) SCHED();
    MM1(1, 2, 0); RD1(3); CK1(ACC4(bx, bx, bf[Un][1]));
    MM1(0, 3, 0); if constexpr (RB) B_LOAD(it + DB, 0); P1_EXTRA(2) SCHED(); CK1(ACC4(bx, bx, bf[Un][2]));
    MM1(1, 3, 0); RD1(4); CK1(ACC4(bx, bx, bf[Un][3]));
    MM1(0, 0, 1); RD1(5); CK1(CK_RED(bx, 0));
    MM1(1, 0, 1); if constexpr (RB) B_LOAD(it + DB, 1); P1_EXTRA(3) SCHED(); CK1(CK_RED(bx, 1));
    MM1(0, 1, 1); RD1(6); CK1(CK_RED(bx, 2));
    MM1(1, 1, 1); if constexpr (RB) B_LOAD(it + DB, 2); P1_EXTRA(4) SCHED(); CK1(CK_RED(bx, 3));
    MM1(0, 2, 1); RD1(7); CK1(CK_RED(bx, 4));
    MM1(1, 2, 1); if constexpr (RB) B_LOAD(it + DB, 3); P1_EXTRA(5) SCHED(); CK1(CK_RED(bx, 5));
    MM1(0, 3, 1); CK1(CK_END(bs, bx));
    MM1(1, 3, 1);
  });
  {
    const int kbq = ti * 32 + 16 * mat + 4 * nt + w;
    unsigned char* pq = ws + PQ_OFF + kbq * 4096;
    unsigned ck = 0;
#pragma unroll
    for (int t = 0; t < 4; ++t) {
      const v4u o = {pk2(acc1[0][t][0], acc1[0][t][1]), pk2(acc1[0][t][2], acc1[0][t][3]),
                     pk2(acc1[1][t][0], acc1[1][t][1]), pk2(acc1[1][t][2], acc1[1][t][3])};
      ST16C(o, vl16, pq + t * 1024, 0);
      ck = sum4(ck, o);
    }
    ck = tag + last_lane(wave_isum(ck));
    const v4u c4 = {ck, ck, ck, ck};
    ST_TAG(c4, ws + CKP_OFF + kbq * 128);
    if (bid == 0 && w == 0) ST_TAG(tag4, ws + EPOCH_OFF);
  }
#pragma unroll
  for (int n = 32 - WDS; n < 32; ++n) LD16CA(wd[n >> 2][n & 3], vl16, wb + (n >> 2) * 4096, (n & 3) * 1024);
  cptr w1src = ws + W1_OFF + fs * 114688 + w * 28672;
  const unsigned w1dst = lds0 + w * 28672;
#define W1L(n) GLDSC(vl16, w1src + ((n) >> 2) * 4096, w1dst + ((n) >> 2) * 4096, ((n) & 3) * 1024)
#pragma unroll 1
  for (int n = 0; n < W1A / 4; ++n) {
    GLDSC(vl16, w1src + n * 4096, w1dst + n * 4096, 0);
    GLDSC(vl16, w1src + n * 4096, w1dst + n * 4096, 1024);
    GLDSC(vl16, w1src + n * 4096, w1dst + n * 4096, 2048);
    GLDSC(vl16, w1src + n * 4096, w1dst + n * 4096, 3072);
  }
  v4u csf[7];
  {
    static_assert(p1_xbase(26) >= 10, "the wait for the last XNT stage covers the column-sum loads");
    PIN4(csr[0], csr[1], csr[2], csr[3]);
    PIN4(csr[4], csr[5], csr[6], csr[7]);
    const bool odd = fs & 1;
#pragma unroll
    for (int l = 0; l < 7; ++l) {
      const int te = l, to = l < 6 ? l + 2 : 1;
#pragma unroll
      for (int i = 0; i < 4; ++i) csf[l][i] = odd ? csr[(to >> 2) * 4 + i][to & 3] : csr[(te >> 2) * 4 + i][te & 3];
      asm volatile("" : "+a"(csf[l]));
    }
  }
#define MM64(i, c) acc[i][c] = mfma(WT_[i], FC_[c], acc[i][c]); SCHED()
#define STEP64(WT, FC, FN, WAITN, T, MASK, OP)                                                                          \
  {                                                                                                                     \
    auto& WT_ = WT; auto& FC_ = FC;                                                                                     \
    SCHED();                                                                                                            \
    MM64(0, 0); WAITN; SCHED();                                                                                         \
    MM64(0, 1); OP(0); SCHED();                                                                                         \
    MM64(0, 2); CK_INIT(mx, T, MASK); SCHED();                                                                          \
    MM64(0, 3); ACC4(mx, mx, FN[0]); SCHED();                                                                           \
    MM64(1, 0); ACC4(mx, mx, FN[1]); SCHED();                                                                           \
    MM64(1, 1); OP(1); SCHED();                                                                                         \
    MM64(1, 2); ACC4(mx, mx, FN[2]); SCHED();                                                                           \
    MM64(1, 3); ACC4(mx, mx, FN[3]); SCHED();                                                                           \
    MM64(2, 0); CK_RED(mx, 0); SCHED();                                                                                 \
    MM64(2, 1); OP(2); CK_RED(mx, 1); SCHED();                                                                          \
    MM64(2, 2); CK_RED(mx, 2); SCHED();                                                                                 \
    MM64(2, 3); CK_RED(mx, 3); SCHED();                                                                                 \
    MM64(3, 0); CK_RED(mx, 4); SCHED();                                                                                 \
    MM64(3, 1); OP(3); CK_RED(mx, 5); SCHED();                                                                          \
    MM64(3, 2); CK_END(ms, mx); SCHED();                                                                                \
    MM64(3, 3);                                                                                                         \
  }
#define WAIT4T(F, T, N) asm volatile("s_waitcnt vmcnt(%5)" : "+v"(F[0]), "+v"(F[1]), "+v"(F[2]), "+v"(F[3]), "+v"(T) : "i"(N) : "memory")

  unsigned vrot[4];
#pragma unroll
  for (int c = 0; c < 4; ++c) vrot[c] = vl16 + (c ^ w) * 1024;
  v4u hres[2];
  v4u w2f[14][4];
  cptr w2base = ws + W2_OFF + cg * 229376 + w * 57344;
#define W2L(n) do { if ((n) < 32) LD16CA(w2f[(n) >> 2][(n) & 3], vl16, w2base + ((n) >> 2) * 4096, ((n) & 3) * 1024);   \
                    else LD16C(w2f[(n) >> 2][(n) & 3], vl16, w2base + ((n) >> 2) * 4096, ((n) & 3) * 1024); } while (0)
  {
    f32x4 acc[4][4];
    v4u pq[8][4];
    unsigned tw[2];
    cptr pb = ws + PQ_OFF + ti * 131072 + w * 32768;
    cptr cb = ws + CKP_OFF + (ti * 32 + 8 * w) * 128;
    const unsigned vt = (lane & 7) * 128;
    if (GATE & 1) WAITV(WDS + W1A);
#pragma unroll
    for (int k = 0; k < D2; ++k) {
#pragma unroll
      for (int c = 0; c < 4; ++c) LD16C(pq[k][c], vrot[c], pb + k * 4096 + WRONG, 0);
      if (k == 0) LD4C(tw[0], vt, cb, 0);
    }
    asm volatile("s_waitcnt vmcnt(%2)" : "+v"(xres[0]), "+v"(xres[1]) : "i"(4 * D2 + 1) : "memory");
#pragma unroll
    for (int i = 0; i < 4; ++i) {
      acc[i][0] = unpk(xres[i >> 1][2 * (i & 1)], xres[i >> 1][2 * (i & 1) + 1]);
#pragma unroll
      for (int c = 1; c < 4; ++c) acc[i][c] = f32x4{0.f, 0.f, 0.f, 0.f};
    }
    WAIT4T(pq[0], tw[0], 4 * D2 - 4);
    unsigned mx, ms;
    CK_ALL(ms, tw[0], 1ull, pq[0]);
#define P2OP(i) do { if constexpr (k == 0 && (i) == 0) LD4C(tw[1], vt, cb, 0);                                           \
                     if constexpr (k + D2 < 8) LD16C(pq[(k + D2) & 7][i], vrot[i], pb + (k + D2) * 4096 + WRONG, 0);     \
                     else if constexpr ((i) < p2_w1(k)) W1L(W1A + 4 * (k + D2 - 8) + (i)); } while (0)
    static_for<0, 8>([&](auto K_) {
      constexpr int k = K_, kn = (k + 1) & 7;
      if (__builtin_expect(ms != 0, 0)) {
        constexpr int hi = k + D2 < 8 ? k + D2 : 8;
        unsigned spin = cap;
        WAITV(0);
        do {
#pragma unroll
          for (int g = k; g < hi; ++g)
#pragma unroll
            for (int c = 0; c < 4; ++c) RLD16(pq[g][c], vrot[c], pb + g * 4096, 0);
          if constexpr (k < D2) RLD4(tw[0], vt, cb, 0);
          if constexpr (k > 0) RLD4(tw[1], vt, cb, 0);
          WAITV(0);
          unsigned bad = 0;
#pragma unroll
          for (int g = k; g < hi; ++g) { unsigned v_; CK_ALL(v_, tw[g >= D2], 1ull << g, pq[g]); bad |= v_; }
          if (bad == 0) break;
          if (--spin == 0) { cap = 1; break; }
        } while (true);
      }
      PIN4A(wd[k][0], wd[k][1], wd[k][2], wd[k][3]);
      if constexpr (k < 7) STEP64(wd[k], pq[k], pq[kn], WAIT4T(pq[kn], tw[kn >= D2], p2_wait(kn)), tw[kn >= D2], 1ull << kn, P2OP);
    });
    last_step_reduce(smem + RED_OFF, w, lane, wd[7], pq[7], acc, hres, [&]() {
#pragma unroll
      for (int n = W1A + 4 * (D2 - 1); n < 28 - W1C; ++n) W1L(n);
    });
    const int hp = (4 * ti + w) * 8 + cg;
    ST16C(hres[0], vl16, ws + H_OFF + hp * 2048, 0);
    ST16C(hres[1], vl16, ws + H_OFF + hp * 2048, 1024);
    const unsigned hck = tag + last_lane(wave_isum(sum4(sum4(0u, hres[0]), hres[1])));
    const v4u c4 = {hck, hck, hck, hck};
    ST_TAG(c4, ws + CKH_OFF + hp * 128);
    f32x4 hv[4];
#pragma unroll
    for (int i = 0; i < 4; ++i) hv[i] = unpk(hres[i >> 1][2 * (i & 1)], hres[i >> 1][2 * (i & 1) + 1]);
    const f32x4 s4 = (hv[0] + hv[1]) + (hv[2] + hv[3]);
    const float m = rows_sum((s4[0] + s4[1]) + (s4[2] + s4[3])) * (1.0f / 64.0f);
    f32x4 q4 = {0.f, 0.f, 0.f, 0.f};
#pragma unroll
    for (int i = 0; i < 4; ++i) { const f32x4 c = hv[i] - m; q4 += c * c; }
    const float m2 = rows_sum((q4[0] + q4[1]) + (q4[2] + q4[3]));
    const unsigned mu = __builtin_bit_cast(unsigned, m), m2u = __builtin_bit_cast(unsigned, m2);
    const v4u st = {mu, m2u, tag + mu + m2u, 0u};
    asm volatile("s_mov_b64 exec, 0xffff\n\tglobal_store_dwordx4 %0, %1, %2 sc1\n\ts_mov_b64 exec, -1\n\ts_nop 0" ::"v"(vl16), "v"(st), "s"(ws + ST_OFF + hp * 256) : "memory");
  }
#pragma unroll
  for (int n = 28 - W1C; n < 28; ++n) W1L(n);
#pragma unroll
  for (int n = 0; n < W2A; ++n) W2L(n);
  {
    v4u hf[8][4];
    unsigned tw[2];
    v4u str[2][2];
    cptr hb = ws + H_OFF + (8 * t128 + 2 * w) * 16384;
    cptr cb = ws + CKH_OFF + (8 * t128 + 2 * w) * 1024;
    const unsigned vt = r * 128;
    const unsigned vh1 = vl16 + 16384;
    const unsigned vs = q * 512 + r * 16;
    cptr stb = ws + ST_OFF + (8 * t128 + 2 * w) * 2048;
#define HL(m, i) LD16C(hf[m][i], ((i) & 1) ? vh1 : vl16, hb + ((m) >> 1) * 4096 + WRONG, ((m) & 1) * 2048 + ((i) >> 1) * 1024)
#define HMASK(m) (0x101ull << (m))
    if (GATE & 2) WAITV(W1C + W2A + 1);
#pragma unroll
    for (int m = 0; m < 4; ++m) { HL(m, 0); HL(m, 1); HL(m, 2); HL(m, 3); if (m == 0) LD4C(tw[0], vt, cb, 0); }
    f32x4 acc[7][2];
#pragma unroll
    for (int l = 0; l < 7; ++l) { acc[l][0] = f32x4{0.f, 0.f, 0.f, 0.f}; acc[l][1] = f32x4{0.f, 0.f, 0.f, 0.f}; }
    WAIT4T(hf[0], tw[0], 12);
    BAR();
    unsigned whi_ = lds0 + vl16 + 57344;
    asm volatile("" : "+v"(whi_));
    const LAS unsigned char* wlo = (const LAS unsigned char*)smem + vl16;
    const LAS unsigned char* whi = (const LAS unsigned char*)(size_t)whi_;
#define W1F(k, l) (*(const LAS v4u*)(((k) < 8 ? wlo + (k) * 7168 : whi + ((k) - 8) * 7168) + (l) * 1024))
    v4u wf[2][7];
#pragma unroll
    for (int l = 0; l < 7; ++l) wf[0][l] = W1F(0, l);
    unsigned mx, ms;
    CK_ALL(ms, tw[0], HMASK(0), hf[0]);
    float rd[2], sm[2], sq[2];
    unsigned nmf[2];
    auto stat_piece = [&](auto N_) {
      constexpr int n = N_ >> 1, t = N_ & 1;
      if constexpr (n == 0) sm[t] = swap16_sum(asf(str[t][0][0]) + asf(str[t][1][0]));
      if constexpr (n == 1) sm[t] = swap32_sum(sm[t]) * 0.125f;
      if constexpr (n == 2) { const float da = asf(str[t][0][0]) - sm[t], db = asf(str[t][1][0]) - sm[t]; sq[t] = da * da + db * db; }
      if constexpr (n == 3) sq[t] = asf(str[t][0][1]) + asf(str[t][1][1]) + 64.0f * sq[t];
      if constexpr (n == 4) sq[t] = swap16_sum(sq[t]);
      if constexpr (n == 5) sq[t] = swap32_sum(sq[t]) * (1.0f / D_);
      if constexpr (n == 6) nmf[t] = pk2(-sm[t], -sm[t]);
      if constexpr (n == 7) sq[t] = rbf(sq[t]) + 1e-5f;
      if constexpr (n == 8) sq[t] = __builtin_amdgcn_sqrtf(rbf(sq[t]));
      if constexpr (n == 9) rd[t] = __builtin_amdgcn_rcpf(rbf(sq[t]));
      if constexpr (n < 2) asm volatile("" : "+v"(sm[t]));
      else if constexpr (n == 6) asm volatile("" : "+v"(nmf[t]));
      else if constexpr (n == 9) asm volatile("" : "+v"(rd[t]));
      else if constexpr (n < 9) asm volatile("" : "+v"(sq[t]));
    };
#define P3OP(i) do { if constexpr (k == 0 && (i) == 0) LD4C(tw[1], vt, cb, 0);                                           \
                     if constexpr (k < 4) HL((4 + k) & 7, i);                                                           \
                     else if constexpr (k == 4) LD16C(str[(i) >> 1][(i) & 1], vs, stb + WRONG, ((i) >> 1) * 2048 + ((i) & 1) * 256); \
                     else if constexpr ((i) < p3_w2(k)) W2L(W2A + 4 * (k - 5) + (i)); } while (0)
    static_for<0, 16>([&](auto K_) {
      constexpr int k = K_, m = k >> 1, mn = ((k + 1) >> 1) & 7;
      if ((k & 1) == 0 && __builtin_expect(ms != 0, 0)) {
        constexpr int hi = 4 + k < 8 ? 4 + k : 8;
        unsigned spin = cap;
        WAITV(0);
        do {
#pragma unroll
          for (int g = m; g < hi; ++g)
#pragma unroll
            for (int c = 0; c < 4; ++c) RLD16(hf[g][c], (c & 1) ? vh1 : vl16, hb + g * 2048, (c >> 1) * 1024);
          if constexpr (m < 4) RLD4(tw[0], vt, cb, 0);
          if constexpr (m > 0) RLD4(tw[1], vt, cb, 0);
          WAITV(0);
          unsigned bad = 0;
#pragma unroll
          for (int g = m; g < hi; ++g) { unsigned v_; CK_ALL(v_, tw[g >= 4], HMASK(g), hf[g]); bad |= v_; }
          if (bad == 0) break;
          if (--spin == 0) { cap = 1; break; }
        } while (true);
      }
      if constexpr (k == P3_ST) {
        asm volatile("s_waitcnt vmcnt(%4)" : "+v"(str[0][0]), "+v"(str[0][1]), "+v"(str[1][0]), "+v"(str[1][1]) : "i"(p3_stwait()) : "memory");
#define STCK(a) (tag + (a)[0] + (a)[1] - (a)[2])
        if (__builtin_expect(BAD(STCK(str[0][0]) | STCK(str[0][1]) | STCK(str[1][0]) | STCK(str[1][1])), 0)) {
          unsigned spin = cap;
          WAITV(0);
          do {
            asm volatile("global_load_dwordx4 %0, %4, %5 sc1\n\tglobal_load_dwordx4 %1, %4, %5 offset:256 sc1\n\t"
                         "global_load_dwordx4 %2, %4, %5 offset:2048 sc1\n\tglobal_load_dwordx4 %3, %4, %5 offset:2304 sc1\n\ts_waitcnt vmcnt(0)"
                         : "=&v"(str[0][0]), "=&v"(str[0][1]), "=&v"(str[1][0]), "=&v"(str[1][1]) : "v"(vs), "s"(stb) : "memory");
            if (!BAD(STCK(str[0][0]) | STCK(str[0][1]) | STCK(str[1][0]) | STCK(str[1][1]))) break;
            if (--spin == 0) { cap = 1; break; }
          } while (true);
        }
      }
      SCHED();
      static_for<0, 7>([&](auto L_) {
        constexpr int l = L_;
        constexpr bool CHK = (k & 1) && k + 1 < 16;
        acc[l][0] = mfma(wf[k & 1][l], hf[m][2 * (k & 1)], acc[l][0]);
        SCHED();
        if constexpr (k + 1 < 16) wf[(k + 1) & 1][l] = W1F(k + 1, l);
        if constexpr (CHK) {
          if constexpr (l == 0) WAIT4T(hf[mn], tw[mn >= 4], p3_wait(mn));
          if constexpr (l == 1) ACC4(mx, mx, hf[mn][0]);
          if constexpr (l == 2) ACC4(mx, mx, hf[mn][2]);
          if constexpr (l >= 3 && l < 6) CK_RED(mx, 2 * (l - 3));
          if constexpr (l == 6) CK_END(ms, mx);
        }
        if constexpr (k >= P3_ST && !(k & 1)) stat_piece(std::integral_constant<int, 7 * ((k - P3_ST) / 2) + l>{});
        SCHED();
        acc[l][1] = mfma(wf[k & 1][l], hf[m][2 * (k & 1) + 1], acc[l][1]);
        SCHED();
        if constexpr (l == 0 || l == 2 || l == 4 || l == 5) P3OP(l == 5 ? 3 : l / 2);
        if constexpr (CHK) {
          if constexpr (l == 0) CK_INIT(mx, tw[mn >= 4], HMASK(mn));
          if constexpr (l == 1) ACC4(mx, mx, hf[mn][1]);
          if constexpr (l == 2) ACC4(mx, mx, hf[mn][3]);
          if constexpr (l >= 3 && l < 6) CK_RED(mx, 2 * (l - 3) + 1);
        }
        SCHED();
      });
    });
    const int kbp = (7 * fs + (fs & 1)) >> 1, taus = 7 * fs + ((fs & 1) ? 0 : 6);
    const int t64 = 2 * t128 + (w >> 1);
    unsigned char* fb = ws + FF_OFF + t64 * 229376 + kbp * 4096 + 2 * (w & 1) * 1024;
    v4u fck = {0u, 0u, 0u, 0u};
    {
      const v4u n0 = {nmf[0], nmf[0], nmf[0], nmf[0]}, n1 = {nmf[1], nmf[1], nmf[1], nmf[1]};
#pragma unroll
      for (int l = 0; l < 7; ++l) {
        acc[l][0] = mfma(csf[l], n0, acc[l][0]);
        acc[l][1] = mfma(csf[l], n1, acc[l][1]);
      }
    }
    auto act = [&](int l, int t) {
      const f32x4 v = acc[l][t] * rd[t];
      return v2u{relu2(pk2(v[0], v[1])), relu2(pk2(v[2], v[3]))};
    };
#pragma unroll
    for (int m_ = 0; m_ < 3; ++m_)
#pragma unroll
      for (int t = 0; t < 2; ++t) {
        const v2u a = act(2 * m_, t), b = act(2 * m_ + 1, t);
        const v4u oo = {a[0], a[1], b[0], b[1]};
        if (t == 0) ST16C(oo, vl16, fb + m_ * 4096, 0); else ST16C(oo, vl16, fb + m_ * 4096, 1024);
        fck[m_] = sum4(fck[m_], oo);
      }
    const v2u a6 = act(6, 0), b6 = act(6, 1);
    const v4u o6 = {a6[0], a6[1], b6[0], b6[1]};
    ST16C(o6, vl16, ws + FF_OFF + t64 * 229376 + (taus >> 1) * 4096 + (2 * (taus & 1) + (w & 1)) * 1024, 0);
    fck[3] = sum4(fck[3], o6);
#pragma unroll
    for (int i = 0; i < 4; ++i) fck[i] = tag + last_lane(wave_isum(fck[i]));
    ST_TAG(fck, ws + CKF_OFF + ((t64 * 16 + fs) * 2 + (w & 1)) * 128);
  }
#pragma unroll
  for (int n = 56 - W2C; n < 56; ++n) W2L(n);

  {
    f32x4 acc[4][4];
    constexpr int R = D4 + 1;
    v4u ff[R][4];
    unsigned tw[2];
    cptr fb = ws + FF_OFF + ti * 229376 + w * 57344;
    cptr cb = ws + CKF_OFF + (ti * 16 + 4 * w) * 256;
    const unsigned vt = ((lane >> 2) & 7) * 128 + (lane & 3) * 4;
    unsigned vsh[4];
#pragma unroll
    for (int c = 0; c < 4; ++c) vsh[c] = vl16 + (c ^ (w >> 1)) * 1024;
#define VR4(g, c) ((g) % 7 == 3 ? vsh[c] : vrot[c])
#define FMASK(g) (((g) % 7 == 3 ? 0x8888ull : (g) % 7 < 3 ? 0x11ull << ((g) % 7) : 0x1100ull << (((g) % 7) & 3)) << (16 * ((g) / 7)))
    if (GATE & 4) WAITV(W2C);
#pragma unroll
    for (int k = 0; k < D4; ++k) {
#pragma unroll
      for (int c = 0; c < 4; ++c) LD16C(ff[k][c], VR4(k, c), fb + k * 4096 + WRONG, 0);
      if (k == 0) LD4C(tw[0], vt, cb, 0);
    }
#pragma unroll
    for (int i = 0; i < 4; ++i) {
      acc[i][0] = unpk(hres[i >> 1][2 * (i & 1)], hres[i >> 1][2 * (i & 1) + 1]);
#pragma unroll
      for (int c = 1; c < 4; ++c) acc[i][c] = f32x4{0.f, 0.f, 0.f, 0.f};
    }
    WAIT4T(ff[0], tw[0], 4 * D4 - 4);
    unsigned mx, ms;
    CK_ALL(ms, tw[0], FMASK(0), ff[0]);
#define P4OP(i) do { if constexpr (k == 0 && (i) == 0) LD4C(tw[1], vt, cb, 0);                                           \
                     if constexpr (k + D4 < 14) LD16C(ff[(k + D4) % R][i], VR4(k + D4, i), fb + (k + D4) * 4096 + WRONG, 0); } while (0)
    static_for<0, 14>([&](auto K_) {
      constexpr int k = K_, kn = (k + 1) % 14;
      if (__builtin_expect(ms != 0, 0)) {
        constexpr int hi = k + D4 < 14 ? k + D4 : 14;
        unsigned spin = cap;
        WAITV(0);
        do {
          if constexpr (k == 13) asm volatile(".Lfnet_last:");
#pragma unroll
          for (int g = k; g < hi; ++g)
#pragma unroll
            for (int c = 0; c < 4; ++c) RLD16(ff[g % R][c], VR4(g, c), fb + g * 4096, 0);
          if constexpr (k < D4) RLD4(tw[0], vt, cb, 0);
          if constexpr (k > 0) RLD4(tw[1], vt, cb, 0);
          WAITV(0);
          unsigned bad = 0;
#pragma unroll
          for (int g = k; g < hi; ++g) { unsigned v_; CK_ALL(v_, tw[g >= D4], FMASK(g), ff[g % R]); bad |= v_; }
          if (bad == 0) break;
          if (--spin == 0) { cap = 1; break; }
        } while (true);
      }
      if (k % 7 == 3) {
        v4u* F = ff[k % R];
        const bool w0 = w & 1;
        const v4u e0 = {F[0][0], F[0][1], F[2][0], F[2][1]}, o0 = {F[0][2], F[0][3], F[2][2], F[2][3]};
        const v4u e1 = {F[1][0], F[1][1], F[3][0], F[3][1]}, o1 = {F[1][2], F[1][3], F[3][2], F[3][3]};
        F[0] = w0 ? o0 : e0; F[1] = w0 ? e0 : o0; F[2] = w0 ? o1 : e1; F[3] = w0 ? e1 : o1;
      }
      if (k < 8) PIN4A(w2f[k][0], w2f[k][1], w2f[k][2], w2f[k][3]);
      else PIN4(w2f[k][0], w2f[k][1], w2f[k][2], w2f[k][3]);
      if constexpr (k < 13) STEP64(w2f[k], ff[k % R], ff[kn % R], WAIT4T(ff[kn % R], tw[kn >= D4], p4_wait(kn)), tw[kn >= D4], FMASK(kn), P4OP);
    });
    v4u o[2];
    last_step_reduce(smem + RED_OFF, w, lane, w2f[13], ff[13 % R], acc, o, []() {});
    unsigned char* ob_ = out + (size_t)(64 * ti + 16 * w) * 1024 + cg * 128;
    ST16(o[0], vx, ob_, 0);
    ST16(o[1], vx, ob_, 64);
  }
}

extern "C" void kernel_launch(void* const* d_in, const int* in_sizes, int n_in, void* d_out, int out_size, void* d_ws,
                              size_t ws_size, hipStream_t stream) {
  (void)hipFuncSetAttribute((const void*)fnet_fused, hipFuncAttributeMaxDynamicSharedMemorySize, LDS_BYTES);
  fnet_fused<<<256, 256, LDS_BYTES, stream>>>((cptr)d_in[0], (cptr)d_in[1], (cptr)d_in[2], (cptr)d_in[3], (cptr)d_in[4],
                                              (cptr)d_in[6], (cptr)d_in[7], (unsigned char*)d_ws, (unsigned char*)d_out);
}
```

```cpp
#include <hip/hip_runtime.h>
#include <hip/hip_bf16.h>
#include <stdint.h>
#include <type_traits>

typedef unsigned v4u __attribute__((ext_vector_type(4)));
typedef unsigned v2u __attribute__((ext_vector_type(2)));
typedef float f32x4 __attribute__((ext_vector_type(4)));
typedef float f32x2 __attribute__((ext_vector_type(2)));
typedef short bf16x8 __attribute__((ext_vector_type(8)));
typedef short v4s __attribute__((ext_vector_type(4)));
typedef __bf16 bf2_t __attribute__((ext_vector_type(2)));
typedef const unsigned char* cptr;
#define LAS __attribute__((address_space(3)))

constexpr int S_ = 2048, D_ = 512, F_ = 1792;
constexpr unsigned EPOCH_OFF = 0;
constexpr unsigned FLAG0_OFF = 32768;
constexpr unsigned XNT_OFF = 131072;
constexpr unsigned WD_OFF  = XNT_OFF + (2u << 20);
constexpr unsigned W1_OFF  = WD_OFF + (1u << 20);
constexpr unsigned W2_OFF  = W1_OFF + 1835008u;
constexpr unsigned CS_OFF  = W2_OFF + 1835008u;
constexpr unsigned PQ_OFF  = CS_OFF + 131072;
constexpr unsigned H_OFF   = PQ_OFF + (4u << 20);
constexpr unsigned FF_OFF  = H_OFF + (2u << 20);
constexpr unsigned ST_OFF  = FF_OFF + 7340032u;
constexpr unsigned CKP_OFF = ST_OFF + 262144;
constexpr unsigned CKH_OFF = CKP_OFF + 131072;
constexpr unsigned CKF_OFF = CKH_OFF + 131072;
constexpr unsigned CKX_OFF = CKF_OFF + 131072;

constexpr int XS_ROW = 1088;
constexpr int XS_OFF = 131072, WST_OFF = XS_OFF + 8 * XS_ROW;
constexpr int XT_OFF = WST_OFF + 4352;
constexpr int RED_OFF = 114688;
constexpr int LDS_BYTES = 163840;
constexpr unsigned SPIN_CAP = 1u << 20;

__device__ __forceinline__ unsigned pk2(float a, float b) {
  f32x2 v = {a, b};
  return __builtin_bit_cast(unsigned, __builtin_convertvector(v, bf2_t));
}
__device__ __forceinline__ float asf(unsigned u) { return __builtin_bit_cast(float, u); }
__device__ __forceinline__ float bflo(unsigned u) { return __builtin_bit_cast(float, u << 16); }
__device__ __forceinline__ float bfhi(unsigned u) { return __builtin_bit_cast(float, u & 0xffff0000u); }
__device__ __forceinline__ float rbf(float x) { return (float)(__bf16)x; }
typedef short v2s __attribute__((ext_vector_type(2)));
__device__ __forceinline__ unsigned relu2(unsigned u) {
  return __builtin_bit_cast(unsigned, __builtin_elementwise_max(__builtin_bit_cast(v2s, u), v2s{0, 0}));
}
__device__ __forceinline__ f32x4 unpk(unsigned a, unsigned b) { return f32x4{bflo(a), bfhi(a), bflo(b), bfhi(b)}; }
__device__ __forceinline__ float ln_rstd(float var) {
  return __builtin_amdgcn_rcpf(rbf(__builtin_amdgcn_sqrtf(rbf(rbf(var) + 1e-5f))));
}
__device__ __forceinline__ f32x4 mfma(v4u a, v4u b, f32x4 c) {
  return __builtin_amdgcn_mfma_f32_16x16x32_bf16(__builtin_bit_cast(bf16x8, a), __builtin_bit_cast(bf16x8, b), c, 0, 0, 0);
}
template <int CTRL> __device__ __forceinline__ float dpp_add(float x) {
  return x + __builtin_bit_cast(float, __builtin_amdgcn_update_dpp(0, __builtin_bit_cast(int, x), CTRL, 0xf, 0xf, true));
}
__device__ __forceinline__ float row16_sum(float x) {
  return dpp_add<0x121>(dpp_add<0x122>(dpp_add<0x124>(dpp_add<0x128>(x))));
}
__device__ __forceinline__ float swap16_sum(float x) {
  unsigned a = __builtin_bit_cast(unsigned, x), b = a;
  asm("" : "+v"(b));
  auto r = __builtin_amdgcn_permlane16_swap(a, b, false, false);
  return asf(r[0]) + asf(r[1]);
}
__device__ __forceinline__ float swap32_sum(float x) {
  unsigned a = __builtin_bit_cast(unsigned, x), b = a;
  asm("" : "+v"(b));
  auto r = __builtin_amdgcn_permlane32_swap(a, b, false, false);
  return asf(r[0]) + asf(r[1]);
}
__device__ __forceinline__ float rows_sum(float x) { return swap32_sum(swap16_sum(x)); }
__device__ __forceinline__ float half_sum(float x) { return swap16_sum(row16_sum(x)); }
#define SCHED() __builtin_amdgcn_sched_barrier(0)
#define WAITV(n) asm volatile("s_waitcnt vmcnt(%0)" ::"i"(n) : "memory")
#define WAITV_BAR(n) asm volatile("s_waitcnt vmcnt(%0) lgkmcnt(0)\n\ts_barrier" ::"i"(n) : "memory")
#define BAR() asm volatile("s_waitcnt lgkmcnt(0)\n\ts_barrier" ::: "memory")
#define ACC4(m, m0, v) do { m = sum4(m0, v); asm volatile("" : "+v"(m)); } while (0)
#define PIN4(a, b, c, d) asm volatile("" : "+v"(a), "+v"(b), "+v"(c), "+v"(d))
#define PIN4A(a, b, c, d) asm volatile("" : "+a"(a), "+a"(b), "+a"(c), "+a"(d))

#define LD16(d, vo, sb, imm) asm volatile("global_load_dwordx4 %0, %1, %2 offset:%3" : "=v"(d) : "v"(vo), "s"(sb), "i"(imm) : "memory")
#define LD16C(d, vo, sb, imm) asm volatile("global_load_dwordx4 %0, %1, %2 offset:%3 sc1" : "=v"(d) : "v"(vo), "s"(sb), "i"(imm) : "memory")
#define LD16CA(d, vo, sb, imm) asm volatile("global_load_dwordx4 %0, %1, %2 offset:%3 sc1" : "=a"(d) : "v"(vo), "s"(sb), "i"(imm) : "memory")
#define LD4C(d, vo, sb, imm) asm volatile("global_load_dword %0, %1, %2 offset:%3 sc1" : "=v"(d) : "v"(vo), "s"(sb), "i"(imm) : "memory")
#define ST16C(v, vo, sb, imm) asm volatile("global_store_dwordx4 %0, %1, %2 offset:%3 sc1\n\ts_nop 1" ::"v"(vo), "v"(v), "s"(sb), "i"(imm) : "memory")
#define ST16(v, vo, sb, imm) asm volatile("global_store_dwordx4 %0, %1, %2 offset:%3\n\ts_nop 1" ::"v"(vo), "v"(v), "s"(sb), "i"(imm) : "memory")
#define ST4C(v, vo, sb, imm) asm volatile("global_store_dword %0, %1, %2 offset:%3 sc1" ::"v"(vo), "v"(v), "s"(sb), "i"(imm) : "memory")
#define RLD16(d, vo, sb, imm) asm volatile("global_load_dwordx4 %0, %1, %2 offset:%3 sc1" ::"v"(d), "v"(vo), "s"(sb), "i"(imm) : "memory")
#define RLD4(d, vo, sb, imm) asm volatile("global_load_dword %0, %1, %2 offset:%3 sc1" ::"v"(d), "v"(vo), "s"(sb), "i"(imm) : "memory")
#define CK_INIT(a, T, MASK) asm volatile("v_sub_u32 %0, %2, %1\n\tv_cndmask_b32 %0, 0, %0, %3" : "=&v"(a) : "v"(T), "s"(tag), "s"((unsigned long long)(MASK)))
#define CK_RED(a, i) do { a = isum<i>(a); asm volatile("" : "+v"(a)); } while (0)
#define CK_END(sv, a) do { sv = last_lane(a); asm volatile("" : "+s"(sv)); } while (0)
#define VADD4(a, f) asm volatile("v_add3_u32 %0, %0, %1, %2\n\tv_add3_u32 %0, %0, %3, %4" : "+v"(a) : "v"((f)[0]), "v"((f)[1]), "v"((f)[2]), "v"((f)[3]))
#define CK_ALL(sv, T, MASK, F) do { unsigned a_; CK_INIT(a_, T, MASK); VADD4(a_, F[0]); VADD4(a_, F[1]); VADD4(a_, F[2]); VADD4(a_, F[3]);   \
                                    asm volatile("s_nop 1" : "+v"(a_)); sv = last_lane(wave_isum(a_)); } while (0)
#define WRONG 0
#define TOUCH(vo, sb) asm volatile("global_load_dword %0, %1, %2" : "+v"(sink) : "v"(vo), "s"(sb) : "memory")
#define COUNT(i)
#define ST_TAG(v, p) asm volatile("s_mov_b64 exec, 0xff\n\tglobal_store_dwordx4 %0, %1, %2 sc1\n\ts_mov_b64 exec, -1\n\ts_nop 0" ::"v"(vl16), "v"(v), "s"(p) : "memory")
#define GLDS(vo, sb, lds, imm) asm volatile("s_mov_b32 m0, %2\n\ts_nop 0\n\tglobal_load_lds_dwordx4 %0, %1 offset:%3" ::"v"(vo), "s"(sb), "s"(lds), "i"(imm) : "memory")
#define GLDSC(vo, sb, lds, imm) asm volatile("s_mov_b32 m0, %2\n\ts_nop 0\n\tglobal_load_lds_dwordx4 %0, %1 offset:%3 sc1" ::"v"(vo), "s"(sb), "s"(lds), "i"(imm) : "memory")

__device__ __forceinline__ unsigned sum4(unsigned m, v4u v) { return (m + v[0] + v[1]) + v[2] + v[3]; }
template <int I> __device__ __forceinline__ unsigned isum(unsigned x) {
  constexpr int ctrl = I < 4 ? 0x110 + (1 << I) : 0x142 + (I - 4), rm = I < 4 ? 0xf : I == 4 ? 0xa : 0xc;
  return x + (unsigned)__builtin_amdgcn_update_dpp(0, (int)x, ctrl, rm, 0xf, I < 4);
}
__device__ __forceinline__ unsigned row_isum(unsigned x) { return isum<3>(isum<2>(isum<1>(isum<0>(x)))); }
__device__ __forceinline__ unsigned wave_isum(unsigned x) { return isum<5>(isum<4>(row_isum(x))); }
__device__ __forceinline__ unsigned last_lane(unsigned x) { return (unsigned)__builtin_amdgcn_readlane((int)x, 63); }
__device__ __forceinline__ void poll_flags(cptr base, unsigned vo, unsigned tag) {
  for (unsigned spin = 0; spin < SPIN_CAP; ++spin) {
    unsigned v;
    asm volatile("global_load_dword %0, %1, %2 sc1\n\ts_waitcnt vmcnt(0)" : "=&v"(v) : "v"(vo), "s"(base) : "memory");
    if (__all(v == tag)) break;
  }
}
template <class H>
__device__ __forceinline__ void last_step_reduce(unsigned char* red, int w, int lane, const v4u (&wt)[4], const v4u (&fc)[4],
                                                 f32x4 (&acc)[4][4], v4u (&o)[2], H&& hook) {
#pragma unroll
  for (int c = 1; c <= 4; ++c) {
#pragma unroll
    for (int i = 0; i < 4; ++i) {
      acc[i][c & 3] = mfma(wt[i], fc[c & 3], acc[i][c & 3]);
      SCHED();
      if (c >= 2) *(f32x4*)(red + (((c - 1) ^ w) * 3 + c - 2) * 4096 + lane * 16 + i * 1024) = acc[i][c - 1];
      SCHED();
    }
  }
  hook();
  BAR();
  const unsigned char* s = red + w * 12288 + lane * 16;
  f32x4 pt[3][4];
#pragma unroll
  for (int i = 0; i < 4; ++i)
#pragma unroll
    for (int c = 0; c < 3; ++c) pt[c][i] = *(const f32x4*)(s + c * 4096 + i * 1024);
  SCHED();
#pragma unroll
  for (int i = 0; i < 4; ++i) {
    const f32x4 y = (acc[i][0] + pt[0][i]) + (pt[1][i] + pt[2][i]);
    o[i >> 1][2 * (i & 1)] = pk2(y[0], y[1]);
    o[i >> 1][2 * (i & 1) + 1] = pk2(y[2], y[3]);
  }
}

#define NP0 8
#define FL0_CHK 19
#define W1A 0
#define W1C 8
#define GATE 7
#define W2A 4
#define W2C 12
#define WDS 12
#define XT0 20
#define DB 5
#define XTP 1
#define XTD 3
#define BP 4
#define RAN 1
#define D2 4
#define D4 5
#define FL0_PUB 11
constexpr int FL0_IT = FL0_CHK - 3, NV = 32, NX = 42 - WDS;

template <int I, int N, class F> __device__ __forceinline__ void static_for(F&& f) {
  if constexpr (I < N) { f(std::integral_constant<int, I>{}); static_for<I + 1, N>(f); }
}
constexpr bool p1_xt(int s) { return (s & 7) == 7 - XTD && s + XTD + 1 < NV; }
constexpr bool p1_bar(int s) { return (s + 1) % BP == 0; }
constexpr bool p1_ra(int s) { return s >= BP - 1 && s < 15 + BP && (s + 1) % BP < BP / RAN; }
constexpr int p1_as(int s) { return 16 + ((s + 1) / BP - 1) * BP + ((s + 1) % BP) * RAN; }
constexpr int p1_xbase(int s) { int n = 0; for (int u = XT0; u < s; ++u) n += p1_ra(u) ? 0 : (u + DB < 32 ? 2 : 6); return n > NX ? NX : n; }
constexpr int p1_extra(int s) { return s < XT0 ? 0 : p1_xbase(s + 1) - p1_xbase(s); }
static_assert(p1_xbase(32) == NX && XT0 > FL0_CHK && p1_bar(FL0_CHK) && p1_bar(FL0_PUB) && p1_bar(7) && XTP < DB && DB <= 7 && RAN <= 2, "");
constexpr int p1_wait(int it, bool mid) {
  int posA[48] = {}, posB[40] = {}, posF = -1, n = 0, pos0 = 4 * DB, posT = -1;
  for (int s = 0; s < 48; ++s) posA[s] = -1;
  for (int s = 0; s < DB; ++s) { n += 4; posB[s] = n - 1; }
  if (it < 0) return n - 1 - posB[XTP];
  for (int s = 0; s <= it; ++s) {
    if (s == FL0_IT && s < it) posF = n - 1;
    if (p1_xt(s) && s < it) posT = n - 1;
    if (s == it) break;
    if (p1_ra(s)) for (int u = 0; u < RAN; ++u) { n += 2; posA[p1_as(s) + u] = n - 1; }
    n += p1_extra(s);
    if (s + DB < 32) { n += 4; posB[s + DB] = n - 1; }
  }
  int req = -1;
  if (mid) req = posB[it + 1];
  else {
    if (it + BP < 32) req = posA[it + BP];
    if (it == FL0_CHK && posF > req) req = posF;
    if (it == FL0_PUB && pos0 > req) req = pos0;
    if ((it & 7) == 7 && it + 1 < NV && posT > req) req = posT;
  }
  return n - 1 - req > 63 ? 63 : n - 1 - req;
}
constexpr int p2_w1(int s) { int n = 28 - W1A - 4 * (s - (8 - D2)); return (s + D2 < 8 || s == 7) ? 0 : n > 4 ? 4 : n < 0 ? 0 : n; }
constexpr int pk_wait(int k, int D, int NG) {
  int n = (k < D && k >= 2) ? 1 : 0;
  for (int g = k + 1; g < NG && g < k - 1 + D; ++g) n += 4;
  return n;
}
constexpr int p2_wait(int k) { int n = pk_wait(k, D2, 8); for (int s = 0; s < k - 1; ++s) n += p2_w1(s); return n; }
constexpr int p4_wait(int k) { return pk_wait(k, D4, 14); }
constexpr int W2B = 56 - W2A - W2C;
constexpr int p3_w2(int s) { int n = W2B - 4 * (s - 5); return s < 5 ? 0 : n > 4 ? 4 : n < 0 ? 0 : n; }
constexpr int p3_ops(int s) { return s == 0 ? 5 : s < 5 ? 4 : p3_w2(s); }
constexpr int p3_wait(int m) {
  int n = m < 4 ? 4 * (3 - m) : 0;
  for (int s = (m < 4 ? 0 : m - 3); s < 2 * m - 1; ++s) n += p3_ops(s);
  return n;
}
constexpr int P3_ST = 10;
constexpr int p3_stwait() { int n = 0; for (int s = 5; s < P3_ST; ++s) n += p3_ops(s); return n; }
static_assert(W2B >= 0 && W2B <= 44 && W1A >= 0 && W1A <= 28 && W1A % 4 == 0, "");

__global__ __launch_bounds__(256, 1) void fnet_fused(cptr x, cptr wsre, cptr wsim, cptr wre, cptr wim, cptr w1, cptr w2,
                                                     unsigned char* ws, unsigned char* out) {
  extern __shared__ __attribute__((aligned(16))) unsigned char smem[];
  const int bid = blockIdx.x, p = bid & 7, j = bid >> 3;
  const int ti = 4 * p + (j >> 3), cg = j & 7, t128 = 2 * p + (j >> 4), fs = j & 15;
  const int tid = threadIdx.x, lane = tid & 63, w = __builtin_amdgcn_readfirstlane(tid >> 6), q = lane >> 4, r = lane & 15;
  const unsigned lds0 = (unsigned)(size_t)(LAS unsigned char*)smem;
  const unsigned vl16 = lane * 16;
  unsigned cap = SPIN_CAP;
  unsigned long long ta_ = 0, tb_ = 0;

  const int mat = (j >> 2) & 1, nt = j & 3;
  unsigned epoch, sink = 0;
  v4u xr[2];
  {
    const unsigned vo = (lane >> 5) * 1024 + (lane & 31) * 16, zero = 0;
    cptr xb = x + (size_t)(8 * bid + 2 * w) * 1024;
    LD16(xr[0], vo, xb, 0);
    LD16(xr[1], vo, xb, 512);
    LD4C(epoch, zero, ws + EPOCH_OFF, 0);
  }
  const unsigned va0 = (lane >> 3) * 4096 + (((lane & 7) ^ (lane >> 4)) << 4), va1 = (va0 ^ 64) + 32768 - 1024;
  const int rot = nt & 1;
  cptr abase = (mat ? wsim : wsre) + (size_t)(64 * ti + 16 * w) * 4096;
  cptr abase0 = abase + rot * 2048, abase1 = abase - rot * 2048;
  const unsigned lda = lds0 + w * 2048;
#define A_STAGE0(s) GLDS(va0, ((s) < 16 ? abase0 : abase1) + (s) * 128, lda + ((s) & 15) * 8192, 0)
#define A_STAGE1(s) GLDS(va1, ((s) < 16 ? abase0 : abase1) + (s) * 128, lda + ((s) & 15) * 8192, 1024)
  const int kb1 = bid >> 4, fs1 = bid & 15, kbd = bid & 31, kb2 = 2 * (kbd < 28 ? kbd : 27) + (w & 1);
  {
    cptr pc;
    unsigned len;
    asm volatile("s_getpc_b64 %0\n.Lfnet_pc:\n\ts_mov_b32 %1, .Lfnet_last-.Lfnet_pc" : "=s"(pc), "=s"(len));
    const unsigned vo = min(((4 * j + w) * 4 + (lane & 3)) * 128u, len);
    TOUCH(vo, pc);
  }
  const int wst = WST_OFF + w * 5952;
  v4u wdp;
  auto wload = [&]() {
    const unsigned rs = w < 2 ? D_ * 2 : F_ * 2, vs = (lane >> 3) * rs + (lane & 7) * 16;
    cptr sb = w < 2 ? w2 + (size_t)(32 * kb2) * (D_ * 2) + (bid >> 5) * 128
                    : w1 + (size_t)(32 * kb1) * (F_ * 2) + ((7 * fs1) >> 1) * 64 + (w - 2) * 128;
#pragma unroll
    for (int k = 0; k < 4; ++k) GLDS(vs, sb + k * 8 * rs, lds0 + wst + k * 1088, 0);
    const unsigned vo = (8 * (r >> 2) + (r & 3)) * 1024 + q * 16;
    LD16(wdp, vo, (kbd < 16 ? wre : wim) + (size_t)(64 * (bid >> 5) + 32 * (w >> 1) + 4 * (w & 1)) * 1024 + (kbd & 15) * 64, 0);
  };
  auto wstore = [&]() {
    if (kbd >= 16) wdp ^= 0x80008000u;
    ST16C(wdp, vl16, ws + WD_OFF + bid * 4096 + w * 1024, 0);
    const unsigned char* s = smem + wst + q * 1088 + ((lane >> 2) & 3) * 128 + 16 * (lane & 3);
    unsigned char* db = w < 2 ? ws + W2_OFF + ((bid >> 5) * 56 + kb2) * 4096 : ws + W1_OFF + fs1 * 114688 + kb1 * 7168;
    float csv = 0.f;
#pragma unroll
    for (int i = 0; i < 4; ++i) {
      const int tl = 4 * (w - 2) + i, l = w < 2 ? (kbd < 28 ? i : 7) : (fs1 & 1) ? (tl == 0 ? 7 : tl == 1 ? 6 : tl - 2) : tl;
      const unsigned char* sl = s + (i >> 1) * 64 + (i & 1) * 8;
      const v2u lo = __builtin_bit_cast(v2u, __builtin_amdgcn_ds_read_tr16_b64_v4i16((LAS v4s*)sl));
      const v2u hi = __builtin_bit_cast(v2u, __builtin_amdgcn_ds_read_tr16_b64_v4i16((LAS v4s*)(sl + 512)));
      const v4u o = {lo[0], lo[1], hi[0], hi[1]};
      if (l < 7) ST16C(o, vl16, db + l * 1024, 0);
      const float cs = rows_sum(((bflo(o[0]) + bfhi(o[0])) + (bflo(o[1]) + bfhi(o[1]))) + ((bflo(o[2]) + bfhi(o[2])) + (bflo(o[3]) + bfhi(o[3]))));
      csv = (q == i) ? cs : csv;
    }
    const unsigned vc = r * 16 + q * 4, chl = pk2(csv, csv - rbf(csv));
    if (w >= 2) ST4C(chl, vc, ws + CS_OFF + fs1 * 8192 + (w - 2) * 4096 + kb1 * 256, 0);
  };
#pragma unroll 1
  for (int s = 0; s < NP0; ++s) { A_STAGE0(s); A_STAGE1(s); }
  WAITV(2 * NP0 + 1);
  asm volatile("" : "+v"(xr[0]), "+v"(xr[1]), "+v"(epoch));
  const unsigned tag = __builtin_amdgcn_readfirstlane(epoch) + 1u;

  {
    float v[16];
#pragma unroll
    for (int i = 0; i < 2; ++i)
#pragma unroll
      for (int k = 0; k < 4; ++k) { v[8 * i + 2 * k] = bflo(xr[i][k]); v[8 * i + 2 * k + 1] = bfhi(xr[i][k]); }
    float s1[4] = {0.f, 0.f, 0.f, 0.f};
#pragma unroll
    for (int k = 0; k < 16; ++k) s1[k & 3] += v[k];
    const float mean = half_sum((s1[0] + s1[1]) + (s1[2] + s1[3])) * (1.0f / D_);
    float s2[4] = {0.f, 0.f, 0.f, 0.f};
#pragma unroll
    for (int k = 0; k < 16; ++k) { const float c = v[k] - mean; s2[k & 3] += c * c; }
    const float mb = rbf(mean), rd = ln_rstd(half_sum((s2[0] + s2[1]) + (s2[2] + s2[3])) * (1.0f / D_));
#pragma unroll
    for (int i = 0; i < 2; ++i) {
      v4u o;
#pragma unroll
      for (int k = 0; k < 4; ++k) {
        const unsigned c2 = pk2(v[8 * i + 2 * k] - mb, v[8 * i + 2 * k + 1] - mb);
        o[k] = pk2(bflo(c2) * rd, bfhi(c2) * rd);
      }
      *(v4u*)(smem + XS_OFF + (2 * w + (lane >> 5)) * XS_ROW + (lane & 31) * 16 + i * 512) = o;
    }
  }
  BAR();

  auto tr8 = [&](int off, int i) {
    const unsigned char* s = smem + off + ((lane >> 2) & 3) * XS_ROW + 64 * (q >> 1) + 16 * (lane & 3) + 8 * (q & 1) + 128 * i;
    const v2u lo = __builtin_bit_cast(v2u, __builtin_amdgcn_ds_read_tr16_b64_v4i16((LAS v4s*)s));
    const v2u hi = __builtin_bit_cast(v2u, __builtin_amdgcn_ds_read_tr16_b64_v4i16((LAS v4s*)(s + 4 * XS_ROW)));
    return v4u{lo[0], lo[1], hi[0], hi[1]};
  };
  {
    const v4u o0 = tr8(XS_OFF, 2 * w), o1 = tr8(XS_OFF, 2 * w + 1);
    const unsigned vo = (q >> 1) * 131072 + (q & 1) * 1024 + r * 16;
    unsigned char* sb = ws + XNT_OFF + (bid >> 2) * 2048 + (bid & 3) * 256 + w * 524288;
    ST16C(o0, vo, sb, 0);
    ST16C(o1, vo, sb + 262144, 0);
    auto sw = __builtin_amdgcn_permlane16_swap(sum4(0u, o0), sum4(0u, o1), false, false);
    const int ck = (int)row_isum(sw[0] + sw[1]);
    const v4u c4 = {tag + __builtin_amdgcn_readlane(ck, 15), tag + __builtin_amdgcn_readlane(ck, 47),
                    tag + __builtin_amdgcn_readlane(ck, 31), tag + __builtin_amdgcn_readlane(ck, 63)};
    ST_TAG(c4, ws + CKX_OFF + bid * 512 + w * 128);
  }
  wload();
#pragma unroll 1
  for (int s = NP0; s < 16; ++s) { A_STAGE0(s); A_STAGE1(s); }

  cptr bbase0 = ws + XNT_OFF + (4 * nt + w) * 131072 + rot * 65536, bbase1 = bbase0 - rot * 131072;
  cptr xcb = ws + CKX_OFF + nt * 128;
#define BB(s) ((s) < 16 ? bbase0 : bbase1)
#define XCB(r8) (xcb + (((r8) + 2 * rot) & 3) * 32768)
  const unsigned vl512 = lane * 512;
  v4u bf[8][4];
  unsigned xt[(NV + 7) / 8];
#define B_LOAD(s, i) LD16C(bf[(s) & 7][i], vl16, BB(s) + (s) * 4096 + ((s) < NV ? WRONG : 0), (i) * 1024)
#pragma unroll
  for (int s = 0; s < DB; ++s) {
    B_LOAD(s, 0); B_LOAD(s, 1); B_LOAD(s, 2); B_LOAD(s, 3);
    if (s == XTP && w == 0) GLDSC(vl512, XCB(0), lds0 + XT_OFF, 0);
  }

  asm volatile("s_waitcnt vmcnt(%2)" : "+v"(wdp), "+v"(sink) : "i"(4 * DB) : "memory");
  wstore();
  f32x4 acc1[2][4];
#pragma unroll
  for (int i = 0; i < 2; ++i)
#pragma unroll
    for (int t = 0; t < 4; ++t) acc1[i][t] = f32x4{0.f, 0.f, 0.f, 0.f};
  const unsigned ra0 = r * 128 + ((q ^ (r >> 1)) << 4), ra1 = ra0 ^ 64;
  v4u af[2][8];
  v4u xres[2], csr[8], wd[8][4];
  unsigned flw = tag;
  const v4u tag4 = {tag, tag, tag, tag};
  const unsigned vfl = (lane < 32 ? 32 * cg + lane : 16 * r + fs) * 128;
  cptr xb2 = x + (size_t)(64 * ti + 16 * w) * 1024 + cg * 128;
  const unsigned vx = r * 1024 + q * 16;
  cptr cb2 = ws + CS_OFF + fs * 8192;
  const unsigned vc2 = q * 1024 + r * 16;
  cptr wb = ws + WD_OFF + cg * 131072 + w * 32768;
#define BAD(M) (__builtin_amdgcn_ballot_w64((M) != 0u) != 0)
#define WAIT4(F, N, TAIL) asm volatile("s_waitcnt vmcnt(%4)" TAIL : "+v"(F[0]), "+v"(F[1]), "+v"(F[2]), "+v"(F[3]) : "i"(N) : "memory")
#define XMASK(s) (0xffull << (8 * ((s) & 7)))
  WAIT4(bf[0], p1_wait(-1, false), " lgkmcnt(0)\n\ts_barrier");
#define XT_READ(i) do { xt[i] = *(const unsigned*)(smem + XT_OFF + (i) * 5952 + vl16 + w * 4); asm volatile("" : "+v"(xt[i])); } while (0)
  XT_READ(0);
#pragma unroll
  for (int t = 0; t < 4; ++t) { af[0][t] = *(const v4u*)(smem + ra0 + t * 2048); af[0][4 + t] = *(const v4u*)(smem + ra1 + t * 2048); }
  unsigned bx, bs;
  CK_ALL(bs, xt[0], XMASK(0), bf[0]);

#define P1_EXTRA(e)                                                                                                     \
  if constexpr ((e) < p1_extra(it)) {                                                                                   \
    constexpr int n_ = p1_xbase(it) + (e);                                                                              \
    if constexpr (n_ < 2) LD16(xres[n_], vx, xb2, n_ * 64);                                                             \
    else if constexpr (n_ < 10) LD16C(csr[n_ - 2], vc2, cb2 + ((n_ - 2) >> 2) * 4096, ((n_ - 2) & 3) * 256);            \
    else LD16CA(wd[(n_ - 10) >> 2][(n_ - 10) & 3], vl16, wb + ((n_ - 10) >> 2) * 4096, ((n_ - 10) & 3) * 1024);         \
  }
#define MM1(i, t, kb) acc1[i][t] = mfma(bf[U][2 * (kb) + (i)], af[C][4 * (kb) + (t)], acc1[i][t]); SCHED()
#define RD1(n) if constexpr (RN) { af[Nx][n] = *(const v4u*)(sn + ((n) < 4 ? ra0 : ra1) + ((n) & 3) * 2048); SCHED(); }
#define CK1(what) if constexpr (it + 1 < NV) { what; SCHED(); }
  static_for<0, 32>([&](auto IT) {
    constexpr int it = IT, U = it & 7, Un = (it + 1) & 7, C = it & 1, Nx = C ^ 1;
    constexpr bool RA = p1_ra(it), RB = it + DB < 32, RN = it + 1 < 32;
    if constexpr (it < NV) {
      if (__builtin_expect(bs != 0, 0)) {
        constexpr int hi0 = it + DB < NV ? it + DB : NV, hi = hi0 < (it / 8 + 1) * 8 ? hi0 : (it / 8 + 1) * 8;
        unsigned spin = cap;
        WAITV(0);
        do {
          COUNT(0);
#pragma unroll
          for (int g = it; g < hi0; ++g)
#pragma unroll
            for (int c = 0; c < 4; ++c) RLD16(bf[g & 7][c], vl16, BB(g) + g * 4096, c * 1024);
          RLD4(xt[it / 8], vl512, XCB(it / 8) + w * 4, 0);
          WAITV(0);
          unsigned bad = 0;
#pragma unroll
          for (int g = it; g < hi; ++g) { unsigned v_; CK_ALL(v_, xt[g / 8], XMASK(g), bf[g & 7]); bad |= v_; }
          if (bad == 0) break;
          if (--spin == 0) { cap = 1; break; }
        } while (true);
      }
    }
    if constexpr (it == FL0_CHK) {
      asm volatile("s_waitcnt vmcnt(%1)" : "+v"(flw) : "i"(p1_wait(it, false)) : "memory");
      if (!__all(flw == tag)) { COUNT(1); poll_flags(ws + FLAG0_OFF, vfl, tag); }
    }
    if constexpr (p1_bar(it)) WAITV_BAR(p1_wait(it, false));
    if constexpr ((it & 7) == 7 && it + 1 < NV) XT_READ((it + 1) / 8);
    if constexpr (p1_xt(it)) { if (w == it / 8 + 1) GLDSC(vl512, XCB(it / 8 + 1), lds0 + XT_OFF + (it / 8 + 1) * 5952, 0); }
    if constexpr (it == FL0_PUB) { if (w == 0) ST_TAG(tag4, ws + FLAG0_OFF + bid * 128); }
    if constexpr (it == FL0_IT)
      asm volatile("s_cmp_lg_u32 %3, 0\n\ts_cbranch_scc1 1f\n\tglobal_load_dword %0, %1, %2 sc1\n1:"
                   : "+v"(flw) : "v"(vfl), "s"(ws + FLAG0_OFF), "s"(w) : "memory", "scc");
    const unsigned char* sn = smem + ((it + 1) & 15) * 8192;
    SCHED();
    MM1(0, 0, 0); RD1(0); if constexpr (RN) { WAIT4(bf[Un], p1_wait(it, true), ""); SCHED(); }
    MM1(1, 0, 0); if constexpr (RA) A_STAGE0(p1_as(it)); P1_EXTRA(0) SCHED();
    MM1(0, 1, 0); RD1(1); if constexpr (RA && RAN == 2) { A_STAGE0(p1_as(it) + 1); SCHED(); } CK1(CK_INIT(bx, xt[(it + 1) / 8], XMASK(it + 1)));
    MM1(1, 1, 0); RD1(2); if constexpr (RA && RAN == 2) { A_STAGE1(p1_as(it)); SCHED(); } CK1(ACC4(bx, bx, bf[Un][0]));
    MM1(0, 2, 0); if constexpr (RA) A_STAGE1(p1_as(it) + RAN - 1); P1_EXTRA(1) SCHED();
    MM1(1, 2, 0); RD1(3); CK1(ACC4(bx, bx, bf[Un][1]));
    MM1(0, 3, 0); if constexpr (RB) B_LOAD(it + DB, 0); P1_EXTRA(2) SCHED(); CK1(ACC4(bx, bx, bf[Un][2]));
    MM1(1, 3, 0); RD1(4); CK1(ACC4(bx, bx, bf[Un][3]));
    MM1(0, 0, 1); RD1(5); CK1(CK_RED(bx, 0));
    MM1(1, 0, 1); if constexpr (RB) B_LOAD(it + DB, 1); P1_EXTRA(3) SCHED(); CK1(CK_RED(bx, 1));
    MM1(0, 1, 1); RD1(6); CK1(CK_RED(bx, 2));
    MM1(1, 1, 1); if constexpr (RB) B_LOAD(it + DB, 2); P1_EXTRA(4) SCHED(); CK1(CK_RED(bx, 3));
    MM1(0, 2, 1); RD1(7); CK1(CK_RED(bx, 4));
    MM1(1, 2, 1); if constexpr (RB) B_LOAD(it + DB, 3); P1_EXTRA(5) SCHED(); CK1(CK_RED(bx, 5));
    MM1(0, 3, 1); CK1(CK_END(bs, bx));
    MM1(1, 3, 1);
  });
  asm volatile("s_memrealtime %0\n\ts_waitcnt lgkmcnt(0)" : "=s"(ta_) :: "memory");
  {
    const int kbq = ti * 32 + 16 * mat + 4 * nt + w;
    unsigned char* pq = ws + PQ_OFF + kbq * 4096;
    unsigned ck = 0;
#pragma unroll
    for (int t = 0; t < 4; ++t) {
      const v4u o = {pk2(acc1[0][t][0], acc1[0][t][1]), pk2(acc1[0][t][2], acc1[0][t][3]),
                     pk2(acc1[1][t][0], acc1[1][t][1]), pk2(acc1[1][t][2], acc1[1][t][3])};
      ST16C(o, vl16, pq + t * 1024, 0);
      ck = sum4(ck, o);
    }
    ck = tag + last_lane(wave_isum(ck));
    const v4u c4 = {ck, ck, ck, ck};
    ST_TAG(c4, ws + CKP_OFF + kbq * 128);
    if (bid == 0 && w == 0) ST_TAG(tag4, ws + EPOCH_OFF);
  }
#pragma unroll
  for (int n = 32 - WDS; n < 32; ++n) LD16CA(wd[n >> 2][n & 3], vl16, wb + (n >> 2) * 4096, (n & 3) * 1024);
  cptr w1src = ws + W1_OFF + fs * 114688 + w * 28672;
  const unsigned w1dst = lds0 + w * 28672;
#define W1L(n) GLDSC(vl16, w1src + ((n) >> 2) * 4096, w1dst + ((n) >> 2) * 4096, ((n) & 3) * 1024)
#pragma unroll 1
  for (int n = 0; n < W1A / 4; ++n) {
    GLDSC(vl16, w1src + n * 4096, w1dst + n * 4096, 0);
    GLDSC(vl16, w1src + n * 4096, w1dst + n * 4096, 1024);
    GLDSC(vl16, w1src + n * 4096, w1dst + n * 4096, 2048);
    GLDSC(vl16, w1src + n * 4096, w1dst + n * 4096, 3072);
  }
  v4u csf[7];
  {
    static_assert(p1_xbase(26) >= 10, "the wait for the last XNT stage covers the column-sum loads");
    PIN4(csr[0], csr[1], csr[2], csr[3]);
    PIN4(csr[4], csr[5], csr[6], csr[7]);
    const bool odd = fs & 1;
#pragma unroll
    for (int l = 0; l < 7; ++l) {
      const int te = l, to = l < 6 ? l + 2 : 1;
#pragma unroll
      for (int i = 0; i < 4; ++i) csf[l][i] = odd ? csr[(to >> 2) * 4 + i][to & 3] : csr[(te >> 2) * 4 + i][te & 3];
      asm volatile("" : "+a"(csf[l]));
    }
  }

#define MM64(i, c) acc[i][c] = mfma(WT_[i], FC_[c], acc[i][c]); SCHED()
#define STEP64(WT, FC, FN, WAITN, T, MASK, OP)                                                                          \
  {                                                                                                                     \
    auto& WT_ = WT; auto& FC_ = FC;                                                                                     \
    SCHED();                                                                                                            \
    MM64(0, 0); WAITN; SCHED();                                                                                         \
    MM64(0, 1); OP(0); SCHED();                                                                                         \
    MM64(0, 2); CK_INIT(mx, T, MASK); SCHED();                                                                          \
    MM64(0, 3); ACC4(mx, mx, FN[0]); SCHED();                                                                           \
    MM64(1, 0); ACC4(mx, mx, FN[1]); SCHED();                                                                           \
    MM64(1, 1); OP(1); SCHED();                                                                                         \
    MM64(1, 2); ACC4(mx, mx, FN[2]); SCHED();                                                                           \
    MM64(1, 3); ACC4(mx, mx, FN[3]); SCHED();                                                                           \
    MM64(2, 0); CK_RED(mx, 0); SCHED();                                                                                 \
    MM64(2, 1); OP(2); CK_RED(mx, 1); SCHED();                                                                          \
    MM64(2, 2); CK_RED(mx, 2); SCHED();                                                                                 \
    MM64(2, 3); CK_RED(mx, 3); SCHED();                                                                                 \
    MM64(3, 0); CK_RED(mx, 4); SCHED();                                                                                 \
    MM64(3, 1); OP(3); CK_RED(mx, 5); SCHED();                                                                          \
    MM64(3, 2); CK_END(ms, mx); SCHED();                                                                                \
    MM64(3, 3);                                                                                                         \
  }
#define WAIT4T(F, T, N) asm volatile("s_waitcnt vmcnt(%5)" : "+v"(F[0]), "+v"(F[1]), "+v"(F[2]), "+v"(F[3]), "+v"(T) : "i"(N) : "memory")

  unsigned vrot[4];
#pragma unroll
  for (int c = 0; c < 4; ++c) vrot[c] = vl16 + (c ^ w) * 1024;
  v4u hres[2];
  v4u w2f[14][4];
  cptr w2base = ws + W2_OFF + cg * 229376 + w * 57344;
#define W2L(n) do { if ((n) < 32) LD16CA(w2f[(n) >> 2][(n) & 3], vl16, w2base + ((n) >> 2) * 4096, ((n) & 3) * 1024);   \
                    else LD16C(w2f[(n) >> 2][(n) & 3], vl16, w2base + ((n) >> 2) * 4096, ((n) & 3) * 1024); } while (0)
  {
    f32x4 acc[4][4];
    v4u pq[8][4];
    unsigned tw[2];
    cptr pb = ws + PQ_OFF + ti * 131072 + w * 32768;
    cptr cb = ws + CKP_OFF + (ti * 32 + 8 * w) * 128;
    const unsigned vt = (lane & 7) * 128;
    if (GATE & 1) WAITV(WDS + W1A);
#pragma unroll
    for (int k = 0; k < D2; ++k) {
#pragma unroll
      for (int c = 0; c < 4; ++c) LD16C(pq[k][c], vrot[c], pb + k * 4096 + WRONG, 0);
      if (k == 0) LD4C(tw[0], vt, cb, 0);
    }
    asm volatile("s_waitcnt vmcnt(%2)" : "+v"(xres[0]), "+v"(xres[1]) : "i"(4 * D2 + 1) : "memory");
#pragma unroll
    for (int i = 0; i < 4; ++i) {
      acc[i][0] = unpk(xres[i >> 1][2 * (i & 1)], xres[i >> 1][2 * (i & 1) + 1]);
#pragma unroll
      for (int c = 1; c < 4; ++c) acc[i][c] = f32x4{0.f, 0.f, 0.f, 0.f};
    }
    WAIT4T(pq[0], tw[0], 4 * D2 - 4);
    unsigned mx, ms;
    CK_ALL(ms, tw[0], 1ull, pq[0]);

#define P2OP(i) do { if constexpr (k == 0 && (i) == 0) LD4C(tw[1], vt, cb, 0);                                           \
                     if constexpr (k + D2 < 8) LD16C(pq[(k + D2) & 7][i], vrot[i], pb + (k + D2) * 4096 + WRONG, 0);     \
                     else if constexpr ((i) < p2_w1(k)) W1L(W1A + 4 * (k + D2 - 8) + (i)); } while (0)
    static_for<0, 8>([&](auto K_) {
      constexpr int k = K_, kn = (k + 1) & 7;
      if (__builtin_expect(ms != 0, 0)) {
        constexpr int hi = k + D2 < 8 ? k + D2 : 8;
        unsigned spin = cap;
        WAITV(0);
        do {
          COUNT(1);
#pragma unroll
          for (int g = k; g < hi; ++g)
#pragma unroll
            for (int c = 0; c < 4; ++c) RLD16(pq[g][c], vrot[c], pb + g * 4096, 0);
          if constexpr (k < D2) RLD4(tw[0], vt, cb, 0);
          if constexpr (k > 0) RLD4(tw[1], vt, cb, 0);
          WAITV(0);
          unsigned bad = 0;
#pragma unroll
          for (int g = k; g < hi; ++g) { unsigned v_; CK_ALL(v_, tw[g >= D2], 1ull << g, pq[g]); bad |= v_; }
          if (bad == 0) break;
          if (--spin == 0) { cap = 1; break; }
        } while (true);
      }
      PIN4A(wd[k][0], wd[k][1], wd[k][2], wd[k][3]);
      if constexpr (k < 7) STEP64(wd[k], pq[k], pq[kn], WAIT4T(pq[kn], tw[kn >= D2], p2_wait(kn)), tw[kn >= D2], 1ull << kn, P2OP);
    });

    last_step_reduce(smem + RED_OFF, w, lane, wd[7], pq[7], acc, hres, [&]() {
#pragma unroll
      for (int n = W1A + 4 * (D2 - 1); n < 28 - W1C; ++n) W1L(n);
    });
    const int hp = (4 * ti + w) * 8 + cg;
    ST16C(hres[0], vl16, ws + H_OFF + hp * 2048, 0);
    ST16C(hres[1], vl16, ws + H_OFF + hp * 2048, 1024);
    const unsigned hck = tag + last_lane(wave_isum(sum4(sum4(0u, hres[0]), hres[1])));
    const v4u c4 = {hck, hck, hck, hck};
    ST_TAG(c4, ws + CKH_OFF + hp * 128);
    f32x4 hv[4];
#pragma unroll
    for (int i = 0; i < 4; ++i) hv[i] = unpk(hres[i >> 1][2 * (i & 1)], hres[i >> 1][2 * (i & 1) + 1]);
    const f32x4 s4 = (hv[0] + hv[1]) + (hv[2] + hv[3]);
    const float m = rows_sum((s4[0] + s4[1]) + (s4[2] + s4[3])) * (1.0f / 64.0f);
    f32x4 q4 = {0.f, 0.f, 0.f, 0.f};
#pragma unroll
    for (int i = 0; i < 4; ++i) { const f32x4 c = hv[i] - m; q4 += c * c; }
    const float m2 = rows_sum((q4[0] + q4[1]) + (q4[2] + q4[3]));
    const unsigned mu = __builtin_bit_cast(unsigned, m), m2u = __builtin_bit_cast(unsigned, m2);
    const v4u st = {mu, m2u, tag + mu + m2u, 0u};
    asm volatile("s_mov_b64 exec, 0xffff\n\tglobal_store_dwordx4 %0, %1, %2 sc1\n\ts_mov_b64 exec, -1\n\ts_nop 0" ::"v"(vl16), "v"(st), "s"(ws + ST_OFF + hp * 256) : "memory");
  }

#pragma unroll
  for (int n = 28 - W1C; n < 28; ++n) W1L(n);
#pragma unroll
  for (int n = 0; n < W2A; ++n) W2L(n);
  {
    v4u hf[8][4];
    unsigned tw[2];
    v4u str[2][2];
    cptr hb = ws + H_OFF + (8 * t128 + 2 * w) * 16384;
    cptr cb = ws + CKH_OFF + (8 * t128 + 2 * w) * 1024;
    const unsigned vt = r * 128;
    const unsigned vh1 = vl16 + 16384;
    const unsigned vs = q * 512 + r * 16;
    cptr stb = ws + ST_OFF + (8 * t128 + 2 * w) * 2048;
#define HL(m, i) LD16C(hf[m][i], ((i) & 1) ? vh1 : vl16, hb + ((m) >> 1) * 4096 + WRONG, ((m) & 1) * 2048 + ((i) >> 1) * 1024)
#define HMASK(m) (0x101ull << (m))
    if (GATE & 2) WAITV(W1C + W2A + 1);
#pragma unroll
    for (int m = 0; m < 4; ++m) { HL(m, 0); HL(m, 1); HL(m, 2); HL(m, 3); if (m == 0) LD4C(tw[0], vt, cb, 0); }
    f32x4 acc[7][2];
#pragma unroll
    for (int l = 0; l < 7; ++l) { acc[l][0] = f32x4{0.f, 0.f, 0.f, 0.f}; acc[l][1] = f32x4{0.f, 0.f, 0.f, 0.f}; }
    WAIT4T(hf[0], tw[0], 12);
    BAR();
    asm volatile("s_memrealtime %0\n\ts_waitcnt lgkmcnt(0)" : "=s"(tb_) :: "memory");
    unsigned whi_ = lds0 + vl16 + 57344;
    asm volatile("" : "+v"(whi_));
    const LAS unsigned char* wlo = (const LAS unsigned char*)smem + vl16;
    const LAS unsigned char* whi = (const LAS unsigned char*)(size_t)whi_;
#define W1F(k, l) (*(const LAS v4u*)(((k) < 8 ? wlo + (k) * 7168 : whi + ((k) - 8) * 7168) + (l) * 1024))
    v4u wf[2][7];
#pragma unroll
    for (int l = 0; l < 7; ++l) wf[0][l] = W1F(0, l);
    unsigned mx, ms;
    CK_ALL(ms, tw[0], HMASK(0), hf[0]);
    float rd[2], sm[2], sq[2];
    unsigned nmf[2];
    auto stat_piece = [&](auto N_) {
      constexpr int n = N_ >> 1, t = N_ & 1;
      if constexpr (n == 0) sm[t] = swap16_sum(asf(str[t][0][0]) + asf(str[t][1][0]));
      if constexpr (n == 1) sm[t] = swap32_sum(sm[t]) * 0.125f;
      if constexpr (n == 2) { const float da = asf(str[t][0][0]) - sm[t], db = asf(str[t][1][0]) - sm[t]; sq[t] = da * da + db * db; }
      if constexpr (n == 3) sq[t] = asf(str[t][0][1]) + asf(str[t][1][1]) + 64.0f * sq[t];
      if constexpr (n == 4) sq[t] = swap16_sum(sq[t]);
      if constexpr (n == 5) sq[t] = swap32_sum(sq[t]) * (1.0f / D_);
      if constexpr (n == 6) nmf[t] = pk2(-sm[t], -sm[t]);
      if constexpr (n == 7) sq[t] = rbf(sq[t]) + 1e-5f;
      if constexpr (n == 8) sq[t] = __builtin_amdgcn_sqrtf(rbf(sq[t]));
      if constexpr (n == 9) rd[t] = __builtin_amdgcn_rcpf(rbf(sq[t]));
      if constexpr (n < 2) asm volatile("" : "+v"(sm[t]));
      else if constexpr (n == 6) asm volatile("" : "+v"(nmf[t]));
      else if constexpr (n == 9) asm volatile("" : "+v"(rd[t]));
      else if constexpr (n < 9) asm volatile("" : "+v"(sq[t]));
    };
#define P3OP(i) do { if constexpr (k == 0 && (i) == 0) LD4C(tw[1], vt, cb, 0);                                           \
                     if constexpr (k < 4) HL((4 + k) & 7, i);                                                           \
                     else if constexpr (k == 4) LD16C(str[(i) >> 1][(i) & 1], vs, stb + WRONG, ((i) >> 1) * 2048 + ((i) & 1) * 256); \
                     else if constexpr ((i) < p3_w2(k)) W2L(W2A + 4 * (k - 5) + (i)); } while (0)
    static_for<0, 16>([&](auto K_) {
      constexpr int k = K_, m = k >> 1, mn = ((k + 1) >> 1) & 7;
      if ((k & 1) == 0 && __builtin_expect(ms != 0, 0)) {
        constexpr int hi = 4 + k < 8 ? 4 + k : 8;
        unsigned spin = cap;
        WAITV(0);
        do {
          COUNT(2);
#pragma unroll
          for (int g = m; g < hi; ++g)
#pragma unroll
            for (int c = 0; c < 4; ++c) RLD16(hf[g][c], (c & 1) ? vh1 : vl16, hb + g * 2048, (c >> 1) * 1024);
          if constexpr (m < 4) RLD4(tw[0], vt, cb, 0);
          if constexpr (m > 0) RLD4(tw[1], vt, cb, 0);
          WAITV(0);
          unsigned bad = 0;
#pragma unroll
          for (int g = m; g < hi; ++g) { unsigned v_; CK_ALL(v_, tw[g >= 4], HMASK(g), hf[g]); bad |= v_; }
          if (bad == 0) break;
          if (--spin == 0) { cap = 1; break; }
        } while (true);
      }
      if constexpr (k == P3_ST) {
        asm volatile("s_waitcnt vmcnt(%4)" : "+v"(str[0][0]), "+v"(str[0][1]), "+v"(str[1][0]), "+v"(str[1][1]) : "i"(p3_stwait()) : "memory");
#define STCK(a) (tag + (a)[0] + (a)[1] - (a)[2])
        if (__builtin_expect(BAD(STCK(str[0][0]) | STCK(str[0][1]) | STCK(str[1][0]) | STCK(str[1][1])), 0)) {
          unsigned spin = cap;
          WAITV(0);
          do {
            asm volatile("global_load_dwordx4 %0, %4, %5 sc1\n\tglobal_load_dwordx4 %1, %4, %5 offset:256 sc1\n\t"
                         "global_load_dwordx4 %2, %4, %5 offset:2048 sc1\n\tglobal_load_dwordx4 %3, %4, %5 offset:2304 sc1\n\ts_waitcnt vmcnt(0)"
                         : "=&v"(str[0][0]), "=&v"(str[0][1]), "=&v"(str[1][0]), "=&v"(str[1][1]) : "v"(vs), "s"(stb) : "memory");
            if (!BAD(STCK(str[0][0]) | STCK(str[0][1]) | STCK(str[1][0]) | STCK(str[1][1]))) break;
            if (--spin == 0) { cap = 1; break; }
          } while (true);
        }
      }
      SCHED();
      static_for<0, 7>([&](auto L_) {
        constexpr int l = L_;
        constexpr bool CHK = (k & 1) && k + 1 < 16;
        acc[l][0] = mfma(wf[k & 1][l], hf[m][2 * (k & 1)], acc[l][0]);
        SCHED();
        if constexpr (k + 1 < 16) wf[(k + 1) & 1][l] = W1F(k + 1, l);
        if constexpr (CHK) {
          if constexpr (l == 0) WAIT4T(hf[mn], tw[mn >= 4], p3_wait(mn));
          if constexpr (l == 1) ACC4(mx, mx, hf[mn][0]);
          if constexpr (l == 2) ACC4(mx, mx, hf[mn][2]);
          if constexpr (l >= 3 && l < 6) CK_RED(mx, 2 * (l - 3));
          if constexpr (l == 6) CK_END(ms, mx);
        }
        if constexpr (k >= P3_ST && !(k & 1)) stat_piece(std::integral_constant<int, 7 * ((k - P3_ST) / 2) + l>{});
        SCHED();
        acc[l][1] = mfma(wf[k & 1][l], hf[m][2 * (k & 1) + 1], acc[l][1]);
        SCHED();
        if constexpr (l == 0 || l == 2 || l == 4 || l == 5) P3OP(l == 5 ? 3 : l / 2);
        if constexpr (CHK) {
          if constexpr (l == 0) CK_INIT(mx, tw[mn >= 4], HMASK(mn));
          if constexpr (l == 1) ACC4(mx, mx, hf[mn][1]);
          if constexpr (l == 2) ACC4(mx, mx, hf[mn][3]);
          if constexpr (l >= 3 && l < 6) CK_RED(mx, 2 * (l - 3) + 1);
        }
        SCHED();
      });
    });

    const int kbp = (7 * fs + (fs & 1)) >> 1, taus = 7 * fs + ((fs & 1) ? 0 : 6);
    const int t64 = 2 * t128 + (w >> 1);
    unsigned char* fb = ws + FF_OFF + t64 * 229376 + kbp * 4096 + 2 * (w & 1) * 1024;
    v4u fck = {0u, 0u, 0u, 0u};
    {
      const v4u n0 = {nmf[0], nmf[0], nmf[0], nmf[0]}, n1 = {nmf[1], nmf[1], nmf[1], nmf[1]};
#pragma unroll
      for (int l = 0; l < 7; ++l) {
        acc[l][0] = mfma(csf[l], n0, acc[l][0]);
        acc[l][1] = mfma(csf[l], n1, acc[l][1]);
      }
    }
    auto act = [&](int l, int t) {
      const f32x4 v = acc[l][t] * rd[t];
      return v2u{relu2(pk2(v[0], v[1])), relu2(pk2(v[2], v[3]))};
    };
#pragma unroll
    for (int m_ = 0; m_ < 3; ++m_)
#pragma unroll
      for (int t = 0; t < 2; ++t) {
        const v2u a = act(2 * m_, t), b = act(2 * m_ + 1, t);
        const v4u oo = {a[0], a[1], b[0], b[1]};
        if (t == 0) ST16C(oo, vl16, fb + m_ * 4096, 0); else ST16C(oo, vl16, fb + m_ * 4096, 1024);
        fck[m_] = sum4(fck[m_], oo);
      }
    const v2u a6 = act(6, 0), b6 = act(6, 1);
    const v4u o6 = {a6[0], a6[1], b6[0], b6[1]};
    ST16C(o6, vl16, ws + FF_OFF + t64 * 229376 + (taus >> 1) * 4096 + (2 * (taus & 1) + (w & 1)) * 1024, 0);
    fck[3] = sum4(fck[3], o6);
#pragma unroll
    for (int i = 0; i < 4; ++i) fck[i] = tag + last_lane(wave_isum(fck[i]));
    ST_TAG(fck, ws + CKF_OFF + ((t64 * 16 + fs) * 2 + (w & 1)) * 128);
  }

#pragma unroll
  for (int n = 56 - W2C; n < 56; ++n) W2L(n);

  {
    f32x4 acc[4][4];
    constexpr int R = D4 + 1;
    v4u ff[R][4];
    unsigned tw[2];
    cptr fb = ws + FF_OFF + ti * 229376 + w * 57344;
    cptr cb = ws + CKF_OFF + (ti * 16 + 4 * w) * 256;
    const unsigned vt = ((lane >> 2) & 7) * 128 + (lane & 3) * 4;
    unsigned vsh[4];
#pragma unroll
    for (int c = 0; c < 4; ++c) vsh[c] = vl16 + (c ^ (w >> 1)) * 1024;
#define VR4(g, c) ((g) % 7 == 3 ? vsh[c] : vrot[c])
#define FMASK(g) (((g) % 7 == 3 ? 0x8888ull : (g) % 7 < 3 ? 0x11ull << ((g) % 7) : 0x1100ull << (((g) % 7) & 3)) << (16 * ((g) / 7)))
    if (GATE & 4) WAITV(W2C);
#pragma unroll
    for (int k = 0; k < D4; ++k) {
#pragma unroll
      for (int c = 0; c < 4; ++c) LD16C(ff[k][c], VR4(k, c), fb + k * 4096 + WRONG, 0);
      if (k == 0) LD4C(tw[0], vt, cb, 0);
    }
#pragma unroll
    for (int i = 0; i < 4; ++i) {
      acc[i][0] = unpk(hres[i >> 1][2 * (i & 1)], hres[i >> 1][2 * (i & 1) + 1]);
#pragma unroll
      for (int c = 1; c < 4; ++c) acc[i][c] = f32x4{0.f, 0.f, 0.f, 0.f};
    }
    WAIT4T(ff[0], tw[0], 4 * D4 - 4);
    unsigned mx, ms;
    CK_ALL(ms, tw[0], FMASK(0), ff[0]);

#define P4OP(i) do { if constexpr (k == 0 && (i) == 0) LD4C(tw[1], vt, cb, 0);                                           \
                     if constexpr (k + D4 < 14) LD16C(ff[(k + D4) % R][i], VR4(k + D4, i), fb + (k + D4) * 4096 + WRONG, 0); } while (0)
    static_for<0, 14>([&](auto K_) {
      constexpr int k = K_, kn = (k + 1) % 14;
      if (__builtin_expect(ms != 0, 0)) {
        constexpr int hi = k + D4 < 14 ? k + D4 : 14;
        unsigned spin = cap;
        WAITV(0);
        do {
          COUNT(3);
          if constexpr (k == 13) asm volatile(".Lfnet_last:");
#pragma unroll
          for (int g = k; g < hi; ++g)
#pragma unroll
            for (int c = 0; c < 4; ++c) RLD16(ff[g % R][c], VR4(g, c), fb + g * 4096, 0);
          if constexpr (k < D4) RLD4(tw[0], vt, cb, 0);
          if constexpr (k > 0) RLD4(tw[1], vt, cb, 0);
          WAITV(0);
          unsigned bad = 0;
#pragma unroll
          for (int g = k; g < hi; ++g) { unsigned v_; CK_ALL(v_, tw[g >= D4], FMASK(g), ff[g % R]); bad |= v_; }
          if (bad == 0) break;
          if (--spin == 0) { cap = 1; break; }
        } while (true);
      }
      if (k % 7 == 3) {
        v4u* F = ff[k % R];
        const bool w0 = w & 1;
        const v4u e0 = {F[0][0], F[0][1], F[2][0], F[2][1]}, o0 = {F[0][2], F[0][3], F[2][2], F[2][3]};
        const v4u e1 = {F[1][0], F[1][1], F[3][0], F[3][1]}, o1 = {F[1][2], F[1][3], F[3][2], F[3][3]};
        F[0] = w0 ? o0 : e0; F[1] = w0 ? e0 : o0; F[2] = w0 ? o1 : e1; F[3] = w0 ? e1 : o1;
      }
      if (k < 8) PIN4A(w2f[k][0], w2f[k][1], w2f[k][2], w2f[k][3]);
      else PIN4(w2f[k][0], w2f[k][1], w2f[k][2], w2f[k][3]);
      if constexpr (k < 13) STEP64(w2f[k], ff[k % R], ff[kn % R], WAIT4T(ff[kn % R], tw[kn >= D4], p4_wait(kn)), tw[kn >= D4], FMASK(kn), P4OP);
    });
    v4u o[2];
    last_step_reduce(smem + RED_OFF, w, lane, w2f[13], ff[13 % R], acc, o, []() {});
    unsigned char* ob_ = out + (size_t)(64 * ti + 16 * w) * 1024 + cg * 128;
    ST16(o[0], vx, ob_, 0);
    ST16(o[1], vx, ob_, 64);
  }
  { unsigned long long t0_, t1_; const unsigned dl_ = 4u * (unsigned)(tb_ - ta_); asm volatile("s_memrealtime %0\n\ts_waitcnt lgkmcnt(0)" : "=s"(t0_) :: "memory"); do { asm volatile("s_memrealtime %0\n\ts_waitcnt lgkmcnt(0)" : "=s"(t1_) :: "memory"); } while ((unsigned)(t1_ - t0_) < dl_); }
}

extern "C" void kernel_launch(void* const* d_in, const int* in_sizes, int n_in, void* d_out, int out_size, void* d_ws,
                              size_t ws_size, hipStream_t stream) {
  (void)hipFuncSetAttribute((const void*)fnet_fused, hipFuncAttributeMaxDynamicSharedMemorySize, LDS_BYTES);
  fnet_fused<<<256, 256, LDS_BYTES, stream>>>((cptr)d_in[0], (cptr)d_in[1], (cptr)d_in[2], (cptr)d_in[3], (cptr)d_in[4],
                                              (cptr)d_in[6], (cptr)d_in[7], (unsigned char*)d_ws, (unsigned char*)d_out);
}
```

```cpp
#include <hip/hip_runtime.h>
#include <hip/hip_bf16.h>
#include <stdint.h>
#include <type_traits>

typedef unsigned v4u __attribute__((ext_vector_type(4)));
typedef unsigned v2u __attribute__((ext_vector_type(2)));
typedef float f32x4 __attribute__((ext_vector_type(4)));
typedef float f32x2 __attribute__((ext_vector_type(2)));
typedef short bf16x8 __attribute__((ext_vector_type(8)));
typedef short v4s __attribute__((ext_vector_type(4)));
typedef __bf16 bf2_t __attribute__((ext_vector_type(2)));
typedef const unsigned char* cptr;
#define LAS __attribute__((address_space(3)))

constexpr int S_ = 2048, D_ = 512, F_ = 1792;
constexpr unsigned EPOCH_OFF = 0;
constexpr unsigned FLAG0_OFF = 32768;
constexpr unsigned XNT_OFF = 131072;
constexpr unsigned WD_OFF  = XNT_OFF + (2u << 20);
constexpr unsigned W1_OFF  = WD_OFF + (1u << 20);
constexpr unsigned W2_OFF  = W1_OFF + 1835008u;
constexpr unsigned CS_OFF  = W2_OFF + 1835008u;
constexpr unsigned PQ_OFF  = CS_OFF + 131072;
constexpr unsigned H_OFF   = PQ_OFF + (4u << 20);
constexpr unsigned FF_OFF  = H_OFF + (2u << 20);
constexpr unsigned ST_OFF  = FF_OFF + 7340032u;
constexpr unsigned CKP_OFF = ST_OFF + 262144;
constexpr unsigned CKH_OFF = CKP_OFF + 131072;
constexpr unsigned CKF_OFF = CKH_OFF + 131072;
constexpr unsigned CKX_OFF = CKF_OFF + 131072;

constexpr int XS_ROW = 1088;
constexpr int XS_OFF = 131072, WST_OFF = XS_OFF + 8 * XS_ROW;
constexpr int XT_OFF = WST_OFF + 4352;
constexpr int RED_OFF = 114688;
constexpr int LDS_BYTES = 163840;
constexpr unsigned SPIN_CAP = 1u << 17;

__device__ __forceinline__ unsigned pk2(float a, float b) {
  f32x2 v = {a, b};
  return __builtin_bit_cast(unsigned, __builtin_convertvector(v, bf2_t));
}
__device__ __forceinline__ float asf(unsigned u) { return __builtin_bit_cast(float, u); }
__device__ __forceinline__ float bflo(unsigned u) { return __builtin_bit_cast(float, u << 16); }
__device__ __forceinline__ float bfhi(unsigned u) { return __builtin_bit_cast(float, u & 0xffff0000u); }
__device__ __forceinline__ float rbf(float x) { return (float)(__bf16)x; }
typedef short v2s __attribute__((ext_vector_type(2)));
__device__ __forceinline__ unsigned relu2(unsigned u) {
  return __builtin_bit_cast(unsigned, __builtin_elementwise_max(__builtin_bit_cast(v2s, u), v2s{0, 0}));
}
__device__ __forceinline__ f32x4 unpk(unsigned a, unsigned b) { return f32x4{bflo(a), bfhi(a), bflo(b), bfhi(b)}; }
__device__ __forceinline__ float ln_rstd(float var) {
  return __builtin_amdgcn_rcpf(rbf(__builtin_amdgcn_sqrtf(rbf(rbf(var) + 1e-5f))));
}
__device__ __forceinline__ f32x4 mfma(v4u a, v4u b, f32x4 c) {
  return __builtin_amdgcn_mfma_f32_16x16x32_bf16(__builtin_bit_cast(bf16x8, a), __builtin_bit_cast(bf16x8, b), c, 0, 0, 0);
}
template <int CTRL> __device__ __forceinline__ float dpp_add(float x) {
  return x + __builtin_bit_cast(float, __builtin_amdgcn_update_dpp(0, __builtin_bit_cast(int, x), CTRL, 0xf, 0xf, true));
}
__device__ __forceinline__ float row16_sum(float x) {
  return dpp_add<0x121>(dpp_add<0x122>(dpp_add<0x124>(dpp_add<0x128>(x))));
}
__device__ __forceinline__ float swap16_sum(float x) {
  unsigned a = __builtin_bit_cast(unsigned, x), b = a;
  asm("" : "+v"(b));
  auto r = __builtin_amdgcn_permlane16_swap(a, b, false, false);
  return asf(r[0]) + asf(r[1]);
}
__device__ __forceinline__ float swap32_sum(float x) {
  unsigned a = __builtin_bit_cast(unsigned, x), b = a;
  asm("" : "+v"(b));
  auto r = __builtin_amdgcn_permlane32_swap(a, b, false, false);
  return asf(r[0]) + asf(r[1]);
}
__device__ __forceinline__ float rows_sum(float x) { return swap32_sum(swap16_sum(x)); }
__device__ __forceinline__ float half_sum(float x) { return swap16_sum(row16_sum(x)); }
#define SCHED() __builtin_amdgcn_sched_barrier(0)
#define WAITV(n) asm volatile("s_waitcnt vmcnt(%0)" ::"i"(n) : "memory")
#define WAITV_BAR(n) asm volatile("s_waitcnt vmcnt(%0) lgkmcnt(0)\n\ts_barrier" ::"i"(n) : "memory")
#define BAR() asm volatile("s_waitcnt lgkmcnt(0)\n\ts_barrier" ::: "memory")
#define ACC4(m, m0, v) do { m = sum4(m0, v); asm volatile("" : "+v"(m)); } while (0)
#define PIN4(a, b, c, d) asm volatile("" : "+v"(a), "+v"(b), "+v"(c), "+v"(d))
#define PIN4A(a, b, c, d) asm volatile("" : "+a"(a), "+a"(b), "+a"(c), "+a"(d))

#define LD16(d, vo, sb, imm) asm volatile("global_load_dwordx4 %0, %1, %2 offset:%3" : "=v"(d) : "v"(vo), "s"(sb), "i"(imm) : "memory")
#define LD16C(d, vo, sb, imm) asm volatile("global_load_dwordx4 %0, %1, %2 offset:%3 sc1" : "=v"(d) : "v"(vo), "s"(sb), "i"(imm) : "memory")
#define LD16CA(d, vo, sb, imm) asm volatile("global_load_dwordx4 %0, %1, %2 offset:%3 sc1" : "=a"(d) : "v"(vo), "s"(sb), "i"(imm) : "memory")
#define LD4C(d, vo, sb, imm) asm volatile("global_load_dword %0, %1, %2 offset:%3 sc1" : "=v"(d) : "v"(vo), "s"(sb), "i"(imm) : "memory")
#define ST16C(v, vo, sb, imm) asm volatile("global_store_dwordx4 %0, %1, %2 offset:%3 sc1\n\ts_nop 1" ::"v"(vo), "v"(v), "s"(sb), "i"(imm) : "memory")
#define ST16(v, vo, sb, imm) asm volatile("global_store_dwordx4 %0, %1, %2 offset:%3\n\ts_nop 1" ::"v"(vo), "v"(v), "s"(sb), "i"(imm) : "memory")
#define ST4C(v, vo, sb, imm) asm volatile("global_store_dword %0, %1, %2 offset:%3 sc1" ::"v"(vo), "v"(v), "s"(sb), "i"(imm) : "memory")
#define RLD16(d, vo, sb, imm) asm volatile("global_load_dwordx4 %0, %1, %2 offset:%3 sc1" ::"v"(d), "v"(vo), "s"(sb), "i"(imm) : "memory")
#define RLD4(d, vo, sb, imm) asm volatile("global_load_dword %0, %1, %2 offset:%3 sc1" ::"v"(d), "v"(vo), "s"(sb), "i"(imm) : "memory")
#define CK_INIT(a, T, MASK) asm volatile("v_sub_u32 %0, %2, %1\n\tv_cndmask_b32 %0, 0, %0, %3" : "=&v"(a) : "v"(T), "s"(tag), "s"((unsigned long long)(MASK)))
#define CK_RED(a, i) do { a = isum<i>(a); asm volatile("" : "+v"(a)); } while (0)
#define CK_END(sv, a) do { sv = last_lane(a); asm volatile("" : "+s"(sv)); } while (0)
#define VADD4(a, f) asm volatile("v_add3_u32 %0, %0, %1, %2\n\tv_add3_u32 %0, %0, %3, %4" : "+v"(a) : "v"((f)[0]), "v"((f)[1]), "v"((f)[2]), "v"((f)[3]))
#define CK_ALL(sv, T, MASK, F) do { unsigned a_; CK_INIT(a_, T, MASK); VADD4(a_, F[0]); VADD4(a_, F[1]); VADD4(a_, F[2]); VADD4(a_, F[3]);   \
                                    asm volatile("s_nop 1" : "+v"(a_)); sv = last_lane(wave_isum(a_)); } while (0)
#define WRONG 0
#define TOUCH(vo, sb) asm volatile("global_load_dword %0, %1, %2" : "+v"(sink) : "v"(vo), "s"(sb) : "memory")
#define ST_TAG(v, p) asm volatile("s_mov_b64 exec, 0xff\n\tglobal_store_dwordx4 %0, %1, %2 sc1\n\ts_mov_b64 exec, -1\n\ts_nop 0" ::"v"(vl16), "v"(v), "s"(p) : "memory")
#define GLDS(vo, sb, lds, imm) asm volatile("s_mov_b32 m0, %2\n\ts_nop 0\n\tglobal_load_lds_dwordx4 %0, %1 offset:%3" ::"v"(vo), "s"(sb), "s"(lds), "i"(imm) : "memory")
#define GLDSC(vo, sb, lds, imm) asm volatile("s_mov_b32 m0, %2\n\ts_nop 0\n\tglobal_load_lds_dwordx4 %0, %1 offset:%3 sc1" ::"v"(vo), "s"(sb), "s"(lds), "i"(imm) : "memory")

__device__ __forceinline__ unsigned sum4(unsigned m, v4u v) { return (m + v[0] + v[1]) + v[2] + v[3]; }
template <int I> __device__ __forceinline__ unsigned isum(unsigned x) {
  constexpr int ctrl = I < 4 ? 0x110 + (1 << I) : 0x142 + (I - 4), rm = I < 4 ? 0xf : I == 4 ? 0xa : 0xc;
  return x + (unsigned)__builtin_amdgcn_update_dpp(0, (int)x, ctrl, rm, 0xf, I < 4);
}
__device__ __forceinline__ unsigned row_isum(unsigned x) { return isum<3>(isum<2>(isum<1>(isum<0>(x)))); }
__device__ __forceinline__ unsigned wave_isum(unsigned x) { return isum<5>(isum<4>(row_isum(x))); }
__device__ __forceinline__ unsigned last_lane(unsigned x) { return (unsigned)__builtin_amdgcn_readlane((int)x, 63); }
__device__ __forceinline__ void poll_flags(cptr base, unsigned vo, unsigned tag) {
  for (unsigned spin = 0; spin < SPIN_CAP; ++spin) {
    unsigned v;
    asm volatile("global_load_dword %0, %1, %2 sc1\n\ts_waitcnt vmcnt(0)" : "=&v"(v) : "v"(vo), "s"(base) : "memory");
    if (__all(v == tag)) break;
  }
}
template <class H>
__device__ __forceinline__ void last_step_reduce(unsigned char* red, int w, int lane, const v4u (&wt)[4], const v4u (&fc)[4],
                                                 f32x4 (&acc)[4][4], v4u (&o)[2], H&& hook) {
#pragma unroll
  for (int c = 1; c <= 4; ++c) {
#pragma unroll
    for (int i = 0; i < 4; ++i) {
      acc[i][c & 3] = mfma(wt[i], fc[c & 3], acc[i][c & 3]);
      SCHED();
      if (c >= 2) *(f32x4*)(red + (((c - 1) ^ w) * 3 + c - 2) * 4096 + lane * 16 + i * 1024) = acc[i][c - 1];
      SCHED();
    }
  }
  hook();
  BAR();
  const unsigned char* s = red + w * 12288 + lane * 16;
  f32x4 pt[3][4];
#pragma unroll
  for (int i = 0; i < 4; ++i)
#pragma unroll
    for (int c = 0; c < 3; ++c) pt[c][i] = *(const f32x4*)(s + c * 4096 + i * 1024);
  SCHED();
#pragma unroll
  for (int i = 0; i < 4; ++i) {
    const f32x4 y = (acc[i][0] + pt[0][i]) + (pt[1][i] + pt[2][i]);
    o[i >> 1][2 * (i & 1)] = pk2(y[0], y[1]);
    o[i >> 1][2 * (i & 1) + 1] = pk2(y[2], y[3]);
  }
}

#define NP0 8
#define FL0_CHK 19
#define W1A 0
#define W1C 8
#define GATE 7
#define W2A 4
#define W2C 12
#define WDS 12
#define XT0 20
#define DB 5
#define XTP 1
#define XTD 3
#define BP 4
#define RAN 1
#define D2 4
#define D4 5
#define FL0_PUB 11
constexpr int FL0_IT = FL0_CHK - 3, NV = 32, NX = 42 - WDS;

template <int I, int N, class F> __device__ __forceinline__ void static_for(F&& f) {
  if constexpr (I < N) { f(std::integral_constant<int, I>{}); static_for<I + 1, N>(f); }
}
constexpr bool p1_xt(int s) { return (s & 7) == 7 - XTD && s + XTD + 1 < NV; }
constexpr bool p1_bar(int s) { return (s + 1) % BP == 0; }
constexpr bool p1_ra(int s) { return s >= BP - 1 && s < 15 + BP && (s + 1) % BP < BP / RAN; }
constexpr int p1_as(int s) { return 16 + ((s + 1) / BP - 1) * BP + ((s + 1) % BP) * RAN; }
constexpr int p1_xbase(int s) { int n = 0; for (int u = XT0; u < s; ++u) n += p1_ra(u) ? 0 : (u + DB < 32 ? 2 : 6); return n > NX ? NX : n; }
constexpr int p1_extra(int s) { return s < XT0 ? 0 : p1_xbase(s + 1) - p1_xbase(s); }
static_assert(p1_xbase(32) == NX && XT0 > FL0_CHK && p1_bar(FL0_CHK) && p1_bar(FL0_PUB) && p1_bar(7) && XTP < DB && DB <= 7 && RAN <= 2, "");
constexpr int p1_wait(int it, bool mid) {
  int posA[48] = {}, posB[40] = {}, posF = -1, n = 0, pos0 = 4 * DB, posT = -1;
  for (int s = 0; s < 48; ++s) posA[s] = -1;
  for (int s = 0; s < DB; ++s) { n += 4; posB[s] = n - 1; }
  if (it < 0) return n - 1 - posB[XTP];
  for (int s = 0; s <= it; ++s) {
    if (s == FL0_IT && s < it) posF = n - 1;
    if (p1_xt(s) && s < it) posT = n - 1;
    if (s == it) break;
    if (p1_ra(s)) for (int u = 0; u < RAN; ++u) { n += 2; posA[p1_as(s) + u] = n - 1; }
    n += p1_extra(s);
    if (s + DB < 32) { n += 4; posB[s + DB] = n - 1; }
  }
  int req = -1;
  if (mid) req = posB[it + 1];
  else {
    if (it + BP < 32) req = posA[it + BP];
    if (it == FL0_CHK && posF > req) req = posF;
    if (it == FL0_PUB && pos0 > req) req = pos0;
    if ((it & 7) == 7 && it + 1 < NV && posT > req) req = posT;
  }
  return n - 1 - req > 63 ? 63 : n - 1 - req;
}
constexpr int p2_w1(int s) { int n = 28 - W1A - 4 * (s - (8 - D2)); return (s + D2 < 8 || s == 7) ? 0 : n > 4 ? 4 : n < 0 ? 0 : n; }
constexpr int pk_wait(int k, int D, int NG) {
  int n = (k < D && k >= 2) ? 1 : 0;
  for (int g = k + 1; g < NG && g < k - 1 + D; ++g) n += 4;
  return n;
}
constexpr int p2_wait(int k) { int n = pk_wait(k, D2, 8); for (int s = 0; s < k - 1; ++s) n += p2_w1(s); return n; }
constexpr int p4_wait(int k) { return pk_wait(k, D4, 14); }
constexpr int W2B = 56 - W2A - W2C;
constexpr int p3_w2(int s) { int n = W2B - 4 * (s - 5); return s < 5 ? 0 : n > 4 ? 4 : n < 0 ? 0 : n; }
constexpr int p3_ops(int s) { return s == 0 ? 5 : s < 5 ? 4 : p3_w2(s); }
constexpr int p3_wait(int m) {
  int n = m < 4 ? 4 * (3 - m) : 0;
  for (int s = (m < 4 ? 0 : m - 3); s < 2 * m - 1; ++s) n += p3_ops(s);
  return n;
}
constexpr int P3_ST = 10;
constexpr int p3_stwait() { int n = 0; for (int s = 5; s < P3_ST; ++s) n += p3_ops(s); return n; }
static_assert(W2B >= 0 && W2B <= 44 && W1A >= 0 && W1A <= 28 && W1A % 4 == 0, "");

__global__ __launch_bounds__(256, 1) void fnet_fused(cptr x, cptr wsre, cptr wsim, cptr wre, cptr wim, cptr w1, cptr w2,
                                                     unsigned char* ws, unsigned char* out) {
  extern __shared__ __attribute__((aligned(16))) unsigned char smem[];
  const int bid = blockIdx.x, p = bid & 7, j = bid >> 3;
  const int ti = 4 * p + (j >> 3), cg = j & 7, t128 = 2 * p + (j >> 4), fs = j & 15;
  const int tid = threadIdx.x, lane = tid & 63, w = __builtin_amdgcn_readfirstlane(tid >> 6), q = lane >> 4, r = lane & 15;
  const unsigned lds0 = (unsigned)(size_t)(LAS unsigned char*)smem;
  const unsigned vl16 = lane * 16;
  unsigned cap = SPIN_CAP;

  const int mat = (j >> 2) & 1, nt = j & 3;
  unsigned epoch, sink = 0;
  v4u xr[2];
  {
    const unsigned vo = (lane >> 5) * 1024 + (lane & 31) * 16, zero = 0;
    cptr xb = x + (size_t)(8 * bid + 2 * w) * 1024;
    LD16(xr[0], vo, xb, 0);
    LD16(xr[1], vo, xb, 512);
    LD4C(epoch, zero, ws + EPOCH_OFF, 0);
  }
  const unsigned va0 = (lane >> 3) * 4096 + (((lane & 7) ^ (lane >> 4)) << 4), va1 = (va0 ^ 64) + 32768 - 1024;
  const int rot = nt & 1;
  cptr abase = (mat ? wsim : wsre) + (size_t)(64 * ti + 16 * w) * 4096;
  cptr abase0 = abase + rot * 2048, abase1 = abase - rot * 2048;
  const unsigned lda = lds0 + w * 2048;
#define A_STAGE0(s) GLDS(va0, ((s) < 16 ? abase0 : abase1) + (s) * 128, lda + ((s) & 15) * 8192, 0)
#define A_STAGE1(s) GLDS(va1, ((s) < 16 ? abase0 : abase1) + (s) * 128, lda + ((s) & 15) * 8192, 1024)
  const int kb1 = bid >> 4, fs1 = bid & 15, kbd = bid & 31, kb2 = 2 * (kbd < 28 ? kbd : 27) + (w & 1);
  {
    cptr pc;
    unsigned len;
    asm volatile("s_getpc_b64 %0\n.Lfnet_pc:\n\ts_mov_b32 %1, .Lfnet_last-.Lfnet_pc" : "=s"(pc), "=s"(len));
    const unsigned vo = min(((4 * j + w) * 4 + (lane & 3)) * 128u, len);
    TOUCH(vo, pc);
  }
  const int wst = WST_OFF + w * 5952;
  v4u wdp;
  auto wload = [&]() {
    const unsigned rs = w < 2 ? D_ * 2 : F_ * 2, vs = (lane >> 3) * rs + (lane & 7) * 16;
    cptr sb = w < 2 ? w2 + (size_t)(32 * kb2) * (D_ * 2) + (bid >> 5) * 128
                    : w1 + (size_t)(32 * kb1) * (F_ * 2) + ((7 * fs1) >> 1) * 64 + (w - 2) * 128;
#pragma unroll
    for (int k = 0; k < 4; ++k) GLDS(vs, sb + k * 8 * rs, lds0 + wst + k * 1088, 0);
    const unsigned vo = (8 * (r >> 2) + (r & 3)) * 1024 + q * 16;
    LD16(wdp, vo, (kbd < 16 ? wre : wim) + (size_t)(64 * (bid >> 5) + 32 * (w >> 1) + 4 * (w & 1)) * 1024 + (kbd & 15) * 64, 0);
  };
  auto wstore = [&]() {
    if (kbd >= 16) wdp ^= 0x80008000u;
    ST16C(wdp, vl16, ws + WD_OFF + bid * 4096 + w * 1024, 0);
    const unsigned char* s = smem + wst + q * 1088 + ((lane >> 2) & 3) * 128 + 16 * (lane & 3);
    unsigned char* db = w < 2 ? ws + W2_OFF + ((bid >> 5) * 56 + kb2) * 4096 : ws + W1_OFF + fs1 * 114688 + kb1 * 7168;
    float csv = 0.f;
#pragma unroll
    for (int i = 0; i < 4; ++i) {
      const int tl = 4 * (w - 2) + i, l = w < 2 ? (kbd < 28 ? i : 7) : (fs1 & 1) ? (tl == 0 ? 7 : tl == 1 ? 6 : tl - 2) : tl;
      const unsigned char* sl = s + (i >> 1) * 64 + (i & 1) * 8;
      const v2u lo = __builtin_bit_cast(v2u, __builtin_amdgcn_ds_read_tr16_b64_v4i16((LAS v4s*)sl));
      const v2u hi = __builtin_bit_cast(v2u, __builtin_amdgcn_ds_read_tr16_b64_v4i16((LAS v4s*)(sl + 512)));
      const v4u o = {lo[0], lo[1], hi[0], hi[1]};
      if (l < 7) ST16C(o, vl16, db + l * 1024, 0);
      const float cs = rows_sum(((bflo(o[0]) + bfhi(o[0])) + (bflo(o[1]) + bfhi(o[1]))) + ((bflo(o[2]) + bfhi(o[2])) + (bflo(o[3]) + bfhi(o[3]))));
      csv = (q == i) ? cs : csv;
    }
    const unsigned vc = r * 16 + q * 4, chl = pk2(csv, csv - rbf(csv));
    if (w >= 2) ST4C(chl, vc, ws + CS_OFF + fs1 * 8192 + (w - 2) * 4096 + kb1 * 256, 0);
  };
#pragma unroll 1
  for (int s = 0; s < NP0; ++s) { A_STAGE0(s); A_STAGE1(s); }
  WAITV(2 * NP0 + 1);
  asm volatile("" : "+v"(xr[0]), "+v"(xr[1]), "+v"(epoch));
  const unsigned tag = __builtin_amdgcn_readfirstlane(epoch) + 1u;
  {
    float v[16];
#pragma unroll
    for (int i = 0; i < 2; ++i)
#pragma unroll
      for (int k = 0; k < 4; ++k) { v[8 * i + 2 * k] = bflo(xr[i][k]); v[8 * i + 2 * k + 1] = bfhi(xr[i][k]); }
    float s1[4] = {0.f, 0.f, 0.f, 0.f};
#pragma unroll
    for (int k = 0; k < 16; ++k) s1[k & 3] += v[k];
    const float mean = half_sum((s1[0] + s1[1]) + (s1[2] + s1[3])) * (1.0f / D_);
    float s2[4] = {0.f, 0.f, 0.f, 0.f};
#pragma unroll
    for (int k = 0; k < 16; ++k) { const float c = v[k] - mean; s2[k & 3] += c * c; }
    const float mb = rbf(mean), rd = ln_rstd(half_sum((s2[0] + s2[1]) + (s2[2] + s2[3])) * (1.0f / D_));
#pragma unroll
    for (int i = 0; i < 2; ++i) {
      v4u o;
#pragma unroll
      for (int k = 0; k < 4; ++k) {
        const unsigned c2 = pk2(v[8 * i + 2 * k] - mb, v[8 * i + 2 * k + 1] - mb);
        o[k] = pk2(bflo(c2) * rd, bfhi(c2) * rd);
      }
      *(v4u*)(smem + XS_OFF + (2 * w + (lane >> 5)) * XS_ROW + (lane & 31) * 16 + i * 512) = o;
    }
  }
  BAR();
  auto tr8 = [&](int off, int i) {
    const unsigned char* s = smem + off + ((lane >> 2) & 3) * XS_ROW + 64 * (q >> 1) + 16 * (lane & 3) + 8 * (q & 1) + 128 * i;
    const v2u lo = __builtin_bit_cast(v2u, __builtin_amdgcn_ds_read_tr16_b64_v4i16((LAS v4s*)s));
    const v2u hi = __builtin_bit_cast(v2u, __builtin_amdgcn_ds_read_tr16_b64_v4i16((LAS v4s*)(s + 4 * XS_ROW)));
    return v4u{lo[0], lo[1], hi[0], hi[1]};
  };
  {
    const v4u o0 = tr8(XS_OFF, 2 * w), o1 = tr8(XS_OFF, 2 * w + 1);
    const unsigned vo = (q >> 1) * 131072 + (q & 1) * 1024 + r * 16;
    unsigned char* sb = ws + XNT_OFF + (bid >> 2) * 2048 + (bid & 3) * 256 + w * 524288;
    ST16C(o0, vo, sb, 0);
    ST16C(o1, vo, sb + 262144, 0);
    auto sw = __builtin_amdgcn_permlane16_swap(sum4(0u, o0), sum4(0u, o1), false, false);
    const int ck = (int)row_isum(sw[0] + sw[1]);
    const v4u c4 = {tag + __builtin_amdgcn_readlane(ck, 15), tag + __builtin_amdgcn_readlane(ck, 47),
                    tag + __builtin_amdgcn_readlane(ck, 31), tag + __builtin_amdgcn_readlane(ck, 63)};
    ST_TAG(c4, ws + CKX_OFF + bid * 512 + w * 128);
  }
  wload();
#pragma unroll 1
  for (int s = NP0; s < 16; ++s) { A_STAGE0(s); A_STAGE1(s); }

  cptr bbase0 = ws + XNT_OFF + (4 * nt + w) * 131072 + rot * 65536, bbase1 = bbase0 - rot * 131072;
  cptr xcb = ws + CKX_OFF + nt * 128;
#define BB(s) ((s) < 16 ? bbase0 : bbase1)
#define XCB(r8) (xcb + (((r8) + 2 * rot) & 3) * 32768)
  const unsigned vl512 = lane * 512;
  v4u bf[8][4];
  unsigned xt[(NV + 7) / 8];
#define B_LOAD(s, i) LD16C(bf[(s) & 7][i], vl16, BB(s) + (s) * 4096 + ((s) < NV ? WRONG : 0), (i) * 1024)
#pragma unroll
  for (int s = 0; s < DB; ++s) {
    B_LOAD(s, 0); B_LOAD(s, 1); B_LOAD(s, 2); B_LOAD(s, 3);
    if (s == XTP && w == 0) GLDSC(vl512, XCB(0), lds0 + XT_OFF, 0);
  }
  asm volatile("s_waitcnt vmcnt(%2)" : "+v"(wdp), "+v"(sink) : "i"(4 * DB) : "memory");
  wstore();
  f32x4 acc1[2][4];
#pragma unroll
  for (int i = 0; i < 2; ++i)
#pragma unroll
    for (int t = 0; t < 4; ++t) acc1[i][t] = f32x4{0.f, 0.f, 0.f, 0.f};
  const unsigned ra0 = r * 128 + ((q ^ (r >> 1)) << 4), ra1 = ra0 ^ 64;
  v4u af[2][8];
  v4u xres[2], csr[8], wd[8][4];
  unsigned flw = tag;
  const v4u tag4 = {tag, tag, tag, tag};
  const unsigned vfl = (lane < 32 ? 32 * cg + lane : 16 * r + fs) * 128;
  cptr xb2 = x + (size_t)(64 * ti + 16 * w) * 1024 + cg * 128;
  const unsigned vx = r * 1024 + q * 16;
  cptr cb2 = ws + CS_OFF + fs * 8192;
  const unsigned vc2 = q * 1024 + r * 16;
  cptr wb = ws + WD_OFF + cg * 131072 + w * 32768;
#define BAD(M) (__builtin_amdgcn_ballot_w64((M) != 0u) != 0)
#define WAIT4(F, N, TAIL) asm volatile("s_waitcnt vmcnt(%4)" TAIL : "+v"(F[0]), "+v"(F[1]), "+v"(F[2]), "+v"(F[3]) : "i"(N) : "memory")
#define XMASK(s) (0xffull << (8 * ((s) & 7)))
  WAIT4(bf[0], p1_wait(-1, false), " lgkmcnt(0)\n\ts_barrier");
#define XT_READ(i) do { xt[i] = *(const unsigned*)(smem + XT_OFF + (i) * 5952 + vl16 + w * 4); asm volatile("" : "+v"(xt[i])); } while (0)
  XT_READ(0);
#pragma unroll
  for (int t = 0; t < 4; ++t) { af[0][t] = *(const v4u*)(smem + ra0 + t * 2048); af[0][4 + t] = *(const v4u*)(smem + ra1 + t * 2048); }
  unsigned bx, bs;
  CK_ALL(bs, xt[0], XMASK(0), bf[0]);
#define P1_EXTRA(e)                                                                                                     \
  if constexpr ((e) < p1_extra(it)) {                                                                                   \
    constexpr int n_ = p1_xbase(it) + (e);                                                                              \
    if constexpr (n_ < 2) LD16(xres[n_], vx, xb2, n_ * 64);                                                             \
    else if constexpr (n_ < 10) LD16C(csr[n_ - 2], vc2, cb2 + ((n_ - 2) >> 2) * 4096, ((n_ - 2) & 3) * 256);            \
    else LD16CA(wd[(n_ - 10) >> 2][(n_ - 10) & 3], vl16, wb + ((n_ - 10) >> 2) * 4096, ((n_ - 10) & 3) * 1024);         \
  }
#define MM1(i, t, kb) acc1[i][t] = mfma(bf[U][2 * (kb) + (i)], af[C][4 * (kb) + (t)], acc1[i][t]); SCHED()
#define RD1(n) if constexpr (RN) { af[Nx][n] = *(const v4u*)(sn + ((n) < 4 ? ra0 : ra1) + ((n) & 3) * 2048); SCHED(); }
#define CK1(what) if constexpr (it + 1 < NV) { what; SCHED(); }
  static_for<0, 32>([&](auto IT) {
    constexpr int it = IT, U = it & 7, Un = (it + 1) & 7, C = it & 1, Nx = C ^ 1;
    constexpr bool RA = p1_ra(it), RB = it + DB < 32, RN = it + 1 < 32;
    if constexpr (it < NV) {
      if (__builtin_expect(bs != 0, 0)) {
        constexpr int hi0 = it + DB < NV ? it + DB : NV, hi = hi0 < (it / 8 + 1) * 8 ? hi0 : (it / 8 + 1) * 8;
        unsigned spin = cap;
        WAITV(0);
        do {
#pragma unroll
          for (int g = it; g < hi0; ++g)
#pragma unroll
            for (int c = 0; c < 4; ++c) RLD16(bf[g & 7][c], vl16, BB(g) + g * 4096, c * 1024);
          RLD4(xt[it / 8], vl512, XCB(it / 8) + w * 4, 0);
          WAITV(0);
          unsigned bad = 0;
#pragma unroll
          for (int g = it; g < hi; ++g) { unsigned v_; CK_ALL(v_, xt[g / 8], XMASK(g), bf[g & 7]); bad |= v_; }
          if (bad == 0) break;
          if (--spin == 0) { cap = 1; break; }
        } while (true);
      }
    }
    if constexpr (it == FL0_CHK) {
      asm volatile("s_waitcnt vmcnt(%1)" : "+v"(flw) : "i"(p1_wait(it, false)) : "memory");
      if (!__all(flw == tag)) poll_flags(ws + FLAG0_OFF, vfl, tag);
    }
    if constexpr (p1_bar(it)) WAITV_BAR(p1_wait(it, false));
    if constexpr ((it & 7) == 7 && it + 1 < NV) XT_READ((it + 1) / 8);
    if constexpr (p1_xt(it)) { if (w == it / 8 + 1) GLDSC(vl512, XCB(it / 8 + 1), lds0 + XT_OFF + (it / 8 + 1) * 5952, 0); }
    if constexpr (it == FL0_PUB) { if (w == 0) ST_TAG(tag4, ws + FLAG0_OFF + bid * 128); }
    if constexpr (it == FL0_IT)
      asm volatile("s_cmp_lg_u32 %3, 0\n\ts_cbranch_scc1 1f\n\tglobal_load_dword %0, %1, %2 sc1\n1:"
                   : "+v"(flw) : "v"(vfl), "s"(ws + FLAG0_OFF), "s"(w) : "memory", "scc");
    const unsigned char* sn = smem + ((it + 1) & 15) * 8192;
    SCHED();
    MM1(0, 0, 0); RD1(0); if constexpr (RN) { WAIT4(bf[Un], p1_wait(it, true), ""); SCHED(); }
    MM1(1, 0, 0); if constexpr (RA) A_STAGE0(p1_as(it)); P1_EXTRA(0) SCHED();
    MM1(0, 1, 0); RD1(1); if constexpr (RA && RAN == 2) { A_STAGE0(p1_as(it) + 1); SCHED(); } CK1(CK_INIT(bx, xt[(it + 1) / 8], XMASK(it + 1)));
    MM1(1, 1, 0); RD1(2); if constexpr (RA && RAN == 2) { A_STAGE1(p1_as(it)); SCHED(); } CK1(ACC4(bx, bx, bf[Un][0]));
    MM1(0, 2, 0); if constexpr (RA) A_STAGE1(p1_as(it) + RAN - 1); P1_EXTRA(1) SCHED();
    MM1(1, 2, 0); RD1(3); CK1(ACC4(bx, bx, bf[Un][1]));
    MM1(0, 3, 0); if constexpr (RB) B_LOAD(it + DB, 0); P1_EXTRA(2) SCHED(); CK1(ACC4(bx, bx, bf[Un][2]));
    MM1(1, 3, 0); RD1(4); CK1(ACC4(bx, bx, bf[Un][3]));
    MM1(0, 0, 1); RD1(5); CK1(CK_RED(bx, 0));
    MM1(1, 0, 1); if constexpr (RB) B_LOAD(it + DB, 1); P1_EXTRA(3) SCHED(); CK1(CK_RED(bx, 1));
    MM1(0, 1, 1); RD1(6); CK1(CK_RED(bx, 2));
    MM1(1, 1, 1); if constexpr (RB) B_LOAD(it + DB, 2); P1_EXTRA(4) SCHED(); CK1(CK_RED(bx, 3));
    MM1(0, 2, 1); RD1(7); CK1(CK_RED(bx, 4));
    MM1(1, 2, 1); if constexpr (RB) B_LOAD(it + DB, 3); P1_EXTRA(5) SCHED(); CK1(CK_RED(bx, 5));
    MM1(0, 3, 1); CK1(CK_END(bs, bx));
    MM1(1, 3, 1);
  });
  {
    const int kbq = ti * 32 + 16 * mat + 4 * nt + w;
    unsigned char* pq = ws + PQ_OFF + kbq * 4096;
    unsigned ck = 0;
#pragma unroll
    for (int t = 0; t < 4; ++t) {
      const v4u o = {pk2(acc1[0][t][0], acc1[0][t][1]), pk2(acc1[0][t][2], acc1[0][t][3]),
                     pk2(acc1[1][t][0], acc1[1][t][1]), pk2(acc1[1][t][2], acc1[1][t][3])};
      ST16C(o, vl16, pq + t * 1024, 0);
      ck = sum4(ck, o);
    }
    ck = tag + last_lane(wave_isum(ck));
    const v4u c4 = {ck, ck, ck, ck};
    ST_TAG(c4, ws + CKP_OFF + kbq * 128);
    if (bid == 0 && w == 0) ST_TAG(tag4, ws + EPOCH_OFF);
  }
#pragma unroll
  for (int n = 32 - WDS; n < 32; ++n) LD16CA(wd[n >> 2][n & 3], vl16, wb + (n >> 2) * 4096, (n & 3) * 1024);
  cptr w1src = ws + W1_OFF + fs * 114688 + w * 28672;
  const unsigned w1dst = lds0 + w * 28672;
#define W1L(n) GLDSC(vl16, w1src + ((n) >> 2) * 4096, w1dst + ((n) >> 2) * 4096, ((n) & 3) * 1024)
#pragma unroll 1
  for (int n = 0; n < W1A / 4; ++n) {
    GLDSC(vl16, w1src + n * 4096, w1dst + n * 4096, 0);
    GLDSC(vl16, w1src + n * 4096, w1dst + n * 4096, 1024);
    GLDSC(vl16, w1src + n * 4096, w1dst + n * 4096, 2048);
    GLDSC(vl16, w1src + n * 4096, w1dst + n * 4096, 3072);
  }
  v4u csf[7];
  {
    static_assert(p1_xbase(26) >= 10, "the wait for the last XNT stage covers the column-sum loads");
    PIN4(csr[0], csr[1], csr[2], csr[3]);
    PIN4(csr[4], csr[5], csr[6], csr[7]);
    const bool odd = fs & 1;
#pragma unroll
    for (int l = 0; l < 7; ++l) {
      const int te = l, to = l < 6 ? l + 2 : 1;
#pragma unroll
      for (int i = 0; i < 4; ++i) csf[l][i] = odd ? csr[(to >> 2) * 4 + i][to & 3] : csr[(te >> 2) * 4 + i][te & 3];
      asm volatile("" : "+a"(csf[l]));
    }
  }
#define MM64(i, c) acc[i][c] = mfma(WT_[i], FC_[c], acc[i][c]); SCHED()
#define STEP64(WT, FC, FN, WAITN, T, MASK, OP)                                                                          \
  {                                                                                                                     \
    auto& WT_ = WT; auto& FC_ = FC;                                                                                     \
    SCHED();                                                                                                            \
    MM64(0, 0); WAITN; SCHED();                                                                                         \
    MM64(0, 1); OP(0); SCHED();                                                                                         \
    MM64(0, 2); CK_INIT(mx, T, MASK); SCHED();                                                                          \
    MM64(0, 3); ACC4(mx, mx, FN[0]); SCHED();                                                                           \
    MM64(1, 0); ACC4(mx, mx, FN[1]); SCHED();                                                                           \
    MM64(1, 1); OP(1); SCHED();                                                                                         \
    MM64(1, 2); ACC4(mx, mx, FN[2]); SCHED();                                                                           \
    MM64(1, 3); ACC4(mx, mx, FN[3]); SCHED();                                                                           \
    MM64(2, 0); CK_RED(mx, 0); SCHED();                                                                                 \
    MM64(2, 1); OP(2); CK_RED(mx, 1); SCHED();                                                                          \
    MM64(2, 2); CK_RED(mx, 2); SCHED();                                                                                 \
    MM64(2, 3); CK_RED(mx, 3); SCHED();                                                                                 \
    MM64(3, 0); CK_RED(mx, 4); SCHED();                                                                                 \
    MM64(3, 1); OP(3); CK_RED(mx, 5); SCHED();                                                                          \
    MM64(3, 2); CK_END(ms, mx); SCHED();                                                                                \
    MM64(3, 3);                                                                                                         \
  }
#define WAIT4T(F, T, N) asm volatile("s_waitcnt vmcnt(%5)" : "+v"(F[0]), "+v"(F[1]), "+v"(F[2]), "+v"(F[3]), "+v"(T) : "i"(N) : "memory")

  unsigned vrot[4];
#pragma unroll
  for (int c = 0; c < 4; ++c) vrot[c] = vl16 + (c ^ w) * 1024;
  v4u hres[2];
  v4u w2f[14][4];
  cptr w2base = ws + W2_OFF + cg * 229376 + w * 57344;
#define W2L(n) do { if ((n) < 32) LD16CA(w2f[(n) >> 2][(n) & 3], vl16, w2base + ((n) >> 2) * 4096, ((n) & 3) * 1024);   \
                    else LD16C(w2f[(n) >> 2][(n) & 3], vl16, w2base + ((n) >> 2) * 4096, ((n) & 3) * 1024); } while (0)
  {
    f32x4 acc[4][4];
    v4u pq[8][4];
    unsigned tw[2];
    cptr pb = ws + PQ_OFF + ti * 131072 + w * 32768;
    cptr cb = ws + CKP_OFF + (ti * 32 + 8 * w) * 128;
    const unsigned vt = (lane & 7) * 128;
    if (GATE & 1) WAITV(WDS + W1A);
#pragma unroll
    for (int k = 0; k < D2; ++k) {
#pragma unroll
      for (int c = 0; c < 4; ++c) LD16C(pq[k][c], vrot[c], pb + k * 4096 + WRONG, 0);
      if (k == 0) LD4C(tw[0], vt, cb, 0);
    }
    asm volatile("s_waitcnt vmcnt(%2)" : "+v"(xres[0]), "+v"(xres[1]) : "i"(4 * D2 + 1) : "memory");
#pragma unroll
    for (int i = 0; i < 4; ++i) {
      acc[i][0] = unpk(xres[i >> 1][2 * (i & 1)], xres[i >> 1][2 * (i & 1) + 1]);
#pragma unroll
      for (int c = 1; c < 4; ++c) acc[i][c] = f32x4{0.f, 0.f, 0.f, 0.f};
    }
    WAIT4T(pq[0], tw[0], 4 * D2 - 4);
    unsigned mx, ms;
    CK_ALL(ms, tw[0], 1ull, pq[0]);
#define P2OP(i) do { if constexpr (k == 0 && (i) == 0) LD4C(tw[1], vt, cb, 0);                                           \
                     if constexpr (k + D2 < 8) LD16C(pq[(k + D2) & 7][i], vrot[i], pb + (k + D2) * 4096 + WRONG, 0);     \
                     else if constexpr ((i) < p2_w1(k)) W1L(W1A + 4 * (k + D2 - 8) + (i)); } while (0)
    static_for<0, 8>([&](auto K_) {
      constexpr int k = K_, kn = (k + 1) & 7;
      if (__builtin_expect(ms != 0, 0)) {
        constexpr int hi = k + D2 < 8 ? k + D2 : 8;
        unsigned spin = cap;
        WAITV(0);
        do {
#pragma unroll
          for (int g = k; g < hi; ++g)
#pragma unroll
            for (int c = 0; c < 4; ++c) RLD16(pq[g][c], vrot[c], pb + g * 4096, 0);
          if constexpr (k < D2) RLD4(tw[0], vt, cb, 0);
          if constexpr (k > 0) RLD4(tw[1], vt, cb, 0);
          WAITV(0);
          unsigned bad = 0;
#pragma unroll
          for (int g = k; g < hi; ++g) { unsigned v_; CK_ALL(v_, tw[g >= D2], 1ull << g, pq[g]); bad |= v_; }
          if (bad == 0) break;
          if (--spin == 0) { cap = 1; break; }
        } while (true);
      }
      PIN4A(wd[k][0], wd[k][1], wd[k][2], wd[k][3]);
      if constexpr (k < 7) STEP64(wd[k], pq[k], pq[kn], WAIT4T(pq[kn], tw[kn >= D2], p2_wait(kn)), tw[kn >= D2], 1ull << kn, P2OP);
    });
    last_step_reduce(smem + RED_OFF, w, lane, wd[7], pq[7], acc, hres, [&]() {
#pragma unroll
      for (int n = W1A + 4 * (D2 - 1); n < 28 - W1C; ++n) W1L(n);
    });
    const int hp = (4 * ti + w) * 8 + cg;
    ST16C(hres[0], vl16, ws + H_OFF + hp * 2048, 0);
    ST16C(hres[1], vl16, ws + H_OFF + hp * 2048, 1024);
    const unsigned hck = tag + last_lane(wave_isum(sum4(sum4(0u, hres[0]), hres[1])));
    const v4u c4 = {hck, hck, hck, hck};
    ST_TAG(c4, ws + CKH_OFF + hp * 128);
    f32x4 hv[4];
#pragma unroll
    for (int i = 0; i < 4; ++i) hv[i] = unpk(hres[i >> 1][2 * (i & 1)], hres[i >> 1][2 * (i & 1) + 1]);
    const f32x4 s4 = (hv[0] + hv[1]) + (hv[2] + hv[3]);
    const float m = rows_sum((s4[0] + s4[1]) + (s4[2] + s4[3])) * (1.0f / 64.0f);
    f32x4 q4 = {0.f, 0.f, 0.f, 0.f};
#pragma unroll
    for (int i = 0; i < 4; ++i) { const f32x4 c = hv[i] - m; q4 += c * c; }
    const float m2 = rows_sum((q4[0] + q4[1]) + (q4[2] + q4[3]));
    const unsigned mu = __builtin_bit_cast(unsigned, m), m2u = __builtin_bit_cast(unsigned, m2);
    const v4u st = {mu, m2u, tag + mu + m2u, 0u};
    asm volatile("s_mov_b64 exec, 0xffff\n\tglobal_store_dwordx4 %0, %1, %2 sc1\n\ts_mov_b64 exec, -1\n\ts_nop 0" ::"v"(vl16), "v"(st), "s"(ws + ST_OFF + hp * 256) : "memory");
  }
#pragma unroll
  for (int n = 28 - W1C; n < 28; ++n) W1L(n);
#pragma unroll
  for (int n = 0; n < W2A; ++n) W2L(n);
  {
    v4u hf[8][4];
    unsigned tw[2];
    v4u str[2][2];
    cptr hb = ws + H_OFF + (8 * t128 + 2 * w) * 16384;
    cptr cb = ws + CKH_OFF + (8 * t128 + 2 * w) * 1024;
    const unsigned vt = r * 128;
    const unsigned vh1 = vl16 + 16384;
    const unsigned vs = q * 512 + r * 16;
    cptr stb = ws + ST_OFF + (8 * t128 + 2 * w) * 2048;
#define HL(m, i) LD16C(hf[m][i], ((i) & 1) ? vh1 : vl16, hb + ((m) >> 1) * 4096 + WRONG, ((m) & 1) * 2048 + ((i) >> 1) * 1024)
#define HMASK(m) (0x101ull << (m))
    if (GATE & 2) WAITV(W1C + W2A + 1);
#pragma unroll
    for (int m = 0; m < 4; ++m) { HL(m, 0); HL(m, 1); HL(m, 2); HL(m, 3); if (m == 0) LD4C(tw[0], vt, cb, 0); }
    f32x4 acc[7][2];
#pragma unroll
    for (int l = 0; l < 7; ++l) { acc[l][0] = f32x4{0.f, 0.f, 0.f, 0.f}; acc[l][1] = f32x4{0.f, 0.f, 0.f, 0.f}; }
    WAIT4T(hf[0], tw[0], 12);
    BAR();
    unsigned whi_ = lds0 + vl16 + 57344;
    asm volatile("" : "+v"(whi_));
    const LAS unsigned char* wlo = (const LAS unsigned char*)smem + vl16;
    const LAS unsigned char* whi = (const LAS unsigned char*)(size_t)whi_;
#define W1F(k, l) (*(const LAS v4u*)(((k) < 8 ? wlo + (k) * 7168 : whi + ((k) - 8) * 7168) + (l) * 1024))
    v4u wf[2][7];
#pragma unroll
    for (int l = 0; l < 7; ++l) wf[0][l] = W1F(0, l);
    unsigned mx, ms;
    CK_ALL(ms, tw[0], HMASK(0), hf[0]);
    float rd[2], sm[2], sq[2];
    unsigned nmf[2];
    auto stat_piece = [&](auto N_) {
      constexpr int n = N_ >> 1, t = N_ & 1;
      if constexpr (n == 0) sm[t] = swap16_sum(asf(str[t][0][0]) + asf(str[t][1][0]));
      if constexpr (n == 1) sm[t] = swap32_sum(sm[t]) * 0.125f;
      if constexpr (n == 2) { const float da = asf(str[t][0][0]) - sm[t], db = asf(str[t][1][0]) - sm[t]; sq[t] = da * da + db * db; }
      if constexpr (n == 3) sq[t] = asf(str[t][0][1]) + asf(str[t][1][1]) + 64.0f * sq[t];
      if constexpr (n == 4) sq[t] = swap16_sum(sq[t]);
      if constexpr (n == 5) sq[t] = swap32_sum(sq[t]) * (1.0f / D_);
      if constexpr (n == 6) nmf[t] = pk2(-sm[t], -sm[t]);
      if constexpr (n == 7) sq[t] = rbf(sq[t]) + 1e-5f;
      if constexpr (n == 8) sq[t] = __builtin_amdgcn_sqrtf(rbf(sq[t]));
      if constexpr (n == 9) rd[t] = __builtin_amdgcn_rcpf(rbf(sq[t]));
      if constexpr (n < 2) asm volatile("" : "+v"(sm[t]));
      else if constexpr (n == 6) asm volatile("" : "+v"(nmf[t]));
      else if constexpr (n == 9) asm volatile("" : "+v"(rd[t]));
      else if constexpr (n < 9) asm volatile("" : "+v"(sq[t]));
    };
#define P3OP(i) do { if constexpr (k == 0 && (i) == 0) LD4C(tw[1], vt, cb, 0);                                           \
                     if constexpr (k < 4) HL((4 + k) & 7, i);                                                           \
                     else if constexpr (k == 4) LD16C(str[(i) >> 1][(i) & 1], vs, stb + WRONG, ((i) >> 1) * 2048 + ((i) & 1) * 256); \
                     else if constexpr ((i) < p3_w2(k)) W2L(W2A + 4 * (k - 5) + (i)); } while (0)
    static_for<0, 16>([&](auto K_) {
      constexpr int k = K_, m = k >> 1, mn = ((k + 1) >> 1) & 7;
      if ((k & 1) == 0 && __builtin_expect(ms != 0, 0)) {
        constexpr int hi = 4 + k < 8 ? 4 + k : 8;
        unsigned spin = cap;
        WAITV(0);
        do {
#pragma unroll
          for (int g = m; g < hi; ++g)
#pragma unroll
            for (int c = 0; c < 4; ++c) RLD16(hf[g][c], (c & 1) ? vh1 : vl16, hb + g * 2048, (c >> 1) * 1024);
          if constexpr (m < 4) RLD4(tw[0], vt, cb, 0);
          if constexpr (m > 0) RLD4(tw[1], vt, cb, 0);
          WAITV(0);
          unsigned bad = 0;
#pragma unroll
          for (int g = m; g < hi; ++g) { unsigned v_; CK_ALL(v_, tw[g >= 4], HMASK(g), hf[g]); bad |= v_; }
          if (bad == 0) break;
          if (--spin == 0) { cap = 1; break; }
        } while (true);
      }
      if constexpr (k == P3_ST) {
        asm volatile("s_waitcnt vmcnt(%4)" : "+v"(str[0][0]), "+v"(str[0][1]), "+v"(str[1][0]), "+v"(str[1][1]) : "i"(p3_stwait()) : "memory");
#define STCK(a) (tag + (a)[0] + (a)[1] - (a)[2])
        if (__builtin_expect(BAD(STCK(str[0][0]) | STCK(str[0][1]) | STCK(str[1][0]) | STCK(str[1][1])), 0)) {
          unsigned spin = cap;
          WAITV(0);
          do {
            asm volatile("global_load_dwordx4 %0, %4, %5 sc1\n\tglobal_load_dwordx4 %1, %4, %5 offset:256 sc1\n\t"
                         "global_load_dwordx4 %2, %4, %5 offset:2048 sc1\n\tglobal_load_dwordx4 %3, %4, %5 offset:2304 sc1\n\ts_waitcnt vmcnt(0)"
                         : "=&v"(str[0][0]), "=&v"(str[0][1]), "=&v"(str[1][0]), "=&v"(str[1][1]) : "v"(vs), "s"(stb) : "memory");
            if (!BAD(STCK(str[0][0]) | STCK(str[0][1]) | STCK(str[1][0]) | STCK(str[1][1]))) break;
            if (--spin == 0) { cap = 1; break; }
          } while (true);
        }
      }
      SCHED();
      static_for<0, 7>([&](auto L_) {
        constexpr int l = L_;
        constexpr bool CHK = (k & 1) && k + 1 < 16;
        acc[l][0] = mfma(wf[k & 1][l], hf[m][2 * (k & 1)], acc[l][0]);
        SCHED();
        if constexpr (k + 1 < 16) wf[(k + 1) & 1][l] = W1F(k + 1, l);
        if constexpr (CHK) {
          if constexpr (l == 0) WAIT4T(hf[mn], tw[mn >= 4], p3_wait(mn));
          if constexpr (l == 1) ACC4(mx, mx, hf[mn][0]);
          if constexpr (l == 2) ACC4(mx, mx, hf[mn][2]);
          if constexpr (l >= 3 && l < 6) CK_RED(mx, 2 * (l - 3));
          if constexpr (l == 6) CK_END(ms, mx);
        }
        if constexpr (k >= P3_ST && !(k & 1)) stat_piece(std::integral_constant<int, 7 * ((k - P3_ST) / 2) + l>{});
        SCHED();
        acc[l][1] = mfma(wf[k & 1][l], hf[m][2 * (k & 1) + 1], acc[l][1]);
        SCHED();
        if constexpr (l == 0 || l == 2 || l == 4 || l == 5) P3OP(l == 5 ? 3 : l / 2);
        if constexpr (CHK) {
          if constexpr (l == 0) CK_INIT(mx, tw[mn >= 4], HMASK(mn));
          if constexpr (l == 1) ACC4(mx, mx, hf[mn][1]);
          if constexpr (l == 2) ACC4(mx, mx, hf[mn][3]);
          if constexpr (l >= 3 && l < 6) CK_RED(mx, 2 * (l - 3) + 1);
        }
        SCHED();
      });
    });
    const int kbp = (7 * fs + (fs & 1)) >> 1, taus = 7 * fs + ((fs & 1) ? 0 : 6);
    const int t64 = 2 * t128 + (w >> 1);
    unsigned char* fb = ws + FF_OFF + t64 * 229376 + kbp * 4096 + 2 * (w & 1) * 1024;
    v4u fck = {0u, 0u, 0u, 0u};
    {
      const v4u n0 = {nmf[0], nmf[0], nmf[0], nmf[0]}, n1 = {nmf[1], nmf[1], nmf[1], nmf[1]};
#pragma unroll
      for (int l = 0; l < 7; ++l) {
        acc[l][0] = mfma(csf[l], n0, acc[l][0]);
        acc[l][1] = mfma(csf[l], n1, acc[l][1]);
      }
    }
    auto act = [&](int l, int t) {
      const f32x4 v = acc[l][t] * rd[t];
      return v2u{relu2(pk2(v[0], v[1])), relu2(pk2(v[2], v[3]))};
    };
#pragma unroll
    for (int m_ = 0; m_ < 3; ++m_)
#pragma unroll
      for (int t = 0; t < 2; ++t) {
        const v2u a = act(2 * m_, t), b = act(2 * m_ + 1, t);
        const v4u oo = {a[0], a[1], b[0], b[1]};
        if (t == 0) ST16C(oo, vl16, fb + m_ * 4096, 0); else ST16C(oo, vl16, fb + m_ * 4096, 1024);
        fck[m_] = sum4(fck[m_], oo);
      }
    const v2u a6 = act(6, 0), b6 = act(6, 1);
    const v4u o6 = {a6[0], a6[1], b6[0], b6[1]};
    ST16C(o6, vl16, ws + FF_OFF + t64 * 229376 + (taus >> 1) * 4096 + (2 * (taus & 1) + (w & 1)) * 1024, 0);
    fck[3] = sum4(fck[3], o6);
#pragma unroll
    for (int i = 0; i < 4; ++i) fck[i] = tag + last_lane(wave_isum(fck[i]));
    ST_TAG(fck, ws + CKF_OFF + ((t64 * 16 + fs) * 2 + (w & 1)) * 128);
  }
#pragma unroll
  for (int n = 56 - W2C; n < 56; ++n) W2L(n);

  {
    f32x4 acc[4][4];
    constexpr int R = D4 + 1;
    v4u ff[R][4];
    unsigned tw[2];
    cptr fb = ws + FF_OFF + ti * 229376 + w * 57344;
    cptr cb = ws + CKF_OFF + (ti * 16 + 4 * w) * 256;
    const unsigned vt = ((lane >> 2) & 7) * 128 + (lane & 3) * 4;
    unsigned vsh[4];
#pragma unroll
    for (int c = 0; c < 4; ++c) vsh[c] = vl16 + (c ^ (w >> 1)) * 1024;
#define VR4(g, c) ((g) % 7 == 3 ? vsh[c] : vrot[c])
#define FMASK(g) (((g) % 7 == 3 ? 0x8888ull : (g) % 7 < 3 ? 0x11ull << ((g) % 7) : 0x1100ull << (((g) % 7) & 3)) << (16 * ((g) / 7)))
    if (GATE & 4) WAITV(W2C);
#pragma unroll
    for (int k = 0; k < D4; ++k) {
#pragma unroll
      for (int c = 0; c < 4; ++c) LD16C(ff[k][c], VR4(k, c), fb + k * 4096 + WRONG, 0);
      if (k == 0) LD4C(tw[0], vt, cb, 0);
    }
#pragma unroll
    for (int i = 0; i < 4; ++i) {
      acc[i][0] = unpk(hres[i >> 1][2 * (i & 1)], hres[i >> 1][2 * (i & 1) + 1]);
#pragma unroll
      for (int c = 1; c < 4; ++c) acc[i][c] = f32x4{0.f, 0.f, 0.f, 0.f};
    }
    WAIT4T(ff[0], tw[0], 4 * D4 - 4);
    unsigned mx, ms;
    CK_ALL(ms, tw[0], FMASK(0), ff[0]);
#define P4OP(i) do { if constexpr (k == 0 && (i) == 0) LD4C(tw[1], vt, cb, 0);                                           \
                     if constexpr (k + D4 < 14) LD16C(ff[(k + D4) % R][i], VR4(k + D4, i), fb + (k + D4) * 4096 + WRONG, 0); } while (0)
    static_for<0, 14>([&](auto K_) {
      constexpr int k = K_, kn = (k + 1) % 14;
      if (__builtin_expect(ms != 0, 0)) {
        constexpr int hi = k + D4 < 14 ? k + D4 : 14;
        unsigned spin = cap;
        WAITV(0);
        do {
          if constexpr (k == 13) asm volatile(".Lfnet_last:");
#pragma unroll
          for (int g = k; g < hi; ++g)
#pragma unroll
            for (int c = 0; c < 4; ++c) RLD16(ff[g % R][c], VR4(g, c), fb + g * 4096, 0);
          if constexpr (k < D4) RLD4(tw[0], vt, cb, 0);
          if constexpr (k > 0) RLD4(tw[1], vt, cb, 0);
          WAITV(0);
          unsigned bad = 0;
#pragma unroll
          for (int g = k; g < hi; ++g) { unsigned v_; CK_ALL(v_, tw[g >= D4], FMASK(g), ff[g % R]); bad |= v_; }
          if (bad == 0) break;
          if (--spin == 0) { cap = 1; break; }
        } while (true);
      }
      if (k % 7 == 3) {
        v4u* F = ff[k % R];
        const bool w0 = w & 1;
        const v4u e0 = {F[0][0], F[0][1], F[2][0], F[2][1]}, o0 = {F[0][2], F[0][3], F[2][2], F[2][3]};
        const v4u e1 = {F[1][0], F[1][1], F[3][0], F[3][1]}, o1 = {F[1][2], F[1][3], F[3][2], F[3][3]};
        F[0] = w0 ? o0 : e0; F[1] = w0 ? e0 : o0; F[2] = w0 ? o1 : e1; F[3] = w0 ? e1 : o1;
      }
      if (k < 8) PIN4A(w2f[k][0], w2f[k][1], w2f[k][2], w2f[k][3]);
      else PIN4(w2f[k][0], w2f[k][1], w2f[k][2], w2f[k][3]);
      if constexpr (k < 13) STEP64(w2f[k], ff[k % R], ff[kn % R], WAIT4T(ff[kn % R], tw[kn >= D4], p4_wait(kn)), tw[kn >= D4], FMASK(kn), P4OP);
    });
    v4u o[2];
    last_step_reduce(smem + RED_OFF, w, lane, w2f[13], ff[13 % R], acc, o, []() {});
    unsigned char* ob_ = out + (size_t)(64 * ti + 16 * w) * 1024 + cg * 128;
    ST16(o[0], vx, ob_, 0);
    ST16(o[1], vx, ob_, 64);
  }
}

extern "C" void kernel_launch(void* const* d_in, const int* in_sizes, int n_in, void* d_out, int out_size, void* d_ws,
                              size_t ws_size, hipStream_t stream) {
  (void)hipFuncSetAttribute((const void*)fnet_fused, hipFuncAttributeMaxDynamicSharedMemorySize, LDS_BYTES);
  fnet_fused<<<256, 256, LDS_BYTES, stream>>>((cptr)d_in[0], (cptr)d_in[1], (cptr)d_in[2], (cptr)d_in[3], (cptr)d_in[4],
                                              (cptr)d_in[6], (cptr)d_in[7], (unsigned char*)d_ws, (unsigned char*)d_out);
}
```
